```python
import torch

def _softmax(x, axis=-1):
    return torch.softmax(x, dim=axis)
def _gelu(x):
    return torch.nn.functional.gelu(x, approximate="tanh")
def _silu(x):
    return torch.nn.functional.silu(x)
def _sigmoid(x):
    return torch.sigmoid(x)
def _layernorm(x, axis=-1, eps=1e-5):
    m = x.mean(dim=axis, keepdim=True)
    v = x.var(dim=axis, keepdim=True, unbiased=False)
    return (x - m) / torch.sqrt(v + eps)
def _scatter_add(tgt, idx, src, axis=0):
    return tgt.clone().index_add_(axis, idx, src)
def _segment_sum(src, seg, n):
    out = torch.zeros((n,) + src.shape[1:], dtype=src.dtype)
    return out.index_add_(0, seg, src)
def _pad(x, pw):
    flat = tuple(v for pair in reversed(pw) for v in pair)
    return torch.nn.functional.pad(x, flat)

kernel_name = 'parallel_sum__sliding_local_dilated__swiglu_ffn__prompt'

def setup_inputs():

    _w = (lambda a: (lambda t: t.to(torch.bfloat16) if t.is_floating_point() else t)(torch.as_tensor(a)))
    def _A_setup():
        B, S, D = 1, 2048, 576
        return {
            "x": _w((lambda *s: torch.randn(*s, dtype=torch.bfloat16) * 0.05)(B, S, D)),
            "wqkv": _w((lambda *s: torch.randn(*s, dtype=torch.bfloat16) * 0.1)(D, 3 * D)),
            "wo": _w((lambda *s: torch.randn(*s, dtype=torch.bfloat16) * 0.05)(D, D)),
        }
    _A_in = _A_setup()

    def _B_setup():
        B, S, D, F = 1, 2048, 576, 4608
        return {
            "x": _w((lambda *s: torch.randn(*s, dtype=torch.bfloat16) * 0.05)(B, S, D)),
            "w_gate": _w((lambda *s: torch.randn(*s, dtype=torch.bfloat16) * 0.05)(D, F)),
            "w_up": _w((lambda *s: torch.randn(*s, dtype=torch.bfloat16) * 0.05)(D, F)),
            "w_down": _w((lambda *s: torch.randn(*s, dtype=torch.bfloat16) * 0.05)(F, D)),
        }
    _B_in = _B_setup()

    return {
        "A_x": _A_in["x"],
        "A_wqkv": _A_in["wqkv"],
        "A_wo": _A_in["wo"],
        "B_x": _B_in["x"],
        "B_w_gate": _B_in["w_gate"],
        "B_w_up": _B_in["w_up"],
        "B_w_down": _B_in["w_down"],
    }

def reference(A_x, A_wqkv, A_wo, B_x, B_w_gate, B_w_up, B_w_down):
    def _A_ref(x, wqkv, wo):
        B, S, D = x.shape
        H = 32; Dh = D // H
        W, dil = 8, 2
        qkv = (lambda x, *d: x.permute(*d))((x @ wqkv).reshape(B, S, 3, H, Dh), 2, 0, 3, 1, 4)
        q, k, v = qkv[0], qkv[1], qkv[2]
        scale = Dh ** -0.5
        pos = torch.arange(S)
        off = torch.arange(W) * dil
        idx = pos[:, None] - off[None, :]
        mask = idx >= 0
        idx_c = torch.where(mask, idx, 0).reshape(-1)
        kg = k[:, :, idx_c, :].reshape(B, H, S, W, Dh)
        vg = v[:, :, idx_c, :].reshape(B, H, S, W, Dh)
        logits = torch.einsum("bhsd,bhswd->bhsw", q, kg) * scale
        logits = torch.where(mask[None, None, :, :], logits, -1e9)
        attn = _softmax(logits, axis=-1)
        o = torch.einsum("bhsw,bhswd->bhsd", attn, vg)
        return (lambda x, *d: x.permute(*d))(o, 0, 2, 1, 3).reshape(B, S, D) @ wo

    def _B_ref(x, w_gate, w_up, w_down):
        g = _silu(x @ w_gate)
        u = x @ w_up
        return (g * u) @ w_down

    x = A_x
    xn = _layernorm(x, axis=-1)
    return x + _A_ref(xn, A_wqkv, A_wo) + _B_ref(xn, B_w_gate, B_w_up, B_w_down)
```

```cpp
#include <hip/hip_runtime.h>
#include <hip/hip_bf16.h>
namespace naive {
typedef unsigned short u16;
__device__ __forceinline__ float b2f(u16 v){ return __uint_as_float(((unsigned)v)<<16); }
__device__ __forceinline__ u16 f2b(float f){ __hip_bfloat16 h = __float2bfloat16(f); return *(u16*)&h; }
__device__ __forceinline__ float rb(float f){ return b2f(f2b(f)); }
constexpr int S=2048,D=576,F=4608,H=32,DH=18;
__global__ void ln_k(const u16* x,u16* xn){
  int r=blockIdx.x, l=threadIdx.x; float s=0;
  for(int i=l;i<D;i+=64) s+=b2f(x[r*D+i]);
  for(int o=32;o;o>>=1) s+=__shfl_xor(s,o);
  float m=s/D; float v=0;
  for(int i=l;i<D;i+=64){float d=b2f(x[r*D+i])-m; v+=d*d;}
  for(int o=32;o;o>>=1) v+=__shfl_xor(v,o);
  v/=D; float mb=rb(m), vb=rb(v); float sd=rb(sqrtf(rb(vb+1e-5f)));
  for(int i=l;i<D;i+=64) xn[r*D+i]=f2b(rb(b2f(x[r*D+i])-mb)/sd);
}
__global__ void gemm_k(const u16* A,const u16* B,u16* C,int M,int N,int K){
  __shared__ float As[16][17], Bs[16][17];
  int tx=threadIdx.x&15, ty=threadIdx.x>>4; int r=blockIdx.y*16+ty, c=blockIdx.x*16+tx; float acc=0;
  for(int k0=0;k0<K;k0+=16){
    As[ty][tx]=b2f(A[(long)r*K+k0+tx]); Bs[ty][tx]=b2f(B[(long)(k0+ty)*N+c]); __syncthreads();
    for(int k=0;k<16;++k) acc+=As[ty][k]*Bs[k][tx]; __syncthreads();
  }
  C[(long)r*N+c]=f2b(acc);
}
__global__ void attn_k(const u16* qkv,u16* o){
  int id=blockIdx.x*blockDim.x+threadIdx.x; int s=id/H,h=id%H; if(s>=S) return;
  float scale=rb(0.23570226039551584f); scale=0.23570226039551584f;
  float lg[8]; float mx=-3e38f;
  for(int w=0;w<8;++w){ int p=s-2*w; float a=0; if(p>=0){ for(int d=0;d<DH;++d) a+=b2f(qkv[(long)s*3*D+h*DH+d])*b2f(qkv[(long)p*3*D+D+h*DH+d]); a=rb(rb(a)*scale);} else a=-1e9f; lg[w]=a; mx=fmaxf(mx,a);}
  float sum=0; for(int w=0;w<8;++w){lg[w]=expf(lg[w]-mx); sum+=lg[w];}
  for(int w=0;w<8;++w) lg[w]=rb(lg[w]/sum);
  for(int d=0;d<DH;++d){ float a=0; for(int w=0;w<8;++w){int p=s-2*w; if(p<0)p=0; a+=lg[w]*b2f(qkv[(long)p*3*D+2*D+h*DH+d]);} o[(long)s*D+h*DH+d]=f2b(a);}
}
__global__ void glu_k(const u16* g,const u16* u,u16* h,int n){
  int i=blockIdx.x*blockDim.x+threadIdx.x; if(i>=n) return; float gv=b2f(g[i]); float sg=rb(gv/(1.f+expf(-gv))); h[i]=f2b(sg*b2f(u[i]));
}
__global__ void fin_k(const u16* x,const u16* a,const u16* b,u16* out,int n){
  int i=blockIdx.x*blockDim.x+threadIdx.x; if(i>=n) return; out[i]=f2b(rb(b2f(x[i])+b2f(a[i]))+b2f(b[i]));
}
struct Bufs{u16 *xn,*qkv,*o,*g,*u,*h,*a,*b;};
inline Bufs carve(void* ws){ u16* p=(u16*)ws; Bufs B; B.xn=p; p+=S*D; B.qkv=p; p+=S*3*D; B.o=p; p+=S*D; B.g=p; p+=S*F; B.u=p; p+=S*F; B.h=p; p+=S*F; B.a=p; p+=S*D; B.b=p; return B;}
inline void run(void* const* d_in, void* d_out, void* ws, hipStream_t st){
  const u16* x=(const u16*)d_in[0],*wqkv=(const u16*)d_in[1],*wo=(const u16*)d_in[2],*wg=(const u16*)d_in[4],*wu=(const u16*)d_in[5],*wd=(const u16*)d_in[6];
  Bufs B=carve(ws);
  ln_k<<<S,64,0,st>>>(x,B.xn);
  gemm_k<<<dim3(3*D/16,S/16),256,0,st>>>(B.xn,wqkv,B.qkv,S,3*D,D);
  attn_k<<<S*H/256,256,0,st>>>(B.qkv,B.o);
  gemm_k<<<dim3(D/16,S/16),256,0,st>>>(B.o,wo,B.a,S,D,D);
  gemm_k<<<dim3(F/16,S/16),256,0,st>>>(B.xn,wg,B.g,S,F,D);
  gemm_k<<<dim3(F/16,S/16),256,0,st>>>(B.xn,wu,B.u,S,F,D);
  glu_k<<<S*F/256,256,0,st>>>(B.g,B.u,B.h,S*F);
  gemm_k<<<dim3(D/16,S/16),256,0,st>>>(B.h,wd,B.b,S,D,F);
  fin_k<<<S*D/256,256,0,st>>>(x,B.a,B.b,(u16*)d_out,S*D);
}
}
extern "C" void kernel_launch(void* const* d_in, const int* in_sizes, int n_in, void* d_out, int out_size, void* d_ws, size_t ws_size, hipStream_t stream){
  naive::run(d_in,d_out,d_ws,stream);
}
```

```cpp
#include <hip/hip_runtime.h>
#include <utility>
#pragma clang diagnostic ignored "-Wint-to-pointer-cast"

typedef float    f32x16 __attribute__((ext_vector_type(16)));
typedef float    f32x4  __attribute__((ext_vector_type(4)));
typedef float    f32x2  __attribute__((ext_vector_type(2)));
typedef __bf16   bf16x8 __attribute__((ext_vector_type(8)));
typedef __bf16   bf16x4 __attribute__((ext_vector_type(4)));
typedef __bf16   bf16x2 __attribute__((ext_vector_type(2)));
typedef unsigned u32x4  __attribute__((ext_vector_type(4)));
typedef unsigned u32x2  __attribute__((ext_vector_type(2)));
typedef __amdgpu_buffer_rsrc_t rsrc_t;
#define LDS __attribute__((address_space(3)))
#define GAS __attribute__((address_space(1)))
#define DI __device__ __forceinline__
#define GAP() __builtin_amdgcn_sched_barrier(0)

namespace psk {

constexpr int S = 2048, D = 576, F = 4608;
constexpr int KSTOT = 162;
constexpr unsigned HBUF_B = 128u * KSTOT * 1024u;
constexpr unsigned SLAB_B = 64u * 4u * 4u * 18u * 1024u;
constexpr size_t WS_H = 0, WS_SLAB = HBUF_B, WS_CNT = WS_SLAB + SLAB_B, WS_DBG = WS_CNT + 64 * 256;
#define H_AUX 0

template <class Fn, int... Is> DI void sfor_impl(Fn&& f, std::integer_sequence<int, Is...>) { (f(std::integral_constant<int, Is>{}), ...); }
template <int N, class Fn> DI void sfor(Fn&& f) { sfor_impl(f, std::make_integer_sequence<int, N>{}); }

DI unsigned rfl(unsigned x) { return __builtin_amdgcn_readfirstlane(x); }
DI rsrc_t mk_rsrc(const void* p, unsigned bytes) { return __builtin_amdgcn_make_buffer_rsrc((void*)p, (short)0, (int)bytes, 0x00020000); }
DI unsigned cvtpk(float lo, float hi) { f32x2 v = {lo, hi}; return __builtin_bit_cast(unsigned, __builtin_convertvector(v, bf16x2)); }
DI float rbf(float x) { return __builtin_bit_cast(float, cvtpk(x, x) << 16); }
DI float dot2(unsigned a, unsigned b, float c) { return __builtin_amdgcn_fdot2_f32_bf16(__builtin_bit_cast(bf16x2, a), __builtin_bit_cast(bf16x2, b), c, false); }
DI float bflo(unsigned v) { return __builtin_bit_cast(float, v << 16); }
DI float bfhi(unsigned v) { return __builtin_bit_cast(float, v & 0xffff0000u); }
DI bf16x8 trread2(unsigned addr, int off0, int off1) {
  bf16x4 a = __builtin_amdgcn_ds_read_tr16_b64_v4bf16((LDS bf16x4*)(addr + off0));
  bf16x4 b = __builtin_amdgcn_ds_read_tr16_b64_v4bf16((LDS bf16x4*)(addr + off1));
  return __builtin_shufflevector(a, b, 0, 1, 2, 3, 4, 5, 6, 7);
}
template <class T> DI void pin(T& x) { asm volatile("" : "+v"(x)); }
DI unsigned opaque(unsigned x) { asm volatile("" : "+v"(x)); return x; }
template <int N> DI void wait_barrier() { asm volatile("s_waitcnt vmcnt(%0)\n\ts_barrier" ::"i"(N) : "memory"); }
DI void lds_barrier() { asm volatile("s_waitcnt lgkmcnt(0)\n\ts_barrier" ::: "memory"); }

#define STAMP(i) do {} while (0)

template <int LOFF, unsigned GOFF> DI void dma16(unsigned m0base, unsigned voff, rsrc_t srd) {
  unsigned t;
  asm volatile("s_add_u32 m0, %1, %2\n\ts_mov_b32 %0, %3\n\tbuffer_load_dwordx4 %4, %5, %0 offen lds"
               : "=&s"(t) : "s"(m0base), "i"(LOFF), "i"(GOFF), "v"(voff), "s"(srd) : "scc", "memory");
}

template <int OFF> DI void bload16(u32x4& d, unsigned voff, rsrc_t srd) {
  asm volatile("buffer_load_dwordx4 %0, %1, %2, 0 offen offset:%3" : "=v"(d) : "v"(voff), "s"(srd), "i"(OFF));
}
template <int N> DI void wait4(u32x4& a, u32x4& b, u32x4& c, u32x4& d) { asm volatile("s_waitcnt vmcnt(%4)" : "+v"(a), "+v"(b), "+v"(c), "+v"(d) : "i"(N)); }
template <int LOFF, int GOFF> DI void dma48x3(unsigned m0base, unsigned v0, unsigned v1, unsigned v2, rsrc_t srd) {
  unsigned t;
  asm volatile("s_mov_b32 exec_hi, 0xffff\n\t"
               "s_add_u32 m0, %1, %2\n\ts_mov_b32 %0, %3\n\tbuffer_load_dwordx4 %4, %7, %0 offen lds\n\t"
               "s_add_u32 m0, m0, 768\n\ts_nop 0\n\tbuffer_load_dwordx4 %5, %7, %0 offen lds\n\t"
               "s_add_u32 m0, m0, 768\n\ts_nop 0\n\tbuffer_load_dwordx4 %6, %7, %0 offen lds\n\t"
               "s_mov_b32 exec_hi, -1"
               : "=&s"(t) : "s"(m0base), "i"(LOFF), "i"(GOFF), "v"(v0), "v"(v1), "v"(v2), "s"(srd) : "scc", "memory");
}
template <int GOFF> DI void bload16x2(u32x4& d0, u32x4& d1, unsigned v0, unsigned v1, unsigned sbase, rsrc_t srd) {
  unsigned t;
  asm volatile("s_add_u32 %2, %5, %6\n\tbuffer_load_dwordx4 %0, %3, %7, %2 offen\n\tbuffer_load_dwordx4 %1, %4, %7, %2 offen"
               : "=&v"(d0), "=&v"(d1), "=&s"(t) : "v"(v0), "v"(v1), "s"(sbase), "i"(GOFF), "s"(srd) : "scc");
}
#define CV(X) (decltype(X)::value)
template <int N> using ic = std::integral_constant<int, N>;

DI void qt_stage1(unsigned (&m)[8], const unsigned (&a)[8]) {
  asm volatile("s_mov_b32 vcc_lo, 0x55555555\n\ts_mov_b32 vcc_hi, 0x55555555\n\t"
               "v_cndmask_b32_dpp %0, %9, %8, vcc quad_perm:[1,0,3,2] row_mask:0xf bank_mask:0xf\n\t"
               "v_cndmask_b32_dpp %2, %11, %10, vcc quad_perm:[1,0,3,2] row_mask:0xf bank_mask:0xf\n\t"
               "v_cndmask_b32_dpp %4, %13, %12, vcc quad_perm:[1,0,3,2] row_mask:0xf bank_mask:0xf\n\t"
               "v_cndmask_b32_dpp %6, %15, %14, vcc quad_perm:[1,0,3,2] row_mask:0xf bank_mask:0xf\n\t"
               "s_not_b64 vcc, vcc\n\t"
               "v_cndmask_b32_dpp %1, %8, %9, vcc quad_perm:[1,0,3,2] row_mask:0xf bank_mask:0xf\n\t"
               "v_cndmask_b32_dpp %3, %10, %11, vcc quad_perm:[1,0,3,2] row_mask:0xf bank_mask:0xf\n\t"
               "v_cndmask_b32_dpp %5, %12, %13, vcc quad_perm:[1,0,3,2] row_mask:0xf bank_mask:0xf\n\t"
               "v_cndmask_b32_dpp %7, %14, %15, vcc quad_perm:[1,0,3,2] row_mask:0xf bank_mask:0xf"
               : "=&v"(m[0]), "=&v"(m[1]), "=&v"(m[2]), "=&v"(m[3]), "=&v"(m[4]), "=&v"(m[5]), "=&v"(m[6]), "=&v"(m[7])
               : "v"(a[0]), "v"(a[1]), "v"(a[2]), "v"(a[3]), "v"(a[4]), "v"(a[5]), "v"(a[6]), "v"(a[7]) : "vcc", "scc");
}
DI void qt_stage2(unsigned (&r)[8], const unsigned (&m)[8]) {
  asm volatile("s_mov_b32 vcc_lo, 0x33333333\n\ts_mov_b32 vcc_hi, 0x33333333\n\t"
               "v_cndmask_b32_dpp %0, %10, %8, vcc quad_perm:[2,3,0,1] row_mask:0xf bank_mask:0xf\n\t"
               "v_cndmask_b32_dpp %1, %11, %9, vcc quad_perm:[2,3,0,1] row_mask:0xf bank_mask:0xf\n\t"
               "v_cndmask_b32_dpp %4, %14, %12, vcc quad_perm:[2,3,0,1] row_mask:0xf bank_mask:0xf\n\t"
               "v_cndmask_b32_dpp %5, %15, %13, vcc quad_perm:[2,3,0,1] row_mask:0xf bank_mask:0xf\n\t"
               "s_not_b64 vcc, vcc\n\t"
               "v_cndmask_b32_dpp %2, %8, %10, vcc quad_perm:[2,3,0,1] row_mask:0xf bank_mask:0xf\n\t"
               "v_cndmask_b32_dpp %3, %9, %11, vcc quad_perm:[2,3,0,1] row_mask:0xf bank_mask:0xf\n\t"
               "v_cndmask_b32_dpp %6, %12, %14, vcc quad_perm:[2,3,0,1] row_mask:0xf bank_mask:0xf\n\t"
               "v_cndmask_b32_dpp %7, %13, %15, vcc quad_perm:[2,3,0,1] row_mask:0xf bank_mask:0xf"
               : "=&v"(r[0]), "=&v"(r[1]), "=&v"(r[2]), "=&v"(r[3]), "=&v"(r[4]), "=&v"(r[5]), "=&v"(r[6]), "=&v"(r[7])
               : "v"(m[0]), "v"(m[1]), "v"(m[2]), "v"(m[3]), "v"(m[4]), "v"(m[5]), "v"(m[6]), "v"(m[7]) : "vcc", "scc");
}
DI float xhalf_sum(float v) {
  unsigned u = __builtin_bit_cast(unsigned, v);
  auto r = __builtin_amdgcn_permlane32_swap(u, u, false, false);
  return __builtin_bit_cast(float, (unsigned)r[0]) + __builtin_bit_cast(float, (unsigned)r[1]);
}

constexpr int QROW = 196;
constexpr int LDS_V = 0, LDS_Q = 256 * QROW, LDS_K = 2 * 256 * QROW;
constexpr int K1_LDS = 3 * 256 * QROW;

template <bool QKV>
DI void k1_body(const char* __restrict__ x, const char* __restrict__ wA, const char* __restrict__ wB, char* __restrict__ ws,
                unsigned smem, int tok0, int col0, int head0, int nheads, int wave, int lane) {
  constexpr int NPG = QKV ? 3 : 4, NSLOT = QKV ? 4 : 6, AHEAD = NSLOT - 1;
  constexpr unsigned RS = QKV ? 3u * D * 2u : F * 2u;
  constexpr int UOFF = NSLOT * 96 * 128 + 32;
  constexpr int IPS = 6 * NPG, NIT = 18 * IPS, DA = 4;
  unsigned long long* dbg = (unsigned long long*)(ws + WS_DBG); (void)dbg;
  STAMP(0);
  u32x4 xr[9][4];
  {
    const rsrc_t sx = mk_rsrc(x, S * D * 2);
    const int tb = tok0 + 32 * wave + 4 * ((lane >> 2) & 7);
    unsigned v0 = tb < 0 ? 0x80000000u : (unsigned)(tb * 1152 + 64 * (lane >> 5) + 16 * (lane & 3));
    unsigned v1 = v0 + 2304;
    sfor<9>([&](auto B) {
      sfor<4>([&](auto J) {
        bload16<(CV(J) & 1) * 1152 + 128 * CV(B)>(xr[B][J], J < 2 ? v0 : v1, sx);
      });
    });
  }
  const int isU = wave >> 2, w3 = wave & 3;
  const rsrc_t sw = mk_rsrc(isU ? wB : wA, QKV ? (unsigned)(D * 3 * D * 2) : (unsigned)(D * F * 2));
  const int cg = ((lane & 7) >> 1) ^ (((lane >> 4) & 1) << 1);
  const unsigned wv = (unsigned)((8 * (4 * (w3 & 1) + (w3 >> 1)) + (lane >> 3)) * RS + (col0 + (QKV ? 48 * isU : 0)) * 2 +
                                 (QKV && cg == 3 ? 2 : cg) * 32 + (lane & 1) * 16);
  const unsigned m0b = smem + isU * UOFF + w3 * 1024;
  auto dma = [&](auto ST, auto I) {
    constexpr int st = ST, i = I, su = st / 6, c = st % 6, b4 = 3 * c + i;
    constexpr int phys = 8 * (b4 >> 1) + 2 * (b4 & 1);
    constexpr unsigned sucol = QKV ? (su == 0 ? 1152u : su == 1 ? 2304u : 0u) : su * 128u;
    dma16<(st % NSLOT) * 12288 + i * 4096, phys * 8 * RS + sucol>(m0b, wv, sw);
  };
  sfor<AHEAD>([&](auto ST) { sfor<3>([&](auto I) { dma(ST, I); }); });
  STAMP(1);
  float rs, rs2;
  {
    float s4[4] = {0.f, 0.f, 0.f, 0.f};
    sfor<9>([&](auto B) {
      wait4<3 * AHEAD + 4 * (8 - CV(B))>(xr[B][0], xr[B][1], xr[B][2], xr[B][3]);
      sfor<2>([&](auto P) {
        unsigned a[8], m[8], r[8];
        sfor<4>([&](auto J) { a[J] = xr[B][J][2 * P]; a[4 + J] = xr[B][J][2 * P + 1]; });
        qt_stage1(m, a); qt_stage2(r, m);
        sfor<4>([&](auto J) { xr[B][J][2 * P] = r[J]; xr[B][J][2 * P + 1] = r[4 + J]; s4[J] = dot2(r[J], 0x3f803f80u, s4[J]); s4[J] = dot2(r[4 + J], 0x3f803f80u, s4[J]); });
      });
      GAP();
    });
    STAMP(2);
    const float mean = xhalf_sum((s4[0] + s4[1]) + (s4[2] + s4[3])) * (1.0f / D);
    const float mb = rbf(mean);
    GAP();
    float q4[4] = {0.f, 0.f, 0.f, 0.f};
    sfor<9>([&](auto B) {
      sfor<4>([&](auto J) {
        sfor<4>([&](auto E) {
          unsigned v = xr[B][J][(int)E]; pin(v);
          const unsigned c = cvtpk(bflo(v) - mb, bfhi(v) - mb);
          xr[B][J][(int)E] = c; q4[E] = dot2(c, c, q4[E]);
        });
        pin(xr[B][J]);
        if constexpr (CV(J) & 1) { pin(q4[0]); pin(q4[1]); pin(q4[2]); pin(q4[3]); GAP(); }
      });
    });
    float var = xhalf_sum((q4[0] + q4[1]) + (q4[2] + q4[3])) * (1.0f / D) - (mean - mb) * (mean - mb);
    var = fmaxf(var, 0.f);
    rs = 1.0f / rbf(sqrtf(rbf(rbf(var) + 1e-5f)));
    rs2 = rs * rs;
  }
  const float cexp = -1.4426950408889634f * rs;
  unsigned ab[2][2];
  {
    const int g = lane >> 4, qq = (lane >> 2) & 3, p = lane & 3;
    const unsigned b = smem + (g & 1) * UOFF + (8 * (g >> 1) + qq) * 128 + 8 * (((p & 1) << 1) | (p >> 1));
    ab[0][0] = opaque(b + 64 * (qq >> 1)); ab[0][1] = opaque(b + 64 * (1 - (qq >> 1)));
    ab[1][0] = opaque(ab[0][0] + 3 * 12288); ab[1][1] = opaque(ab[0][1] + 3 * 12288);
  }
  auto aread = [&](auto N) -> bf16x8 {
    constexpr int n = N, st = n / IPS, pg = (n % IPS) / 6, kk = n % 6, slot = st % NSLOT;
    constexpr int off = (slot % 3) * 12288 + kk * 2048 + (pg & 1) * 32;
    return trread2(ab[slot / 3][pg >> 1], off, off + 512);
  };
  const rsrc_t sh = mk_rsrc(ws + WS_H, HBUF_B);
  const unsigned hv = (unsigned)((((tok0 >> 4) + 2 * wave + ((lane >> 4) & 1)) * KSTOT + (col0 >> 5)) * 1024 + (lane >> 5) * 256 + (lane & 15) * 16);
  const unsigned stg = smem + (32 * wave + (lane & 31)) * QROW + (lane >> 5) * 16;
  f32x16 acc[NPG];
  bf16x8 af[DA];
  u32x4 hp;
  u32x4 pk[2][NPG][2];
  auto epi = [&](auto SU, auto PG, auto J) {
    constexpr int su = SU, pg = PG, j = J;
    if constexpr (!QKV) {
      if constexpr (j >= 1 && j <= 4) {
        float hh[2];
        sfor<2>([&](auto E) {
          const float g = acc[pg][2 * (j - 1) + E], u = acc[pg][8 + 2 * (j - 1) + E];
          const float sg = __builtin_amdgcn_rcpf(1.0f + __builtin_amdgcn_exp2f(g * cexp));
          hh[E] = (g * u) * (sg * rs2);
        });
        hp[j - 1] = cvtpk(hh[0], hh[1]);
      } else if constexpr (j == 5) {
        __builtin_amdgcn_raw_buffer_store_b128(hp, sh, hv + pg * 512, su * 2048, H_AUX);
      }
    } else {
      if constexpr (j == 1 || j == 2) {
        u32x4 t;
        sfor<4>([&](auto E) { t[(int)E] = cvtpk(acc[pg][8 * (j - 1) + 2 * E] * rs, acc[pg][8 * (j - 1) + 2 * E + 1] * rs); });
        if constexpr (su == 0) sfor<4>([&](auto E) { *(LDS unsigned*)(stg + LDS_K + 96 * (j - 1) + 32 * pg + 4 * E) = t[(int)E]; });
        else pk[su - 1][pg][j - 1] = t;
      }
    }
  };
  STAMP(3);
  GAP(); wait_barrier<3 * (AHEAD - 2)>(); GAP();
  sfor<DA - 1>([&](auto N) { af[N] = aread(N); });
  GAP();
  STAMP(4);
  sfor<NIT>([&](auto N) {
    constexpr int n = N, st = n / IPS, it = n % IPS, pg = it / 6, kk = it % 6, su = st / 6, c = st % 6;
    if constexpr (it == 0 && st > 0 && st < 17) {
      constexpr int last = (st - 1 + AHEAD) < 17 ? (st - 1 + AHEAD) : 17;
      wait_barrier<3 * (last - (st + 1))>(); GAP();
    }
    if constexpr (n + DA - 1 < NIT) af[(n + DA - 1) % DA] = aread(ic<n + DA - 1>{});
    constexpr int kap = 6 * c + kk;
    const bf16x8 bf = __builtin_bit_cast(bf16x8, xr[kap >> 2][kap & 3]);
    if constexpr (c == 0 && kk == 0) acc[pg] = __builtin_amdgcn_mfma_f32_32x32x16_bf16(af[n % DA], bf, f32x16{}, 0, 0, 0);
    else acc[pg] = __builtin_amdgcn_mfma_f32_32x32x16_bf16(af[n % DA], bf, acc[pg], 0, 0, 0);
    GAP();
    if constexpr (it % 2 == 1 && it < 6 && st + AHEAD < 18) { dma(ic<st + AHEAD>{}, ic<it / 2>{}); }
    if constexpr (c == 5 && pg > 0) epi(ic<su>{}, ic<pg - 1>{}, ic<kk>{});
    if constexpr (c == 0 && pg == 0 && su > 0) epi(ic<su - 1>{}, ic<NPG - 1>{}, ic<kk>{});
    GAP();
    if constexpr (it == IPS - 1 && c == 5) STAMP(5 + su);
  });
  sfor<6>([&](auto J) { epi(ic<2>{}, ic<NPG - 1>{}, J); });
  if constexpr (QKV) {
    lds_barrier();
    sfor<NPG>([&](auto PG) { sfor<2>([&](auto Hf) { sfor<4>([&](auto E) {
      *(LDS unsigned*)(stg + LDS_V + 96 * Hf + 32 * PG + 4 * E) = pk[0][PG][Hf][(int)E];
      *(LDS unsigned*)(stg + LDS_Q + 96 * Hf + 32 * PG + 4 * E) = pk[1][PG][Hf][(int)E];
    }); }); });
    lds_barrier();
    STAMP(8);
    const int tid = wave * 64 + lane, lo = head0 * 18 - col0;
    if (tid < nheads * 80) {
      const int hd = tid / 80, i = tid % 80, t0 = 16 + 6 * (i >> 1) + (i & 1);
      const unsigned rowb = smem + (t0 - 14) * QROW + (lo + hd * 18) * 2;
      unsigned q[3][9];
      sfor<3>([&](auto A) { sfor<9>([&](auto E) { q[A][E] = *(LDS unsigned*)(rowb + LDS_Q + (14 + 2 * A) * QROW + 4 * E); }); });
      float lg[3][8];
      sfor<10>([&](auto R) {
        unsigned kx[9];
        sfor<9>([&](auto E) { kx[E] = *(LDS unsigned*)(rowb + LDS_K + 2 * R * QROW + 4 * E); });
        sfor<3>([&](auto A) {
          if constexpr (CV(R) - CV(A) >= 0 && CV(R) - CV(A) < 8) { float s = 0.f; sfor<9>([&](auto E) { s = dot2(q[A][E], kx[E], s); }); lg[A][R - A] = s; }
        });
      });
      const int g0 = tok0 + t0 - 14;
      constexpr float SC = 0.23570226039551584f * 1.4426950408889634f;
      sfor<3>([&](auto A) {
        float mx = -3.0e38f;
        sfor<8>([&](auto W) { lg[A][W] = (g0 + 2 * (A + W) >= 0) ? lg[A][W] * SC : -3.0e38f; mx = fmaxf(mx, lg[A][W]); });
        float sum = 0.f;
        sfor<8>([&](auto W) { lg[A][W] = __builtin_amdgcn_exp2f(lg[A][W] - mx); sum += lg[A][W]; });
        const float inv = __builtin_amdgcn_rcpf(sum);
        sfor<8>([&](auto W) { lg[A][W] *= inv; });
      });
      float o[3][18];
      sfor<3>([&](auto A) { sfor<18>([&](auto E) { o[A][E] = 0.f; }); });
      sfor<10>([&](auto R) {
        sfor<9>([&](auto E) {
          const unsigned vx = *(LDS unsigned*)(rowb + LDS_V + 2 * R * QROW + 4 * E);
          const float a = bflo(vx), b = bfhi(vx);
          sfor<3>([&](auto A) { if constexpr (CV(R) - CV(A) >= 0 && CV(R) - CV(A) < 8) { o[A][2 * E] += lg[A][R - A] * a; o[A][2 * E + 1] += lg[A][R - A] * b; } });
        });
      });
      sfor<3>([&](auto A) { sfor<9>([&](auto E) { *(LDS unsigned*)(rowb + LDS_Q + (14 + 2 * A) * QROW + 4 * E) = cvtpk(o[A][2 * E], o[A][2 * E + 1]); }); });
    }
    lds_barrier();
    STAMP(9);
    const int hi = lo + nheads * 18;
    for (int n = tid; n < 240 * 12; n += 512) {
      const int p = n / 240, tl = 16 + n % 240, tg = tok0 + tl;
      if (tg >= S || 8 * p + 8 <= lo || 8 * p >= hi) continue;
      u32x4 v;
      sfor<4>([&](auto E) { v[(int)E] = *(LDS unsigned*)(smem + LDS_Q + tl * QROW + 16 * p + 4 * E); });
      const int k8 = (F + col0) / 8 + p;
      const unsigned off = (unsigned)(((tg >> 4) * KSTOT + (k8 >> 2)) * 1024 + ((k8 & 3) * 16 + (tg & 15)) * 16);
      if (8 * p >= lo && 8 * p + 8 <= hi) __builtin_amdgcn_raw_buffer_store_b128(v, sh, off, 0, H_AUX);
      else sfor<4>([&](auto E) { if (8 * p + 2 * E >= lo && 8 * p + 2 * E < hi) __builtin_amdgcn_raw_buffer_store_b32(v[(int)E], sh, off + 4 * E, 0, H_AUX); });
    }
  }
  STAMP(10);
}

__global__ __launch_bounds__(512, 2) void k1(const char* __restrict__ x, const char* __restrict__ wqkv, const char* __restrict__ wg,
                                             const char* __restrict__ wu, char* __restrict__ ws) {
  __shared__ __attribute__((aligned(256))) char smem_[K1_LDS];
  const unsigned smem = (unsigned)(size_t)(LDS char*)smem_;
  const int wave = rfl(threadIdx.x >> 6), lane = threadIdx.x & 63;
  const int xcd = blockIdx.x & 7, r = blockIdx.x >> 3;
  if (r < 24) {
    const int rt = (xcd & 1) * 4 + r / 6, cw = (xcd >> 1) * 6 + r % 6;
    k1_body<false>(x, wg, wu, ws, smem, rt * 256, cw * 192, 0, 0, wave, lane);
  } else {
    const int item = xcd * 8 + (r - 24);
    if (item == 63) {
      if (threadIdx.x < 64) *(unsigned*)(ws + WS_CNT + threadIdx.x * 256) = 0u;
      return;
    }
    const int t9 = item / 7, g = item % 7;
    const int head0 = g < 4 ? 5 * g : 4 * g + 4, nh = g < 4 ? 5 : 4;
    const int c0 = g == 6 ? 480 : (head0 * 18) & ~7;
    k1_body<true>(x, wqkv, wqkv, ws, smem, t9 * 240 - 16, c0, head0, nh, wave, lane);
  }
}

constexpr int K2_SLOT = 64 * 288, K2_NS = 21, K2_LDS = 8 * K2_SLOT;

__global__ __launch_bounds__(512, 2) void k2(const char* __restrict__ x, const char* __restrict__ wo, const char* __restrict__ wd,
                                             char* __restrict__ ws, char* __restrict__ out) {
  __shared__ __attribute__((aligned(256))) char smem_[K2_LDS];
  const unsigned smem = (unsigned)(size_t)(LDS char*)smem_;
  const int wave = rfl(threadIdx.x >> 6), lane = threadIdx.x & 63;
  const int xcd = blockIdx.x & 7, r = blockIdx.x >> 3;
  const int b = xcd >> 1, rt = (xcd & 1) * 8 + (r >> 2), nt = r & 3, tile = rt * 4 + nt;
  const int tg = wave & 3, kh = wave >> 2;
  unsigned long long* dbg = (unsigned long long*)(ws + WS_DBG) + 256 * 8 * 16; (void)dbg;
  STAMP(0);
  const int oid = b == 0 ? 0 : 2 * b + 1;
  const rsrc_t sWd = mk_rsrc(wd + (size_t)b * 1152 * 1152, 1152u * 1152u);
  const rsrc_t sWo = mk_rsrc(wo + (size_t)oid * 64 * 1152, (b == 0 ? 3u : 2u) * 64u * 1152u);
  const rsrc_t sWz = mk_rsrc(wo, b == 0 ? 3u * 64u * 1152u : 0u);
  const rsrc_t sH = mk_rsrc(ws + WS_H, HBUF_B);
  const rsrc_t sHz = mk_rsrc(ws + WS_H, b == 0 ? HBUF_B : 0u);
  unsigned wv[3];
  sfor<3>([&](auto I) {
    const int n = 48 * I + lane, lr = 8 * wave + n / 18, c = n % 18;
    const int gr = (lr & ~12) | ((lr & 4) << 1) | ((lr & 8) >> 1);
    wv[I] = (unsigned)(gr * 1152 + nt * 288 + c * 16);
  });
  const unsigned m0b = smem + wave * 2304;
  const int t16 = rt * 8 + 2 * tg;
  const unsigned hv0 = (unsigned)(((t16 + kh) * KSTOT + kh) * 1024 + lane * 16), hv1 = (unsigned)(((t16 + 1 - kh) * KSTOT + kh) * 1024 + lane * 16);
  const unsigned hsb = (unsigned)(36 * b * 1024), osb = (unsigned)((144 + 2 * oid) * 1024);
  u32x4 hf[8][2];
  auto issue = [&](auto ST) {
    constexpr int st = ST, so = st < 18 ? st : st - 18;
    dma48x3<(st % 8) * K2_SLOT, so * 64 * 1152>(m0b, wv[0], wv[1], wv[2], st < 18 ? sWd : st < 20 ? sWo : sWz);
    bload16x2<so * 2048>(hf[st % 8][0], hf[st % 8][1], hv0, hv1, st < 18 ? hsb : osb, st < 20 ? sH : sHz);
  };
  sfor<7>([&](auto ST) { issue(ST); });
  unsigned ab[3];
  {
    const int q = lane >> 4, qq = (lane >> 2) & 3, p = lane & 3;
    ab[0] = opaque(smem + (kh * 32 + 16 * (q >> 1) + 4 * (q & 1) + qq) * 288 + p * 8);
    ab[1] = opaque(ab[0] + 3 * K2_SLOT); ab[2] = opaque(ab[0] + 6 * K2_SLOT);
  }
  constexpr int DA = 4, NIT = K2_NS * 9;
  auto aread = [&](auto N) -> bf16x8 {
    constexpr int n = N, st = n / 9, i = n % 9, slot = st % 8, off = (slot % 3) * K2_SLOT + i * 32;
    return trread2(ab[slot / 3], off, off + 2304);
  };
  f32x4 acc[9][2];
  bf16x8 af[DA];
  STAMP(1);
  GAP(); wait_barrier<25>(); GAP();
  STAMP(2);
  sfor<DA - 1>([&](auto N) { af[N] = aread(N); });
  GAP();
  sfor<NIT>([&](auto N) {
    constexpr int n = N, st = n / 9, i = n % 9;
    if constexpr (i == 0 && st > 0 && st < K2_NS - 1) {
      constexpr int last = (st + 6) < K2_NS - 1 ? (st + 6) : K2_NS - 1;
      wait_barrier<5 * (last - (st + 1))>(); GAP();
    }
    if constexpr (n + DA - 1 < NIT) af[(n + DA - 1) % DA] = aread(ic<n + DA - 1>{});
    sfor<2>([&](auto T) {
      const bf16x8 bf = __builtin_bit_cast(bf16x8, hf[st % 8][T]);
      if constexpr (st == 0) acc[i][T] = __builtin_amdgcn_mfma_f32_16x16x32_bf16(af[n % DA], bf, f32x4{}, 0, 0, 0);
      else acc[i][T] = __builtin_amdgcn_mfma_f32_16x16x32_bf16(af[n % DA], bf, acc[i][T], 0, 0, 0);
    });
    GAP();
    if constexpr (i == 0 && st + 7 < K2_NS) { issue(ic<st + 7>{}); GAP(); }
  });
  STAMP(3);
  const bool owner = tg == b;
  const int tok = (t16 + kh) * 16 + (lane & 15);
  const unsigned ooff = (unsigned)(tok * 1152 + nt * 288 + (lane >> 4) * 8);
  u32x2 xres[9];
  if (owner) sfor<9>([&](auto I) { xres[I] = *(const u32x2*)(x + ooff + 32 * I); });
  lds_barrier();
  sfor<9>([&](auto I) { *(LDS f32x4*)(smem + (wave * 9 + I) * 1024 + lane * 16) = acc[I][1]; });
  lds_barrier();
  sfor<9>([&](auto I) { acc[I][0] += *(LDS f32x4*)(smem + ((wave ^ 4) * 9 + I) * 1024 + lane * 16); });
  const rsrc_t sS = mk_rsrc(ws + WS_SLAB, SLAB_B);
  const unsigned sl = (unsigned)(((tile * 4 + tg) * 4 * 2 + kh) * 9 * 1024 + lane * 16);
  if (!owner) sfor<9>([&](auto I) { __builtin_amdgcn_raw_buffer_store_b128(__builtin_bit_cast(u32x4, acc[I][0]), sS, sl + I * 1024, b * 18432, 16); });
  asm volatile("s_waitcnt vmcnt(0)\n\ts_barrier" ::: "memory");
  STAMP(4);
  GAS unsigned* cnt = (GAS unsigned*)(ws + WS_CNT + tile * 256);
  if (threadIdx.x == 0) __hip_atomic_fetch_add(cnt, 1u, __ATOMIC_RELAXED, __HIP_MEMORY_SCOPE_AGENT);
  if (!owner) return;
  for (unsigned spins = 0; spins < (1u << 22); ++spins) {
    if (rfl(__hip_atomic_load(cnt, __ATOMIC_RELAXED, __HIP_MEMORY_SCOPE_AGENT)) >= 4u) break;
    __builtin_amdgcn_s_sleep(1);
  }
  __builtin_amdgcn_fence(__ATOMIC_ACQUIRE, "wavefront");
  STAMP(5);
  u32x4 pv[3][9];
  sfor<3>([&](auto J) { const int src = (b + 1 + J) & 3; sfor<9>([&](auto I) { pv[J][I] = __builtin_amdgcn_raw_buffer_load_b128(sS, sl + I * 1024, src * 18432, 16); }); });
  sfor<9>([&](auto I) {
    f32x4 v = acc[I][0];
    sfor<3>([&](auto J) { v += __builtin_bit_cast(f32x4, pv[J][I]); });
    u32x2 o2 = {cvtpk(v[0] + bflo(xres[I][0]), v[1] + bfhi(xres[I][0])), cvtpk(v[2] + bflo(xres[I][1]), v[3] + bfhi(xres[I][1]))};
    *(u32x2*)(out + ooff + 32 * I) = o2;
  });
  STAMP(6);
}

}

extern "C" void kernel_launch(void* const* d_in, const int* in_sizes, int n_in, void* d_out, int out_size, void* d_ws, size_t ws_size,
                              hipStream_t stream) {
  const char* x = (const char*)d_in[0];
  psk::k1<<<256, 512, 0, stream>>>(x, (const char*)d_in[1], (const char*)d_in[4], (const char*)d_in[5], (char*)d_ws);
  psk::k2<<<256, 512, 0, stream>>>(x, (const char*)d_in[2], (const char*)d_in[6], (char*)d_ws, (char*)d_out);
}
```

```cpp
#include <hip/hip_runtime.h>
#include <utility>
#pragma clang diagnostic ignored "-Wint-to-pointer-cast"

typedef float    f32x4  __attribute__((ext_vector_type(4)));
typedef float    f32x2  __attribute__((ext_vector_type(2)));
typedef __bf16   bf16x8 __attribute__((ext_vector_type(8)));
typedef __bf16   bf16x4 __attribute__((ext_vector_type(4)));
typedef __bf16   bf16x2 __attribute__((ext_vector_type(2)));
typedef unsigned u32x4  __attribute__((ext_vector_type(4)));
typedef unsigned u32x2  __attribute__((ext_vector_type(2)));
typedef unsigned long long u64;
typedef __amdgpu_buffer_rsrc_t rsrc_t;
#define LDS __attribute__((address_space(3)))
#define DI __device__ __forceinline__
#define GAP() __builtin_amdgcn_sched_barrier(0)
#define CV(X) (decltype(X)::value)

namespace psk {

constexpr int S = 2048, D = 576, F = 4608;
constexpr int KSTOT = 165;
constexpr unsigned HBUF_B = 128u * KSTOT * 1024u;
constexpr unsigned SLAB_B = 64u * 8u * 4u * 5120u;
constexpr unsigned XN_B = S * D * 2u;
constexpr unsigned FS = 128;
constexpr unsigned FL_EPOCH = 0, FL_X = FS, FL_H = FL_X + 256 * FS, FL_O = FL_H + 64 * 24 * 4 * FS, FL_S = FL_O + 128 * 8 * FS, FL_B = FL_S + 64 * 8 * 4 * FS;
constexpr size_t WS_H = 262144, WS_SLAB = WS_H + HBUF_B, WS_XN = WS_SLAB + SLAB_B, WS_FL = WS_XN + XN_B, WS_DBG = WS_FL + FL_B;
constexpr int SC1 = 16;
constexpr unsigned SPIN_CAP = 1u << 18;

template <int N> using ic = std::integral_constant<int, N>;
template <class Fn, int... Is> DI void sfor_impl(Fn&& f, std::integer_sequence<int, Is...>) { (f(ic<Is>{}), ...); }
template <int N, class Fn> DI void sfor(Fn&& f) { sfor_impl(f, std::make_integer_sequence<int, N>{}); }

DI unsigned rfl(unsigned x) { return __builtin_amdgcn_readfirstlane(x); }
DI rsrc_t mk_rsrc(const void* p, unsigned bytes) { return __builtin_amdgcn_make_buffer_rsrc((void*)p, (short)0, (int)bytes, 0x00020000); }
DI unsigned cvtpk(float lo, float hi) { f32x2 v = {lo, hi}; return __builtin_bit_cast(unsigned, __builtin_convertvector(v, bf16x2)); }
DI float rbf(float x) { return __builtin_bit_cast(float, cvtpk(x, x) << 16); }
DI float bflo(unsigned v) { return __builtin_bit_cast(float, v << 16); }
DI float bfhi(unsigned v) { return __builtin_bit_cast(float, v & 0xffff0000u); }
DI bf16x4 trread(unsigned addr, int off) { return __builtin_amdgcn_ds_read_tr16_b64_v4bf16((LDS bf16x4*)(addr + off)); }
DI bf16x8 cat8(bf16x4 a, bf16x4 b) { return __builtin_shufflevector(a, b, 0, 1, 2, 3, 4, 5, 6, 7); }
DI bf16x8 as_bf(u32x4 v) { return __builtin_bit_cast(bf16x8, v); }
DI f32x4 mfma(bf16x8 a, bf16x8 b, f32x4 c) { return __builtin_amdgcn_mfma_f32_16x16x32_bf16(a, b, c, 0, 0, 0); }
DI void mfma_acc(f32x4& c, bf16x8 a, bf16x8 b) { asm volatile("v_mfma_f32_16x16x32_bf16 %0, %1, %2, %0" : "+v"(c) : "v"(a), "v"(b)); }
DI void mfma_new(f32x4& c, bf16x8 a, bf16x8 b) { asm volatile("v_mfma_f32_16x16x32_bf16 %0, %1, %2, 0" : "=v"(c) : "v"(a), "v"(b)); }
DI void mfma_drain() { asm volatile("s_nop 7\n\ts_nop 1"); GAP(); }
template <class T> DI void pin(T& x) { asm volatile("" : "+v"(x)); }
DI unsigned lane_now() { unsigned l; asm volatile("v_mbcnt_lo_u32_b32 %0, -1, 0\n\tv_mbcnt_hi_u32_b32 %0, -1, %0" : "=v"(l)); return l; }
DI unsigned spin(unsigned x) { asm volatile("" : "+s"(x)); return x; }
template <int N> DI void wait_vm() { if constexpr (N < 63) asm volatile("s_waitcnt vmcnt(%0)" ::"i"(N) : "memory"); }
template <int N> DI void wait_barrier() { asm volatile("s_waitcnt vmcnt(%0) lgkmcnt(0)\n\ts_barrier" ::"i"(N) : "memory"); }
DI void lds_barrier() { asm volatile("s_waitcnt lgkmcnt(0)\n\ts_barrier" ::: "memory"); }
DI float v_mul(float a, float b) { float r; asm volatile("v_mul_f32 %0, %1, %2" : "=v"(r) : "v"(a), "v"(b)); return r; }
DI float v_mulk(float a) { float r; asm volatile("v_mul_f32 %0, 0xbfb8aa3b, %1" : "=v"(r) : "v"(a)); return r; }
DI float v_add1(float a) { float r; asm volatile("v_add_f32 %0, 1.0, %1" : "=v"(r) : "v"(a)); return r; }
DI float v_exp(float a) { float r; asm volatile("v_exp_f32 %0, %1" : "=v"(r) : "v"(a)); return r; }
DI float v_rcp(float a) { float r; asm volatile("v_rcp_f32 %0, %1" : "=v"(r) : "v"(a)); return r; }
DI unsigned v_cvtpk(float a, float b) { unsigned r; asm volatile("v_cvt_pk_bf16_f32 %0, %1, %2" : "=v"(r) : "v"(a), "v"(b)); return r; }

#define STAMP2(i) do {} while (0)
#define STAMP(i) do {} while (0)

template <int LOFF, unsigned GOFF> DI void dma16(unsigned m0base, unsigned voff, rsrc_t srd) {
  unsigned t;
  asm volatile("s_add_u32 m0, %1, %2\n\ts_mov_b32 %0, %3\n\tbuffer_load_dwordx4 %4, %5, %0 offen lds"
               : "=&s"(t) : "s"(m0base), "i"(LOFF), "i"(GOFF), "v"(voff), "s"(srd) : "scc", "memory");
}
template <int B, int LOFF> DI void dma_w2(unsigned m0base, unsigned voff, unsigned soff, rsrc_t srd) {
  if constexpr (B == 0) asm volatile("s_add_u32 m0, %0, %1\n\ts_nop 0\n\tbuffer_load_dwordx4 %2, %3, %4 offen lds" ::"s"(m0base), "i"(LOFF), "v"(voff), "s"(srd), "s"(soff) : "scc", "memory");
  else if constexpr (B == 1) asm volatile("buffer_load_dwordx4 %0, %1, %2 offen offset:1024 lds" ::"v"(voff), "s"(srd), "s"(soff) : "memory");
  else asm volatile("s_mov_b64 exec, 0xffff\n\tbuffer_load_dwordx4 %0, %1, %2 offen offset:2048 lds\n\ts_mov_b64 exec, -1" ::"v"(voff), "s"(srd), "s"(soff) : "memory");
}
DI void load16s_sc1(u32x4& d, unsigned voff, unsigned soff, rsrc_t srd) { asm volatile("buffer_load_dwordx4 %0, %1, %2, %3 offen sc1" : "=v"(d) : "v"(voff), "s"(srd), "s"(soff)); }
template <int GOFF> DI void load16_sc1(u32x4& d, unsigned voff, unsigned sbase, rsrc_t srd) {
  unsigned t;
  asm volatile("s_add_u32 %1, %3, %4\n\tbuffer_load_dwordx4 %0, %2, %5, %1 offen sc1" : "=v"(d), "=&s"(t) : "v"(voff), "s"(sbase), "i"(GOFF), "s"(srd) : "scc");
}
template <int OFF> DI void store16_sc1(u32x4 d, rsrc_t srd, unsigned voff, unsigned soff) {
  asm volatile("buffer_store_dwordx4 %0, %1, %2, %3 offen offset:%4 sc1\n\ts_nop 1" ::"v"(d), "v"(voff), "s"(srd), "s"(soff), "i"(OFF) : "memory");
}
DI void store16(u32x4 d, rsrc_t srd, unsigned voff, unsigned soff) {
  asm volatile("buffer_store_dwordx4 %0, %1, %2, %3 offen\n\ts_nop 1" ::"v"(d), "v"(voff), "s"(srd), "s"(soff) : "memory");
}
DI void load16(u32x4& d, unsigned voff, rsrc_t srd) { asm volatile("buffer_load_dwordx4 %0, %1, %2, 0 offen" : "=v"(d) : "v"(voff), "s"(srd)); }
template <int OFF> DI void load8(u32x2& d, unsigned voff, rsrc_t srd) { asm volatile("buffer_load_dwordx2 %0, %1, %2, 0 offen offset:%3" : "=v"(d) : "v"(voff), "s"(srd), "i"(OFF)); }
DI void load4(unsigned& d, unsigned voff, rsrc_t srd) { asm volatile("buffer_load_dword %0, %1, %2, 0 offen" : "=v"(d) : "v"(voff), "s"(srd)); }
DI void load4_sc1(unsigned& d, unsigned voff, rsrc_t srd) { asm volatile("buffer_load_dword %0, %1, %2, 0 offen sc1" : "=v"(d) : "v"(voff), "s"(srd)); }
template <int N, int SLEEP = 2> DI void flags_wait(unsigned& f, unsigned voff, unsigned tag, u64 mask, rsrc_t srd) {
  unsigned cnt;
  asm volatile("s_waitcnt vmcnt(%6)\n\ts_mov_b32 %1, %7\n"
               "1:\n\tv_cmp_ne_u32 vcc, %3, %0\n\ts_nop 1\n\ts_and_b64 vcc, vcc, %4\n\ts_cbranch_scc0 2f\n\t"
               "s_sleep %8\n\tbuffer_load_dword %0, %2, %5, 0 offen sc1\n\ts_waitcnt vmcnt(0)\n\ts_sub_u32 %1, %1, 1\n\ts_cbranch_scc0 1b\n"
               "2:"
               : "+v"(f), "=&s"(cnt) : "v"(voff), "s"(tag), "s"(mask), "s"(srd), "i"(N), "i"(SPIN_CAP), "i"(SLEEP) : "vcc", "scc", "memory");
}
DI void publish(unsigned tag, unsigned soff, rsrc_t srd) {
  unsigned t;
  asm volatile("v_mov_b32 %0, %1\n\ts_waitcnt vmcnt(0)\n\ts_mov_b64 exec, 1\n\tbuffer_store_dword %0, off, %3, %2 sc1\n\ts_mov_b64 exec, -1"
               : "=&v"(t) : "s"(tag), "s"(soff), "s"(srd) : "memory");
}

DI void publish2(unsigned tag, unsigned s0, rsrc_t r0, unsigned s1, rsrc_t r1) {
  unsigned t;
  asm volatile("v_mov_b32 %0, %1\n\ts_waitcnt vmcnt(0)\n\ts_mov_b64 exec, 1\n\tbuffer_store_dword %0, off, %3, %2 sc1\n\tbuffer_store_dword %0, off, %5, %4 sc1\n\ts_mov_b64 exec, -1"
               : "=&v"(t) : "s"(tag), "s"(s0), "s"(r0), "s"(s1), "s"(r1) : "memory");
}
template <int OFF> DI void load8_if(u32x2& d, unsigned voff, rsrc_t srd, unsigned on) {
  asm volatile("s_cmp_lg_u32 %4, 0\n\ts_cbranch_scc1 1f\n\tbuffer_load_dwordx2 %0, %1, %2, 0 offen offset:%3\n1:" : "=v"(d) : "v"(voff), "s"(srd), "i"(OFF), "s"(on) : "scc");
}

template <class T> DI T pick(bool c, T a, T b) { return c ? a : b; }
DI float vmax(float a, float b) { float r; asm("v_max_f32 %0, %1, %2" : "=v"(r) : "v"(a), "v"(b)); return r; }
template <bool MAX> DI float x16(float v) {
  const unsigned u = __builtin_bit_cast(unsigned, v);
  auto a = __builtin_amdgcn_permlane16_swap(u, u, false, false);
  const float p = __builtin_bit_cast(float, (unsigned)a[0]), q = __builtin_bit_cast(float, (unsigned)a[1]);
  return MAX ? vmax(p, q) : p + q;
}
template <bool MAX> DI float x32(float v) {
  const unsigned u = __builtin_bit_cast(unsigned, v);
  auto a = __builtin_amdgcn_permlane32_swap(u, u, false, false);
  const float p = __builtin_bit_cast(float, (unsigned)a[0]), q = __builtin_bit_cast(float, (unsigned)a[1]);
  return MAX ? vmax(p, q) : p + q;
}
template <bool MAX> DI void xq10(float (&m)[10]) {
  sfor<2>([&](auto R) {
    unsigned a[10], b[10];
    sfor<10>([&](auto E) { a[E] = __builtin_bit_cast(unsigned, m[E]); b[E] = a[E]; pin(b[E]); });
    GAP();
    sfor<10>([&](auto E) {
      if constexpr (CV(R) == 0) { auto r = __builtin_amdgcn_permlane16_swap(a[E], b[E], false, false); a[E] = r[0]; b[E] = r[1]; }
      else { auto r = __builtin_amdgcn_permlane32_swap(a[E], b[E], false, false); a[E] = r[0]; b[E] = r[1]; }
    });
    GAP();
    sfor<10>([&](auto E) { const float p = __builtin_bit_cast(float, a[E]), q = __builtin_bit_cast(float, b[E]); m[E] = MAX ? vmax(p, q) : p + q; pin(m[E]); });
    GAP();
  });
}
template <int CTRL> DI float dpp_add(float v) { return v + __builtin_bit_cast(float, __builtin_amdgcn_update_dpp(0, __builtin_bit_cast(int, v), CTRL, 0xf, 0xf, true)); }
DI float wave_sum(float v) { return x32<false>(x16<false>(dpp_add<0x140>(dpp_add<0x141>(dpp_add<0x4e>(dpp_add<0xb1>(v)))))); }
__host__ __device__ constexpr int grp_col0(int g) { return 8 * (int)((0x3C362D21160B00ull >> (8 * g)) & 0xff); }

constexpr int LDS_V = 2 * 24576, LDS_K = 4 * 24576;
constexpr int LDS_LN = 6 * 24576, LDS_B = LDS_LN + 9216;
constexpr int SLOT2 = 64 * 288, NS2 = 21, NIT2 = NS2 * 9, DA = 4, HD = 12;

enum { NONE, W1, XF, TOUCH, W2, H2, SAMPLE,   HST, CHECK, PUB, XRES };
struct Op { int kind, a, b; };
template <bool QKV> struct Plan {
  static constexpr int NCG = QKV ? 6 : 8, KS = QKV ? 3 : 6, NST = 54 / KS, SPU = 18 / KS, NSLOT = QKV ? 4 : 3;
  static constexpr int SB = KS * 8192, IPS = KS * NCG, NIT = NST * IPS, BAR = IPS - (DA - 1);
  static constexpr int EARLY = NSLOT;
  static constexpr int NPRO = 6;
#define XPACE 3
  static constexpr int XP = XPACE;
  static constexpr int NPRE2 = QKV ? 27 : 39;
  static constexpr bool kmajor(int st) { return st % SPU != SPU - 1 && (st < SPU || st % SPU != 0); }
  static constexpr int jof(int n) { return kmajor(n / IPS) ? n % NCG : (n % IPS) / KS; }
  static constexpr int kof(int n) { return kmajor(n / IPS) ? (n % IPS) / NCG : n % KS; }
  static constexpr Op pre2(int i) {
    return i < 6 ? Op{W2, i / 3, i % 3} : i < 18 ? Op{H2, (i - 6) / 2, i & 1} : i < 27 ? Op{W2, 2 + (i - 18) / 3, i % 3} : Op{H2, 6 + (i - 27) / 2, (i - 27) & 1};
  }
  static constexpr Op at1(int n) {
    const int st = n / IPS, it = n % IPS;
    if (QKV) {
      if (n % 2 == 0 && n < 60) return Op{XF, 6 + n / 2, 0};
      if (st > 0 && it < 6 && it % 2 == 1 && st + 3 < NST) return Op{W1, st + 3, it / 2};
      if (st == 3 && it == 9) return Op{TOUCH, 0, 0};
      if (st == 15 && it == 0) return Op{SAMPLE, 0, 0};
      if (st == 15 && it == 12) return Op{CHECK, 0, 0};
      if (n > 15 * IPS + 12 && n % 3 == 1 && (n - 15 * IPS - 13) / 3 < 12) return pre2(6 + (n - 15 * IPS - 13) / 3);
      return Op{NONE, 0, 0};
    }
    if (n % XP == 0 && n < 30 * XP) return Op{XF, NPRO + n / XP, 0};
    if (st == 0) { if (it == 37 || it == 41) return Op{TOUCH, (it - 37) / 4, 0}; }
    if (st == 1) { if (it % 6 == 1 && it > 12) return Op{W1, 3, (it - 13) / 6}; }
    if (st > 1) { const int i0 = st == 4 ? 4 : 0; if (it >= i0 && it < i0 + 12 && it % 2 == 1 && st + 2 < NST) return Op{W1, st + 2, (it - i0) / 2}; }
    if (st == 2 || st == 5) { if (it == 47) return Op{HST, st / 3, 0}; }
    if (st == 3 || st == 6) { if (it == 0) return Op{HST, st / 3 - 1, 1}; if (it == 31 || it == 32) return Op{HST, st / 3 - 1, it - 29}; }
    if (st == 4 && it == 2) return Op{PUB, 0, 0};
    if (st == 4 && it == 18) return Op{TOUCH, 2, 0};
    if (st == 6 && it == 20) return Op{SAMPLE, 0, 0};
    if (st == 7) {
      if (it == 2) return Op{PUB, 1, 0};
      if (it == 8) return Op{CHECK, 0, 0};
      if (it >= 9 && it < 45 && it % 2 == 1) return pre2((it - 9) / 2);
    }
    if (st == 8) {
      if (it == 4) return Op{SAMPLE, 1, 0};
      if (it < 18 && it % 2 == 1) return pre2(18 + it / 2);
      if (it == 22) return Op{CHECK, 1, 0};
      if (it >= 23 && it < 47 && it % 2 == 1) return pre2(27 + (it - 23) / 2);
      if (it == 47) return Op{HST, 2, 0};
    }
    return Op{NONE, 0, 0};
  }
  static constexpr Op at2(int m) {
    if (!QKV && m == 0) return Op{HST, 2, 1};
    if (QKV && m < 24 && m % 2 == 0) return pre2(27 + m / 2);
    if (m < 30 && m % 2 == 1) { const int e = m / 2; if (e < 9 || m > 16) return Op{W2, 5 + e / 3, e % 3}; }
    if (!QKV && (m == 31 || m == 32)) return Op{HST, 2, m - 29};
    if (m == 30) return Op{SAMPLE, 2, 0};
    if (!QKV && m == 44) return Op{PUB, 2, 0};
    if (m == 45) return Op{CHECK, 2, 0};
    if (m >= 46 && m < 52) return Op{W2, 10 + (m - 46) / 3, (m - 46) % 3};
    if (m >= 54) { const int d = (m - 54) / 18, e = (m - 54) % 18; if (e % 3 == 0 && 12 + 2 * d + e / 9 < NS2) return Op{W2, 12 + 2 * d + e / 9, (e / 3) % 3}; }
    if (m >= 55 && m < 72 && m % 3 == 1) return Op{H2, 12 + (m - 55) / 6, ((m - 55) / 3) & 1};
    if (m == 82) return Op{SAMPLE, 3, 0};
    if (m == 94) return Op{CHECK, 3, 0};
    if (m > 94 && m % 3 != 0) { const int e = (m - 90) - (m - 90) / 3 - 4; if (e >= 0 && e < 12) return Op{H2, 15 + e / 2, e & 1}; }
    if (m >= 160 && m < 178 && m % 2 == 0) return Op{XRES, (m - 160) / 2, 0};
    return Op{NONE, 0, 0};
  }
  static constexpr int wait1(int n0, int kind, int amax) {
    int cnt = 0, need = -1;
    for (int i = -NPRO - 1; i < n0; ++i) {
      const Op o = i < -1 ? Op{XF, i + NPRO + 1, 0} : i < 0 ? Op{TOUCH, 9, 0} : at1(i);
      if (o.kind == PUB) need = -1;
      if (o.kind == NONE || o.kind > SAMPLE) continue;
      ++cnt;
      if (o.kind == kind && o.a <= amax) need = cnt;
    }
    return need < 0 || cnt - need > 62 ? 63 : cnt - need;
  }
  static constexpr int wait2(int m0, int kind, int amax) {
    int cnt = 0, need = -1;
    for (int i = -NPRE2; i < m0; ++i) {
      const Op o = i < 0 ? pre2(i + NPRE2) : at2(i);
      if (o.kind == PUB) need = -1;
      if (o.kind == NONE || o.kind > SAMPLE) continue;
      ++cnt;
      if (o.kind == kind && o.a <= amax) need = cnt;
    }
    return need < 0 || cnt - need > 62 ? 63 : cnt - need;
  }
};
constexpr int cmin(int a, int b) { return a < b ? a : b; }

struct Args { const char* x; const char* wqkv; const char* wo; const char* wg; const char* wu; const char* wd; char* ws; char* out; };

template <bool QKV>
DI void body(const Args& A, unsigned smem, int tok0, int col0, int grp, int share, bool spare, int wave, int lane) {
  using P = Plan<QKV>;
  constexpr int NCG = P::NCG, KS = P::KS, NSLOT = P::NSLOT, IPS = P::IPS, NIT = P::NIT, SPU = P::SPU, SB = P::SB;
  constexpr int PL1 = QKV ? 3 : 4;
  constexpr unsigned RS = QKV ? 3u * D * 2u : F * 2u, WB = QKV ? D * 3 * D * 2 : D * F * 2;
  char* ws = A.ws;
  const int bid = blockIdx.x;
  STAMP(0);
  const rsrc_t sF = mk_rsrc(ws + WS_FL, FL_B);
  const int isU = wave >> 2, w3 = wave & 3;
  unsigned wv;
  {
    const int cgl = ((lane & 7) >> 1) ^ ((lane >> 4) & 3);
    wv = (unsigned)((16 * (w3 & 1) + 4 * (w3 >> 1) + 8 * (lane >> 5) + ((lane >> 3) & 3)) * RS + col0 * 2 + (QKV && cgl == 3 ? 2 : cgl) * 32 + (lane & 1) * 16);
  }
  const char* wA = QKV ? A.wqkv : A.wg;
  const char* wB = QKV ? A.wqkv + 96 : A.wu;
  const rsrc_t sw = mk_rsrc(isU ? wB : wA, QKV && isU ? WB - 96 : WB);
  const unsigned m0b = smem + isU * (SB / 2) + w3 * 1024;
  auto w1 = [&](auto ST, auto B, unsigned m0, rsrc_t srd) {
    constexpr int a = CV(ST), b = CV(B), su = a / SPU;
    constexpr unsigned sucol = QKV ? (su == 0 ? 1152u : su == 1 ? 2304u : 0u) : su * 128u;
    dma16<(a % NSLOT) * SB + b * 4096, 32 * (KS * (a % SPU) + b) * RS + sucol>(m0, wv, srd);
  };
  unsigned tag, tch[3];
  {
    u32x4 v; unsigned v9, ep;
    const rsrc_t sx = mk_rsrc(A.x, S * D * 2);
    const unsigned xo = (unsigned)((bid * 8 + wave) * 1152);
    load16(v, xo + lane * 16, sx);
    asm volatile("buffer_load_ushort %0, %1, %2, 0 offen offset:1024" : "=v"(v9) : "v"(xo + lane * 2), "s"(sx));
    load4_sc1(ep, FL_EPOCH, sF);
    asm volatile("s_barrier" ::: "memory");
    const unsigned tr = QKV ? ((tok0 + 16) / 240) * 64 + lane : ((tok0 >> 8) & 3) * 144 + (w3 - 1) * 64 + lane;
    const bool ton = !spare && w3 > 0 && (!QKV || isU);
    load4(tch[0], (!QKV && w3 == 3 && lane >= 16 ? 0x80000000u : 0u) + tr * RS + col0 * 2 + (QKV ? 1152 - 94 + 94 * w3 : 0), mk_rsrc(isU && !QKV ? wB : wA, ton ? WB : 0u));
    wait_vm<1>();
    pin(v); pin(v9); pin(ep);
    STAMP(1);
    tag = rfl(ep) * 1664525u + 1013904223u;
    float f[9];
    sfor<4>([&](auto E) { f[2 * E] = bflo(v[(int)E]); f[2 * E + 1] = bfhi(v[(int)E]); });
    f[8] = bflo(v9);
    const float mean = wave_sum(((f[0] + f[1]) + (f[2] + f[3])) + ((f[4] + f[5]) + (f[6] + f[7])) + f[8]) * (1.0f / D);
    float q = 0.f;
    sfor<9>([&](auto E) { const float d = f[E] - mean; q += d * d; });
    const float var = wave_sum(q) * (1.0f / D);
    const float mb = rbf(mean), rs = __builtin_amdgcn_rcpf(rbf(__builtin_amdgcn_sqrtf(rbf(rbf(var) + 1e-5f))));
    sfor<4>([&](auto E) { const unsigned c = cvtpk(f[2 * E] - mb, f[2 * E + 1] - mb); v[(int)E] = cvtpk(bflo(c) * rs, bfhi(c) * rs); });
    *(LDS u32x4*)(smem + LDS_LN + lane * 128 + wave * 16) = v;
    *(LDS unsigned short*)(smem + LDS_LN + 8192 + (lane >> 3) * 128 + wave * 16 + (lane & 7) * 2) = (unsigned short)cvtpk(rbf(f[8] - mb) * rs, 0.f);
  }
  lds_barrier();
  STAMP(2);
  const rsrc_t sxn = mk_rsrc(ws + WS_XN, XN_B);
  if (isU && !spare) {
    const rsrc_t sa = mk_rsrc(wA, WB);
    sfor<P::EARLY>([&](auto ST) { sfor<KS>([&](auto B) { w1(ST, B, m0b - SB / 2, sa); w1(ST, B, m0b, sw); }); });
  }
  if (wave == 0) {
    u32x4 t[9];
    sfor<9>([&](auto I) { t[I] = *(LDS u32x4*)(smem + LDS_LN + 1024 * I + lane * 16); });
    const unsigned o = (unsigned)((bid >> 1) * 18432 + (bid & 1) * 128 + (lane >> 3) * 256 + (lane & 7) * 16);
    sfor<9>([&](auto I) { store16_sc1<0>(t[I], sxn, o, 2048 * I); });
    publish(tag, FL_X + FS * bid, sF);
    STAMP(3);
    if (spare) {
      sfor<4>([&](auto I) { unsigned f; const unsigned fo = FL_X + FS * (64 * I + lane); load4_sc1(f, fo, sF); flags_wait<0>(f, fo, tag, ~0ull, sF); });
      publish(tag, FL_EPOCH, sF);
    } else {
      const int fi = (tok0 >> 3) + (lane & 31);
      const unsigned fo = FL_X + FS * (fi < 0 ? 0 : fi > 255 ? 255 : fi);
      unsigned f; load4_sc1(f, fo, sF); flags_wait<0>(f, fo, tag, ~0ull, sF);
    }
  }
  asm volatile("s_barrier" ::: "memory");
  STAMP(4);
  const int t16 = (tok0 >> 4) + 2 * wave;
  const rsrc_t sh = mk_rsrc(ws + WS_H, HBUF_B);
  const int xcd = bid & 7, b2 = xcd >> 1, rt2 = (xcd & 1) * 8 + (bid >> 5), nt2 = (bid >> 3) & 3, tg = wave & 3, kh = wave >> 2;
  const int u16 = rt2 * 8 + 2 * tg;
  const rsrc_t sWd = mk_rsrc(A.wd, F * D * 2);
  const unsigned m0c = smem + wave * 2304, hs = (unsigned)(36 * b2 + kh);
  const unsigned hb0 = spin((u16 + kh) * KSTOT * 1024);
  const unsigned ws2 = spin(hs * (32 * 1152)), hs2 = spin(hb0 + hs * 1024);
  const rsrc_t sh1 = mk_rsrc(ws + WS_H - KSTOT * 1024, HBUF_B + KSTOT * 1024);
  const unsigned nto = spin(nt2 * 288), oso = spin((u16 + kh) * 16 * 1152 + nto);
  const unsigned oj = spin(6 * b2 + kh);
  const unsigned fh0 = spin(FL_H + ((rt2 * 4 + tg) * 24 + 6 * b2) * 4 * FS), fo0 = spin(FL_O + (u16 * 8 + 2 * b2) * FS), fo1 = spin(b2 == 3 ? 0 : FS);
  const unsigned guest = spin(tg ^ b2);
  u32x4 xf[18][2], hf[HD][2], hp[2];
  unsigned l16 = lane * 16, wv2[3], hv0, hv1, fl, flo, ooff, sw2, sh2, nw2, nh2;
  u32x2 xres[9];
  auto setup2 = [&]() {
    const unsigned l = lane_now();
    sfor<3>([&](auto I) { const unsigned n = 64 * I + l, r = n / 18; wv2[I] = (8 * tg + r) * 1152 + nto + (n - 18 * r) * 16 - 1024 * I; pin(wv2[I]); });
    hv0 = l * 16 + KSTOT * 1024; pin(hv0); hv1 = l * 16 + (2 - 2 * kh) * KSTOT * 1024; pin(hv1);
  };
  auto vmem = [&](auto K, auto AA, auto BB, auto NN) {
    constexpr int kind = CV(K), a = CV(AA), b = CV(BB);
    if constexpr (kind == W1) w1(AA, BB, m0b, sw);
    else if constexpr (kind == XF) {
      const int tt = t16 + (a & 1);
      load16_sc1<(a / 2) * 1024>(xf[a / 2][a & 1], l16, (unsigned)(tt * 18432), QKV ? mk_rsrc(ws + WS_XN, tt >= 0 && tt < 128 ? XN_B : 0u) : sxn);
    } else if constexpr (kind == TOUCH && QKV) {
      const unsigned l = lane_now();
      load4(tch[0], (((tok0 + 16) / 240) * 64 + l) * RS + col0 * 2 + (isU ? 2304 : 0) + (w3 < 3 ? 94 * w3 : 0x80000000u), mk_rsrc(wA, WB));
    } else if constexpr (kind == TOUCH && a < 2) {
      const unsigned l = lane_now();
      const unsigned id = ((tok0 >> 8) & 3) * 288 + w3 * 72 + 64 * a + l;
      load4(tch[a], (id >> 1) * RS + col0 * 2 + 128 + (id & 1) * 128 + (a == 1 && l >= 8 ? 0x80000000u : 0u), sw);
    } else if constexpr (kind == TOUCH) {
      const unsigned gi = spin((bid >> 3) * 8 + wave);
      const bool isO = gi >= 162;
      const unsigned o0 = spin(grp_col0(2 * b2) * 1152 - 162 * 8192), o1 = spin(b2 * 1152 * 1152);
      load4(tch[0], (isO ? o0 : o1) + gi * 8192 + 8 * l16, mk_rsrc(isO ? A.wo : A.wd, isO ? D * D * 2 : F * D * 2));
    } else if constexpr (kind == XRES) {
      if constexpr (a == 0) { const unsigned l = lane_now(); ooff = (l & 15) * 1152 + oso + (l >> 4) * 8; }
      load8_if<32 * a>(xres[a], ooff, mk_rsrc(A.x, S * D * 2), guest);
    } else if constexpr (kind == W2 || kind == H2) {
      constexpr bool isO = a >= 12 && a < 15;
      if constexpr (b == 0 && !isO) {
        constexpr int e = a < 12 ? a : a - 3, ko = 6 * (e % 6) + 2 * (e / 6);
        if constexpr (kind == W2) sw2 = spin(ws2 + ko * 32 * 1152); else sh2 = spin(hs2 + ko * 1024);
      } else if constexpr (b == 0) {
        const unsigned j = spin(oj) + 2 * (a - 12), g = (j * 43) >> 7;
        if constexpr (kind == W2) { sw2 = spin((grp_col0(g) + 32 * (j - 3 * g)) * 1152); nw2 = spin(j < 21 ? D * D * 2 : 0); }
        else { sh2 = spin(hb0 + (144 + j) * 1024); nh2 = spin(j < 21 ? HBUF_B + KSTOT * 1024 : 0u); }
      }
      if constexpr (kind == W2) dma_w2<b, (a % 8) * SLOT2>(m0c, wv2[b], sw2, isO ? mk_rsrc(A.wo, nw2) : sWd);
      else load16s_sc1(hf[a % HD][b], b == 0 ? hv0 : hv1, sh2, isO ? mk_rsrc(ws + WS_H - KSTOT * 1024, nh2) : sh1);
    } else if constexpr (kind == SAMPLE) {
      const unsigned l = lane_now();
      unsigned fa = fh0 + FS * l, fb = fo0 + (l & 2) * (4 * FS) + (l & 1) * fo1;
      pin(fa); pin(fb);
      flo = l < 24 ? fa : fb;
      load4_sc1(fl, flo, sF);
    } else if constexpr (kind == CHECK) {
      STAMP2(a == 0 ? 5 : a == 1 ? 7 : a == 2 ? 11 : 13);
      flags_wait<CV(NN), QKV ? 8 : 2>(fl, flo, tag, a == 0 ? 0x111111ull : a == 1 ? 0x222222ull : a == 2 ? 0xf000000ull : 0x444444ull, sF);
      STAMP2(a == 0 ? 6 : a == 1 ? 8 : a == 2 ? 12 : 14);
    } else if constexpr (kind == PUB) {
      u64 t0 = 0, t1 = 0; (void)t0; (void)t1;
      publish(tag, (unsigned)(FL_H + ((((tok0 >> 5) + wave) * 24 + col0 / 192) * 4 + a) * FS), sF);

    } else if constexpr (kind == HST) {
      store16_sc1<0>(hp[b & 1], sh, l16, ((t16 + (b & 1)) * KSTOT + (col0 >> 5) + 2 * a + (b >> 1)) * 1024);
    }
  };
  f32x4 acc[NCG][2], acc2[9][2];
  bf16x4 al[DA], ah[DA];
  unsigned ab[4], ab2[3];
  auto bases2 = [&]() {
    const unsigned l = lane_now();
    ab2[0] = smem + (kh * 32 + (l >> 2)) * 288 + (l & 3) * 8; pin(ab2[0]);
    ab2[1] = ab2[0] + 3 * SLOT2; pin(ab2[1]); ab2[2] = ab2[0] + 6 * SLOT2; pin(ab2[2]);
  };
  auto aread = [&](auto N, auto T) {
    constexpr int n = CV(N);
    bf16x4 r;
    if constexpr (n < NIT) {
      constexpr int st = n / IPS, j = P::jof(n), ks = P::kof(n), isu = QKV ? j / 3 : j & 1, cg = QKV ? j % 3 : j >> 1;
      r = trread(ab[cg], (st % NSLOT) * SB % 49152 + isu * (SB / 2) + ks * 4096 + CV(T) * 2048);
    } else {
      constexpr int m = n - NIT, sl = (m / 9) % 8;
      r = trread(ab2[sl / 3], (sl % 3) * SLOT2 + (m % 9) * 32 + CV(T) * 16 * 288);
    }
    if constexpr (CV(T) == 0) al[n % DA] = r; else ah[n % DA] = r;
  };
  float em, eg[2], er, eh[2];
  auto epi = [&](auto GG, auto GPV) {
    constexpr int g = CV(GG), gpv = CV(GPV), v = g / gpv, ph = g % gpv, p = v >> 3, th = (v >> 2) & 1, r = v & 3;
    if constexpr (g >= 0 && v <= 32) {
      if constexpr (ph == 0 && v < 32) { em = v_mulk(acc[2 * p][th][r]); eg[v & 1] = v_mul(acc[2 * p][th][r], acc[2 * p + 1][th][r]); }
      if constexpr (ph == 1) { if constexpr (v < 32) em = v_exp(em); if constexpr (v > 0) eh[(v - 1) & 1] = v_mul(eg[(v - 1) & 1], er); }
      if constexpr (ph == 2) {
        if constexpr (v < 32) em = v_add1(em);
        if constexpr (v > 0 && (v & 1) == 0) { constexpr int u = v - 2; hp[(u >> 2) & 1][2 * ((u >> 3) & 1) + ((u >> 1) & 1)] = v_cvtpk(eh[0], eh[1]); }
      }
      if constexpr (ph == gpv - 1 && v < 32) er = v_rcp(em);
    }
  };
  u32x2 pk[6][2];
  const unsigned stg = smem + wave * 6144 + lane * 8;
  auto epiq = [&](auto SU, auto HB, auto J) {
    constexpr int su = CV(SU), j = CV(J);
    if constexpr (j >= 2 && j < 8) {
      constexpr int e = j - 2, cg = 3 * CV(HB) + (e >> 1), th = e & 1;
      pin(acc[cg][th]);
      const f32x4 a = acc[cg][th];
      u32x2 t = {cvtpk(a[0], a[1]), cvtpk(a[2], a[3])};
      if constexpr (su == 0) *(LDS u32x2*)(stg + LDS_K + th * 3072 + 512 * cg) = t;
      else { pin(t); pk[cg][th] = t; }
    }
  };
  if (!QKV || !spare) {
    sfor<P::NPRO>([&](auto I) { vmem(ic<XF>{}, I, ic<0>{}, ic<0>{}); });
    {
      constexpr int NSH = QKV ? 7 : 6, PER = (16 * 120 + NSH - 1) / NSH;
      const unsigned id = wave * 40 + lane, ln = share * PER + id, tg16 = ln / 120;
      const int tt = (tok0 >> 4) + (int)tg16;
      load4_sc1(tch[2], lane < 40 && id < PER && tg16 < 16 && tt >= 0 ? (unsigned)(tt * 18432) + 3072 + (ln - tg16 * 120) * 128 : 0x80000000u, sxn);
    }
    {
      const int g = lane >> 4, qq = (lane >> 2) & 3, p = lane & 3, c = (2 * g + (qq >> 1)) & 3;
      sfor<4>([&](auto C) { ab[C] = smem + (4 * g + qq) * 128 + ((CV(C) ^ c) * 32) + 8 * p; pin(ab[C]); });
    }
    GAP(); wait_barrier<P::wait1(0, XF, 1)>(); GAP();
    pin(tch[0]);
    STAMP(5);
    sfor<DA - 1>([&](auto N) { aread(N, ic<0>{}); aread(N, ic<1>{}); });
    GAP();
    sfor<NIT>([&](auto N) {
      constexpr int n = CV(N), st = n / IPS, it = n % IPS, j = P::jof(n), ks = P::kof(n), su = st / SPU, c = st % SPU, m = n + DA - 1;
      constexpr bool more = m < NIT || !QKV;
      if constexpr (QKV && n == NIT - (DA - 1)) { lds_barrier(); GAP(); }
      if constexpr (it == P::BAR && st < P::NST - 1) {
        wait_barrier<P::wait1(n, W1, st + 1)>();
        constexpr int s0 = (st % NSLOT) * SB / 49152, s1 = ((st + 1) % NSLOT) * SB / 49152;
        if constexpr (s0 != s1) sfor<4>([&](auto C) { ab[C] += (unsigned)((s1 - s0) * 49152); pin(ab[C]); });
        GAP();
      }
      if constexpr (!QKV && n == NIT - (DA - 1)) { bases2(); wait_barrier<cmin(P::wait1(n, W2, 1), P::wait1(n, H2, 0))>(); GAP(); }
      { constexpr int pp = (it + DA - 1) % IPS; if constexpr (pp % (IPS / PL1) == 0 && pp / (IPS / PL1) < PL1) { __builtin_amdgcn_s_setprio(PL1 - 1 - pp / (IPS / PL1)); GAP(); } }
      if constexpr (su == 0 && j == 0 && n > 0) { wait_vm<P::wait1(n, XF, 2 * (KS * c + ks) + 1)>(); GAP(); }
      const bf16x8 a = cat8(al[n % DA], ah[n % DA]);
      constexpr bool swp = QKV && su == 1;
      sfor<2>([&](auto T) {
        if constexpr (more) { aread(ic<m>{}, T); GAP(); }
        const bf16x8 b = as_bf(xf[KS * c + ks][T]);
        if constexpr (c == 0 && ks == 0) mfma_new(acc[j][T], swp ? b : a, swp ? a : b); else mfma_acc(acc[j][T], swp ? b : a, swp ? a : b);
        GAP();
        if constexpr (!QKV) {
          if constexpr (c == 2) epi(ic<2 * (it - 14) + CV(T)>{}, ic<4>{});
          else if constexpr (c == 0 && su > 0) epi(ic<2 * (it + 34) + CV(T)>{}, ic<4>{});
          GAP();
        }
      });
      if constexpr (QKV) {
        if constexpr (c == 5 && it >= IPS / 2) epiq(ic<su>{}, ic<0>{}, ic<it - IPS / 2>{});
        if constexpr (c == 0 && it < IPS / 2 && su > 0) epiq(ic<su - 1>{}, ic<1>{}, ic<it>{});
        if constexpr (st == 16 && it < 12) *(LDS u32x2*)(stg + LDS_V + (it % 2) * 3072 + 512 * (it / 2)) = pk[it / 2][it % 2];
      }
      if constexpr (!QKV && st == 4 && it == 8) { pin(tch[0]); pin(tch[1]); }
      if constexpr (!QKV && st == 7 && it == 8) pin(tch[0]);
      if constexpr (n == 36) pin(tch[2]);
      if constexpr (QKV && st == 15 && it == 10) setup2();
      if constexpr (QKV && st == 8 && it == 0) pin(tch[0]);
      if constexpr (!QKV && st == 7 && it == 8) setup2();
      constexpr Op o = P::at1(n);
      if constexpr (o.kind != NONE) vmem(ic<o.kind>{}, ic<o.a>{}, ic<o.b>{}, ic<P::wait1(n, SAMPLE, 9)>{});
      GAP();
      if constexpr (it == IPS - 1 && (!QKV || st % 2 == 1)) STAMP(6 + (QKV ? st / 2 : st));
    });
    if constexpr (QKV) {
      mfma_drain();
      sfor<9>([&](auto J) { epiq(ic<2>{}, ic<1>{}, J); });
      const int i16 = lane & 15, q4 = lane >> 4, odd = lane & 1;
      const int lo = grp == 6 ? 6 : grp < 4 ? 2 * grp : 0, hlo = grp == 6 ? 1 : 0, hhi = grp < 4 || grp == 6 ? 5 : 4;
      const bool cur0 = 4 * q4 + odd <= i16, cur1 = 4 * q4 + 2 + odd <= i16;
      const unsigned s0 = odd ? 0x01000c0cu : 0x0c0c0100u, s1 = odd ? 0x03020c0cu : 0x0c0c0302u;
      const unsigned sel[4] = {cur0 ? 0x0c0c0c0cu : s0, cur1 ? 0x0c0c0c0cu : s1, cur0 ? s0 : 0x0c0c0c0cu, cur1 ? s1 : 0x0c0c0c0cu};
      constexpr float SC = 0.23570226039551584f * 1.4426950408889634f;
      auto ld2 = [&](unsigned a0, unsigned a1) { const u32x2 u = *(volatile LDS u32x2*)a0, v = *(volatile LDS u32x2*)a1; return as_bf(u32x4{u[0], u[1], v[0], v[1]}); };
      const unsigned prv0 = wave == 0 ? stg : stg - 3072;
      const float cp = t16 < 0 ? -1.0e30f : 0.f;
      f32x4 sp[10], sc[10];
      float l0[10], l1[10];
      bf16x8 pb[10], kb[5][3], vb[2][6];
      sfor<5>([&](auto H) {
        kb[H][0] = ld2(prv0 + LDS_K + 512 * H, prv0 + LDS_K + 512 * H + 512); kb[H][1] = ld2(stg + LDS_K + 512 * H, stg + LDS_K + 512 * H + 512);
        kb[H][2] = ld2(stg + LDS_K + 3072 + 512 * H, stg + LDS_K + 3072 + 512 * H + 512);
      });
      GAP();
      auto pf = [&](auto I) { constexpr Op o = P::pre2(CV(I)); GAP(); vmem(ic<o.kind>{}, ic<o.a>{}, ic<o.b>{}, ic<0>{}); GAP(); };
      pf(ic<0>{});
      sfor<5>([&](auto HH) {
        constexpr int hd = CV(HH);
        const int sh0 = lo + 2 * hd;
        sfor<2>([&](auto TH) {
          const bf16x8 qb = as_bf(u32x4{4 * q4 >= sh0 ? pk[hd][TH][0] : 0u, 4 * q4 + 2 >= sh0 ? pk[hd][TH][1] : 0u,
                                        4 * q4 <= sh0 ? pk[hd + 1][TH][0] : 0u, 4 * q4 + 2 <= sh0 ? pk[hd + 1][TH][1] : 0u});
          sp[2 * hd + TH] = CV(TH) ? mfma(kb[hd][1], qb, f32x4{cp, cp, cp, cp}) : mfma(kb[hd][0], qb, f32x4{});
          sc[2 * hd + TH] = mfma(kb[hd][1 + TH], qb, f32x4{});
        });
        pf(ic<1 + hd>{});
      });
      GAP();
      sfor<6>([&](auto J) { vb[0][J] = ld2(prv0 + LDS_V + 512 * J, stg + LDS_V + 512 * J); vb[1][J] = ld2(stg + LDS_V + 512 * J, stg + LDS_V + 3072 + 512 * J); });
      GAP();
      lds_barrier();
      float mx[10];
      sfor<10>([&](auto E) {
        l0[E] = pick<float>(cur0, pick<float>(odd, sc[E][1], sc[E][0]), pick<float>(odd, sp[E][1], sp[E][0])) * SC;
        l1[E] = pick<float>(cur1, pick<float>(odd, sc[E][3], sc[E][2]), pick<float>(odd, sp[E][3], sp[E][2])) * SC;
        mx[E] = vmax(l0[E], l1[E]); pin(mx[E]); pin(l0[E]); pin(l1[E]);
        if constexpr (CV(E) % 3 == 0) pf(ic<18 + CV(E) / 3>{});
      });
      GAP();
      xq10<true>(mx);
      pf(ic<22>{});
      sfor<10>([&](auto E) { l0[E] = __builtin_amdgcn_exp2f(l0[E] - mx[E]); l1[E] = __builtin_amdgcn_exp2f(l1[E] - mx[E]); mx[E] = l0[E] + l1[E]; }); GAP();
      pf(ic<23>{});
      xq10<false>(mx);
      pf(ic<24>{});
      sfor<10>([&](auto E) {
        constexpr int hd = CV(E) >> 1;
        const float inv = __builtin_amdgcn_rcpf(mx[E]) * ((hd >= hlo && hd < hhi) ? 1.f : 0.f);
        const unsigned pp = cvtpk(l0[E] * inv, l1[E] * inv);
        pb[E] = as_bf(u32x4{__builtin_amdgcn_perm(pp, pp, sel[0]), __builtin_amdgcn_perm(pp, pp, sel[1]),
                            __builtin_amdgcn_perm(pp, pp, sel[2]), __builtin_amdgcn_perm(pp, pp, sel[3])});
      });
      pf(ic<25>{});
      sfor<10>([&](auto E) { sp[E] = mfma(vb[CV(E) & 1][CV(E) >> 1], pb[E], f32x4{}); sc[E] = mfma(vb[CV(E) & 1][(CV(E) >> 1) + 1], pb[E], f32x4{}); });
      pf(ic<26>{});
      const bool on0 = wave > 0 && t16 < 128, on1 = t16 + 1 < 128;
      sfor<2>([&](auto TH) {
        unsigned ow[12];
        sfor<12>([&](auto E) {
          constexpr int j = CV(E) >> 1, e = CV(E) & 1;
          unsigned a = 0u, b = 0u;
          if constexpr (j < 5) a = cvtpk(sp[2 * j + TH][2 * e], sp[2 * j + TH][2 * e + 1]);
          if constexpr (j > 0) b = cvtpk(sc[2 * j - 2 + TH][2 * e], sc[2 * j - 2 + TH][2 * e + 1]);
          ow[E] = 4 * q4 + 2 * e >= lo + 2 * j ? a : b;
        });
        const rsrc_t so = mk_rsrc(ws + WS_H, (CV(TH) ? on1 : on0) ? HBUF_B : 0u);
        sfor<3>([&](auto M) {
          store16_sc1<1024 * CV(M)>(u32x4{ow[4 * M], ow[4 * M + 1], ow[4 * M + 2], ow[4 * M + 3]}, so, l16, ((t16 + TH) * KSTOT + 144 + 3 * grp) * 1024);
        });
      });
      STAMP(15);
      vmem(ic<SAMPLE>{}, ic<1>{}, ic<0>{}, ic<0>{});
      publish2(tag, (unsigned)(FL_O + (t16 * 8 + grp) * FS), mk_rsrc(ws + WS_FL, on0 ? FL_B : 0u), (unsigned)(FL_O + (t16 * 8 + 8 + grp) * FS), mk_rsrc(ws + WS_FL, on1 ? FL_B : 0u));
      vmem(ic<CHECK>{}, ic<1>{}, ic<0>{}, ic<63>{});
      sfor<6>([&](auto I) { pin(hf[I][0]); pin(hf[I][1]); });
      STAMP(16);
    }
  } else {
    wait_vm<0>(); pin(tch[0]);
    setup2();
    vmem(ic<SAMPLE>{}, ic<0>{}, ic<0>{}, ic<0>{}); vmem(ic<CHECK>{}, ic<0>{}, ic<0>{}, ic<0>{});
    sfor<27>([&](auto I) { constexpr Op o = P::pre2(CV(I)); vmem(ic<o.kind>{}, ic<o.a>{}, ic<o.b>{}, ic<0>{}); });
    vmem(ic<CHECK>{}, ic<1>{}, ic<0>{}, ic<0>{});
    sfor<6>([&](auto I) { pin(hf[I][0]); pin(hf[I][1]); });
  }
  if constexpr (QKV) {
    bases2();
    GAP(); lds_barrier(); GAP();
    sfor<DA - 1>([&](auto N) { aread(ic<NIT + CV(N)>{}, ic<0>{}); aread(ic<NIT + CV(N)>{}, ic<1>{}); });
    GAP();
    STAMP(17);
  }
  sfor<NIT2>([&](auto M) {
    constexpr int m = CV(M), n = NIT + m, st = m / 9, i = m % 9;
    if constexpr (i == 9 - (DA - 1) && st % 2 == 1 && st < 19) { wait_barrier<P::wait2(m, W2, st == 17 ? 20 : st + 2)>(); GAP(); }
    { constexpr int pp = m < 159 ? (m + DA - 1) % 18 : (m - 159) * 3 / 5; if constexpr (pp % 6 == 0 && (m < 159 || (m - 159) % 10 == 0)) { __builtin_amdgcn_s_setprio(2 - pp / 6); GAP(); } }
    if constexpr (i == 0 && st > 0) { wait_vm<P::wait2(m, H2, st)>(); GAP(); }
    const bf16x8 a = cat8(al[n % DA], ah[n % DA]);
    sfor<2>([&](auto T) {
      if constexpr (m + DA - 1 < NIT2) { aread(ic<n + DA - 1>{}, T); GAP(); }
      if constexpr (st == 0) mfma_new(acc2[i][T], a, as_bf(hf[st % HD][T])); else mfma_acc(acc2[i][T], a, as_bf(hf[st % HD][T]));
      GAP();
      if constexpr (!QKV && m < 34) { epi(ic<2 * (m + 34) + CV(T)>{}, ic<4>{}); GAP(); }
    });
    constexpr Op o = P::at2(m);
    if constexpr (o.kind != NONE) { vmem(ic<o.kind>{}, ic<o.a>{}, ic<o.b>{}, ic<P::wait2(m, SAMPLE, 9)>{}); GAP(); }
    if constexpr (m % 27 == 26) STAMP(18 + m / 27);
  });
  mfma_drain();
  sfor<9>([&](auto I) { pin(acc2[I][0]); pin(acc2[I][1]); });
  lane = lane_now();
  const int tile = rt2 * 4 + nt2;
  const unsigned xr = smem + ((wave ^ 4) < 6 ? 5 * SLOT2 + (wave ^ 4) * 9216 : ((wave ^ 4) - 6) * 9216);
  unsigned xw = smem + (wave < 6 ? 5 * SLOT2 + wave * 9216 : (wave - 6) * 9216) + l16, xl = xr + l16;
  pin(xw); pin(xl);
  sfor<9>([&](auto I) { *(LDS f32x4*)(xw + I * 1024) = acc2[I][1]; });
  lds_barrier();
  sfor<9>([&](auto I) { acc2[I][0] += *(LDS f32x4*)(xl + I * 1024); });
  STAMP(26);
  const rsrc_t sS = mk_rsrc(ws + WS_SLAB, SLAB_B);
  const unsigned sl = (unsigned)((tile * 8 + wave) * 4 * 5120);
  if (guest) {
    auto pk2 = [&](f32x4 v, int e) { return cvtpk(v[2 * e], v[2 * e + 1]); };
    sfor<4>([&](auto I) {
      store16_sc1<1024 * CV(I)>(u32x4{pk2(acc2[2 * I][0], 0), pk2(acc2[2 * I][0], 1), pk2(acc2[2 * I + 1][0], 0), pk2(acc2[2 * I + 1][0], 1)}, sS, l16, sl + b2 * 5120);
    });
    __builtin_amdgcn_raw_buffer_store_b64(u32x2{pk2(acc2[8][0], 0), pk2(acc2[8][0], 1)}, sS, 4096 + lane * 8, sl + b2 * 5120, SC1);
    publish(tag, (unsigned)(FL_S + ((tile * 8 + wave) * 4 + b2) * FS), sF);
    STAMP(27);
    return;
  }
  const unsigned one = (lane >> 4) == ((lane >> 2) & 3) ? (lane & 1 ? 0x3f800000u : 0x3f80u) : 0u, e0 = lane & 2 ? 0u : one, e1 = lane & 2 ? one : 0u;
  const bf16x8 elo = as_bf(u32x4{e0, e1, 0u, 0u}), ehi = as_bf(u32x4{0u, 0u, e0, e1});
  unsigned fin[5][2];
  {
    const unsigned fo = FL_S + ((tile * 8 + wave) * 4 + (lane & 3)) * FS;
    unsigned f; load4_sc1(f, fo, sF);
    wait_vm<1>();
    sfor<9>([&](auto I) { pin(xres[I]); acc2[I][0] = mfma(elo, as_bf(u32x4{xres[I][0], xres[I][1], 0u, 0u}), acc2[I][0]); });
    sfor<5>([&](auto I) {
      const unsigned n = 64 * I + lane, t = n / 18, c = n - 18 * t;
      fin[I][0] = t * 304 + c * 16; fin[I][1] = n < 288 ? t * 1152 + c * 16 : 0x80000000u; pin(fin[I][0]); pin(fin[I][1]);
    });
    flags_wait<0, 1>(f, fo, tag, 0xfull & ~(1ull << b2), sF);
  }
  STAMP(28);
  u32x4 pv[3][4]; u32x2 pw[3];
  sfor<3>([&](auto J) {
    const unsigned so = sl + ((b2 + 1 + J) & 3) * 5120;
    sfor<4>([&](auto I) { pv[J][I] = __builtin_amdgcn_raw_buffer_load_b128(sS, l16 + 1024 * I, so, SC1); });
    pw[J] = __builtin_amdgcn_raw_buffer_load_b64(sS, 4096 + lane * 8, so, SC1);
  });
  GAP();
  const unsigned tw = xr + (lane & 15) * 304 + (lane >> 4) * 8;
  sfor<3>([&](auto J) {
    sfor<8>([&](auto I) { acc2[I][0] = mfma(CV(I) & 1 ? ehi : elo, as_bf(pv[J][CV(I) >> 1]), acc2[I][0]); });
    acc2[8][0] = mfma(elo, as_bf(u32x4{pw[J][0], pw[J][1], 0u, 0u}), acc2[8][0]);
  });
  sfor<9>([&](auto I) { const f32x4 v = acc2[I][0]; *(LDS u32x2*)(tw + 32 * I) = u32x2{cvtpk(v[0], v[1]), cvtpk(v[2], v[3])}; });
  asm volatile("" ::: "memory");
  const rsrc_t sO = mk_rsrc(A.out, S * D * 2);
  u32x4 ov[5];
  sfor<5>([&](auto I) { ov[I] = *(LDS u32x4*)(xr + fin[I][0]); });
  sfor<5>([&](auto I) { store16(ov[I], sO, fin[I][1], oso); });
  STAMP(29);
}

__global__ __launch_bounds__(512, 2) void fused(Args A) {
  __shared__ __attribute__((aligned(256))) char smem_[LDS_B];
  const unsigned smem = (unsigned)(size_t)(LDS char*)smem_;
  const int wave = rfl(threadIdx.x >> 6), lane = threadIdx.x & 63;
  const int xcd = blockIdx.x & 7, r = blockIdx.x >> 3;
  if (r < 24) {
    body<false>(A, smem, ((xcd & 1) * 4 + r / 6) * 256, ((xcd >> 1) * 6 + r % 6) * 192, 0, r % 6, false, wave, lane);
  } else {
    const int item = xcd * 8 + (r - 24), t9 = item / 7, g = item % 7;
    body<true>(A, smem, t9 * 240 - 16, grp_col0(g), g, g, item == 63, wave, lane);
  }
}

}

extern "C" void kernel_launch(void* const* d_in, const int* in_sizes, int n_in, void* d_out, int out_size, void* d_ws, size_t ws_size,
                              hipStream_t stream) {
  psk::Args A;
  A.x = (const char*)d_in[0]; A.wqkv = (const char*)d_in[1]; A.wo = (const char*)d_in[2]; A.wg = (const char*)d_in[4];
  A.wu = (const char*)d_in[5]; A.wd = (const char*)d_in[6]; A.ws = (char*)d_ws; A.out = (char*)d_out;
  psk::fused<<<256, 512, 0, stream>>>(A);
}
```

```cpp
#include <hip/hip_runtime.h>
#include <utility>
#pragma clang diagnostic ignored "-Wint-to-pointer-cast"

typedef float    f32x4  __attribute__((ext_vector_type(4)));
typedef float    f32x2  __attribute__((ext_vector_type(2)));
typedef __bf16   bf16x8 __attribute__((ext_vector_type(8)));
typedef __bf16   bf16x4 __attribute__((ext_vector_type(4)));
typedef __bf16   bf16x2 __attribute__((ext_vector_type(2)));
typedef unsigned u32x4  __attribute__((ext_vector_type(4)));
typedef unsigned u32x2  __attribute__((ext_vector_type(2)));
typedef unsigned long long u64;
typedef __amdgpu_buffer_rsrc_t rsrc_t;
#define LDS __attribute__((address_space(3)))
#define DI __device__ __forceinline__
#define GAP() __builtin_amdgcn_sched_barrier(0)
#define CV(X) (decltype(X)::value)

#define TOFF 0
namespace psk {

constexpr int S = 2048, D = 576, F = 4608;
constexpr int KSTOT = 165;
constexpr unsigned HBUF_B = 128u * KSTOT * 1024u;
constexpr unsigned SLAB_B = 64u * 8u * 4u * 5120u;
constexpr unsigned XN_B = S * D * 2u;
constexpr unsigned FS = 128;
constexpr unsigned FL_EPOCH = 0, FL_X = FS, FL_H = FL_X + 256 * FS, FL_O = FL_H + 64 * 24 * 4 * FS, FL_S = FL_O + 128 * 8 * FS, FL_B = FL_S + 64 * 8 * 4 * FS;
constexpr size_t WS_H = 0, WS_SLAB = WS_H + HBUF_B, WS_XN = WS_SLAB + SLAB_B, WS_FL = WS_XN + XN_B, WS_DBG = WS_FL + FL_B;
constexpr int SC1 = 16;
constexpr unsigned SPIN_CAP = 1u << 18;

template <int N> using ic = std::integral_constant<int, N>;
template <class Fn, int... Is> DI void sfor_impl(Fn&& f, std::integer_sequence<int, Is...>) { (f(ic<Is>{}), ...); }
template <int N, class Fn> DI void sfor(Fn&& f) { sfor_impl(f, std::make_integer_sequence<int, N>{}); }

DI unsigned rfl(unsigned x) { return __builtin_amdgcn_readfirstlane(x); }
DI rsrc_t mk_rsrc(const void* p, unsigned bytes) { return __builtin_amdgcn_make_buffer_rsrc((void*)p, (short)0, (int)bytes, 0x00020000); }
DI unsigned cvtpk(float lo, float hi) { f32x2 v = {lo, hi}; return __builtin_bit_cast(unsigned, __builtin_convertvector(v, bf16x2)); }
DI float rbf(float x) { return __builtin_bit_cast(float, cvtpk(x, x) << 16); }
DI float bflo(unsigned v) { return __builtin_bit_cast(float, v << 16); }
DI float bfhi(unsigned v) { return __builtin_bit_cast(float, v & 0xffff0000u); }
DI bf16x4 trread(unsigned addr, int off) { return __builtin_amdgcn_ds_read_tr16_b64_v4bf16((LDS bf16x4*)(addr + off)); }
DI bf16x8 cat8(bf16x4 a, bf16x4 b) { return __builtin_shufflevector(a, b, 0, 1, 2, 3, 4, 5, 6, 7); }
DI bf16x8 as_bf(u32x4 v) { return __builtin_bit_cast(bf16x8, v); }
DI f32x4 mfma(bf16x8 a, bf16x8 b, f32x4 c) { return __builtin_amdgcn_mfma_f32_16x16x32_bf16(a, b, c, 0, 0, 0); }
DI void mfma_acc(f32x4& c, bf16x8 a, bf16x8 b) { asm volatile("v_mfma_f32_16x16x32_bf16 %0, %1, %2, %0" : "+v"(c) : "v"(a), "v"(b)); }
DI void mfma_new(f32x4& c, bf16x8 a, bf16x8 b) { asm volatile("v_mfma_f32_16x16x32_bf16 %0, %1, %2, 0" : "=v"(c) : "v"(a), "v"(b)); }
DI void mfma_drain() { asm volatile("s_nop 7\n\ts_nop 1"); GAP(); }
template <class T> DI void pin(T& x) { asm volatile("" : "+v"(x)); }
DI unsigned lane_now() { unsigned l; asm volatile("v_mbcnt_lo_u32_b32 %0, -1, 0\n\tv_mbcnt_hi_u32_b32 %0, -1, %0" : "=v"(l)); return l; }
DI unsigned spin(unsigned x) { asm volatile("" : "+s"(x)); return x; }
template <int N> DI void wait_vm() { if constexpr (N < 63) asm volatile("s_waitcnt vmcnt(%0)" ::"i"(N) : "memory"); }
template <int N> DI void wait_barrier() { asm volatile("s_waitcnt vmcnt(%0) lgkmcnt(0)\n\ts_barrier" ::"i"(N) : "memory"); }
DI void lds_barrier() { asm volatile("s_waitcnt lgkmcnt(0)\n\ts_barrier" ::: "memory"); }
DI float v_mul(float a, float b) { float r; asm volatile("v_mul_f32 %0, %1, %2" : "=v"(r) : "v"(a), "v"(b)); return r; }
DI float v_expn(float a) { float r; asm volatile("v_exp_f32_e64 %0, -%1" : "=v"(r) : "v"(a)); return r; }
DI float v_fmak(float a, float k) { float r; asm volatile("v_fma_f32 %0, %1, %2, %2" : "=v"(r) : "v"(a), "s"(k)); return r; }
template <unsigned K> DI unsigned v_addk(unsigned a) { unsigned r; asm volatile("v_add_u32 %0, %2, %1" : "=v"(r) : "v"(a), "i"(K)); return r; }
DI float v_rcp(float a) { float r; asm volatile("v_rcp_f32 %0, %1" : "=v"(r) : "v"(a)); return r; }
DI unsigned v_cvtpk(float a, float b) { unsigned r; asm volatile("v_cvt_pk_bf16_f32 %0, %1, %2" : "=v"(r) : "v"(a), "v"(b)); return r; }

#define STAMP(i) do {} while (0)

template <int LOFF, unsigned GOFF> DI void dma16(unsigned m0base, unsigned voff, rsrc_t srd) {
  unsigned t;
  asm volatile("s_add_u32 m0, %1, %2\n\ts_mov_b32 %0, %3\n\tbuffer_load_dwordx4 %4, %5, %0 offen lds"
               : "=&s"(t) : "s"(m0base), "i"(LOFF), "i"(GOFF), "v"(voff), "s"(srd) : "scc", "memory");
}
template <int LOFF, int GOFF> DI unsigned dma_w2a(unsigned m0base, unsigned voff, unsigned sbase, rsrc_t srd) {
  unsigned so;
  asm volatile("s_add_u32 m0, %1, %2\n\ts_add_u32 %0, %5, %6\n\tbuffer_load_dwordx4 %3, %4, %0 offen lds"
               : "=&s"(so) : "s"(m0base), "i"(LOFF), "v"(voff), "s"(srd), "s"(sbase), "i"(GOFF) : "scc", "memory");
  return so;
}
DI void dma_w2b(unsigned voff, unsigned so, rsrc_t srd) { asm volatile("buffer_load_dwordx4 %0, %1, %2 offen offset:1024 lds" ::"v"(voff), "s"(srd), "s"(so) : "memory"); }
DI void dma_w2c(unsigned voff, unsigned so, rsrc_t srd) {
  asm volatile("s_mov_b64 exec, 0xffff\n\tbuffer_load_dwordx4 %0, %1, %2 offen offset:2048 lds\n\ts_mov_b64 exec, -1" ::"v"(voff), "s"(srd), "s"(so) : "memory");
}
template <int GOFF> DI void load16_sc1(u32x4& d, unsigned voff, unsigned sbase, rsrc_t srd) {
  unsigned t;
  asm volatile("s_add_u32 %1, %3, %4\n\tbuffer_load_dwordx4 %0, %2, %5, %1 offen sc1" : "=v"(d), "=&s"(t) : "v"(voff), "s"(sbase), "i"(GOFF), "s"(srd) : "scc");
}
template <int OFF> DI void store16_sc1(u32x4 d, rsrc_t srd, unsigned voff, unsigned soff) {
  asm volatile("buffer_store_dwordx4 %0, %1, %2, %3 offen offset:%4 sc1\n\ts_nop 1" ::"v"(d), "v"(voff), "s"(srd), "s"(soff), "i"(OFF) : "memory");
}
DI void store16(u32x4 d, rsrc_t srd, unsigned voff, unsigned soff) {
  asm volatile("buffer_store_dwordx4 %0, %1, %2, %3 offen\n\ts_nop 1" ::"v"(d), "v"(voff), "s"(srd), "s"(soff) : "memory");
}
DI void load16(u32x4& d, unsigned voff, rsrc_t srd) { asm volatile("buffer_load_dwordx4 %0, %1, %2, 0 offen" : "=v"(d) : "v"(voff), "s"(srd)); }
DI void load4(unsigned& d, unsigned voff, rsrc_t srd) { asm volatile("buffer_load_dword %0, %1, %2, 0 offen" : "=v"(d) : "v"(voff), "s"(srd)); }
template <unsigned SOFF> DI void load4s(unsigned& d, unsigned voff, rsrc_t srd) {
  unsigned t;
  asm volatile("s_mov_b32 %1, %4\n\tbuffer_load_dword %0, %2, %3, %1 offen" : "=v"(d), "=&s"(t) : "v"(voff), "s"(srd), "i"(SOFF));
}
DI void load4_sc1(unsigned& d, unsigned voff, rsrc_t srd) { asm volatile("buffer_load_dword %0, %1, %2, 0 offen sc1" : "=v"(d) : "v"(voff), "s"(srd)); }
template <int N, int SLEEP = 2> DI void flags_wait(unsigned& f, unsigned voff, unsigned tag, u64 mask, rsrc_t srd) {
  unsigned cnt;
  asm volatile("s_waitcnt vmcnt(%6)\n\ts_mov_b32 %1, %7\n"
               "1:\n\tv_cmp_ne_u32 vcc, %3, %0\n\ts_nop 1\n\ts_and_b64 vcc, vcc, %4\n\ts_cbranch_scc0 2f\n\t"
               "s_sleep %8\n\tbuffer_load_dword %0, %2, %5, 0 offen sc1\n\ts_waitcnt vmcnt(0)\n\ts_sub_u32 %1, %1, 1\n\ts_cbranch_scc0 1b\n"
               "2:"
               : "+v"(f), "=&s"(cnt) : "v"(voff), "s"(tag), "s"(mask), "s"(srd), "i"(N), "i"(SPIN_CAP), "i"(SLEEP) : "vcc", "scc", "memory");
}
DI void publish(unsigned tag, unsigned soff, rsrc_t srd) {
  unsigned t;
  asm volatile("v_mov_b32 %0, %1\n\ts_waitcnt vmcnt(0)\n\ts_mov_b64 exec, 1\n\tbuffer_store_dword %0, off, %3, %2 sc1\n\ts_mov_b64 exec, -1"
               : "=&v"(t) : "s"(tag), "s"(soff), "s"(srd) : "memory");
}

DI void publish2(unsigned tag, unsigned s0, rsrc_t r0, unsigned s1, rsrc_t r1) {
  unsigned t;
  asm volatile("v_mov_b32 %0, %1\n\ts_waitcnt vmcnt(0)\n\ts_mov_b64 exec, 1\n\tbuffer_store_dword %0, off, %3, %2 sc1\n\tbuffer_store_dword %0, off, %5, %4 sc1\n\ts_mov_b64 exec, -1"
               : "=&v"(t) : "s"(tag), "s"(s0), "s"(r0), "s"(s1), "s"(r1) : "memory");
}
template <int OFF> DI void load8_if(u32x2& d, unsigned voff, rsrc_t srd, unsigned on) {
  asm volatile("s_cmp_lg_u32 %4, 0\n\ts_cbranch_scc1 1f\n\tbuffer_load_dwordx2 %0, %1, %2, 0 offen offset:%3\n1:" : "=v"(d) : "v"(voff), "s"(srd), "i"(OFF), "s"(on) : "scc");
}

template <class T> DI T pick(bool c, T a, T b) { return c ? a : b; }
DI float vmax(float a, float b) { float r; asm("v_max_f32 %0, %1, %2" : "=v"(r) : "v"(a), "v"(b)); return r; }
template <bool MAX> DI float x16(float v) {
  const unsigned u = __builtin_bit_cast(unsigned, v);
  auto a = __builtin_amdgcn_permlane16_swap(u, u, false, false);
  const float p = __builtin_bit_cast(float, (unsigned)a[0]), q = __builtin_bit_cast(float, (unsigned)a[1]);
  return MAX ? vmax(p, q) : p + q;
}
template <bool MAX> DI float x32(float v) {
  const unsigned u = __builtin_bit_cast(unsigned, v);
  auto a = __builtin_amdgcn_permlane32_swap(u, u, false, false);
  const float p = __builtin_bit_cast(float, (unsigned)a[0]), q = __builtin_bit_cast(float, (unsigned)a[1]);
  return MAX ? vmax(p, q) : p + q;
}
template <bool MAX> DI void xq10(float (&m)[10]) {
  sfor<2>([&](auto R) {
    unsigned a[10], b[10];
    sfor<10>([&](auto E) { a[E] = __builtin_bit_cast(unsigned, m[E]); b[E] = a[E]; pin(b[E]); });
    GAP();
    sfor<10>([&](auto E) {
      if constexpr (CV(R) == 0) { auto r = __builtin_amdgcn_permlane16_swap(a[E], b[E], false, false); a[E] = r[0]; b[E] = r[1]; }
      else { auto r = __builtin_amdgcn_permlane32_swap(a[E], b[E], false, false); a[E] = r[0]; b[E] = r[1]; }
    });
    GAP();
    sfor<10>([&](auto E) { const float p = __builtin_bit_cast(float, a[E]), q = __builtin_bit_cast(float, b[E]); m[E] = MAX ? vmax(p, q) : p + q; pin(m[E]); });
    GAP();
  });
}
template <int CTRL> DI float dpp_add(float v) { return v + __builtin_bit_cast(float, __builtin_amdgcn_update_dpp(0, __builtin_bit_cast(int, v), CTRL, 0xf, 0xf, true)); }
DI float wave_sum(float v) { return x32<false>(x16<false>(dpp_add<0x140>(dpp_add<0x141>(dpp_add<0x4e>(dpp_add<0xb1>(v)))))); }
__host__ __device__ constexpr int grp_col0(int g) { return 8 * (int)((0x3C362D21160B00ull >> (8 * g)) & 0xff); }

constexpr int LDS_V = 2 * 24576, LDS_K = 4 * 24576;
constexpr int LDS_X = 6 * 24576, LDS_B = LDS_X + 16384;
constexpr int SLOT2 = 64 * 288, NS2 = 21, NIT2 = NS2 * 9, DA = 4, HD = 12;
constexpr float LOG2E = 1.4426950408889634f;
constexpr int cmin(int a, int b) { return a < b ? a : b; }

enum { NONE, W1, XF, TOUCH, W2, H2, SAMPLE,   CHECK, PUB, XRES };
struct Op { int kind, a, b; };
template <bool QKV> struct Plan {
  static constexpr int NCG = QKV ? 6 : 8, KS = QKV ? 3 : 9, NST = 54 / KS, SPU = 18 / KS, NSLOT = QKV ? 4 : 2;
  static constexpr int SB = KS * 8192, IPS = KS * NCG, NIT = NST * IPS, BAR = IPS - (DA - 1), NPRO = 6, NPRE2 = QKV ? 24 : 36;
  static constexpr bool kmajor(int n) { return QKV ? ((n / IPS) % 6 != 5 && (n < 6 * IPS || (n / IPS) % 6 != 0)) : n < 96; }
  static constexpr int jof(int n) { return kmajor(n) ? n % NCG : (!QKV && n < 144) ? (n - 96) / 6 : (n % IPS) / KS; }
  static constexpr int sof(int n) { return kmajor(n) ? (n % (18 * NCG)) / NCG : (!QKV && n < 144) ? 12 + n % 6 : KS * ((n / IPS) % SPU) + n % KS; }
  static constexpr bool rebase(int m) {
    if (m >= NIT) { const int p = m - NIT, sl = (p / 9) % 8; return p > 0 && p < NIT2 && p % 9 == 0 && sl % 3 == 0; }
    if (QKV) return m >= IPS && (m / IPS) % 2 == 0 && (kmajor(m) ? m % IPS < 3 : (m % IPS) % 3 == 0 && m % IPS < 9);
    return m < 144 ? (m >= 72 && m < 80) : m % 9 == 0;
  }
  static constexpr int estart(int su) { return su == 0 ? 108 : 144 * su + 92; }
  static constexpr int eidx(int su, int n, int t) {
    if (n < estart(su) || n > estart(su) + 110 || (t == 1 && rebase(n + 4))) return -1;
    int c = 0;
    for (int i = estart(su); i < n; ++i) c += rebase(i + 4) ? 1 : 2;
    return c + t < 176 ? c + t : -1;
  }
  static constexpr int eat(int su, int e) { for (int n = estart(su);; ++n) if (eidx(su, n, 0) == e || eidx(su, n, 1) == e) return n; }
  static constexpr Op pre2(int i) { return i < 12 ? Op{W2, i / 3, i % 3} : Op{H2, (i - 12) / 2, i & 1}; }
  static constexpr Op at1(int n) {
    const int st = n / IPS, it = n % IPS;
    if (QKV) {
      if (n % 2 == 0 && n < 60) return Op{XF, NPRO + n / 2, 0};
      if (st > 0 && it < 6 && it % 2 == 1 && st + 3 < NST) return Op{W1, st + 3, it / 2};
      if (st == 6 && it == 9) return Op{TOUCH, 0, 0};
      if (st == 15 && it == 0) return Op{SAMPLE, 0, 0};
      if (st == 15 && it == 12) return Op{CHECK, 0, 0};
      if (n > 15 * IPS + 12 && n % 3 == 1 && (n - 15 * IPS - 13) / 3 < 12) return pre2(12 + (n - 15 * IPS - 13) / 3);
      return Op{NONE, 0, 0};
    }
    if (n % 3 == 0 && n < 90) return Op{XF, NPRO + n / 3, 0};
    if (st >= 1 && st <= 4) { const int i0 = st == 3 ? 10 : 1; if (it >= i0 && (it - i0) % 3 == 0 && (it - i0) / 3 < 9) return Op{W1, st + 1, (it - i0) / 3}; }
    if (n == 224) return Op{PUB, 0, 0};
    if (st >= 1 && st <= 3 && it == (st == 3 ? 37 : 31)) return Op{TOUCH, st + 2, 0};
    if (st >= 1 && st <= 3 && it == (st == 3 ? 39 : 33)) return Op{TOUCH, st - 1, 1};
    if (n == 330) return Op{SAMPLE, 0, 0};
    if (n == 352) return Op{PUB, 1, 0};
    if (n == 353) return Op{CHECK, 0, 0};
    if (n == 393) return Op{SAMPLE, 1, 0};
    if (n == 405) return Op{CHECK, 1, 0};
    if (n >= 358 && n % 2 == 0 && (n - 358) / 2 < 36) return pre2((n - 358) / 2);
    return Op{NONE, 0, 0};
  }
  static constexpr Op at2(int m) {
    if (QKV && m < 24 && m % 2 == 0) return pre2(24 + m / 2);
    if (m < 36 && m % 2 == 1) return Op{W2, 4 + m / 6, (m / 2) % 3};
    if (m == 38) return Op{SAMPLE, 2, 0};
    if (!QKV && m == 58) return Op{PUB, 2, 0};
    if (m == 59) return Op{CHECK, 2, 0};
    if (m >= 60 && m < 72) return m % 2 == 0 ? Op{W2, 10 + (m - 60) / 6, ((m - 60) / 2) % 3} : Op{H2, 12 + (m - 60) / 4, ((m - 60) / 2) & 1};
    if (m >= 72 && m < 96 && m % 2 == 0) return Op{W2, 12 + (m - 72) / 6, ((m - 72) / 2) % 3};
    if (m == 95) return Op{SAMPLE, 3, 0};
    if (m == 106) return Op{CHECK, 3, 0};
    if (m > 106 && m % 3 != 0) { const int e = (m - 102) - (m - 102) / 3 - 4; if (e < 12) return Op{H2, 15 + e / 2, e & 1}; }
    if (m >= 108 && m % 3 == 0 && m < 153) return Op{W2, 16 + (m - 108) / 9, ((m - 108) / 3) % 3};
    if (m >= 178 && m < 187) return Op{XRES, m - 178, 0};
    return Op{NONE, 0, 0};
  }
  static constexpr int wait1(int n0, int kind, int amax) {
    int cnt = 0, need = -1;
    for (int i = -NPRO - 1; i < n0; ++i) {
      const Op o = i < -1 ? Op{XF, i + NPRO + 1, 0} : i < 0 ? Op{TOUCH, 9, 0} : at1(i);
      if (o.kind == PUB) need = -1;
      if (o.kind == NONE || o.kind > SAMPLE) continue;
      ++cnt;
      if (o.kind == kind && o.a <= amax) need = cnt;
    }
    return need < 0 || cnt - need > 62 ? 63 : cnt - need;
  }
  static constexpr int wait2(int m0, int kind, int amax) {
    int cnt = 0, need = -1;
    for (int i = -NPRE2; i < m0; ++i) {
      const Op o = i < 0 ? pre2(i + NPRE2) : at2(i);
      if (o.kind == PUB) need = -1;
      if (o.kind == NONE || o.kind > SAMPLE) continue;
      ++cnt;
      if (o.kind == kind && o.a <= amax) need = cnt;
    }
    return need < 0 || cnt - need > 62 ? 63 : cnt - need;
  }
};
using PF = Plan<false>;
static_assert(PF::eat(0, 175) < 224 - 16 && PF::eat(1, 175) < 352 - 16 && PF::eat(2, 175) < PF::NIT + 58 - 14, "a unit is published after its last store");
static_assert(PF::eat(0, 44) >= 96 + 26 && PF::eat(0, 88) >= 96 + 38 && PF::eat(0, 132) >= 96 + 50, "unit 0: accumulators complete before they are read");
static_assert(PF::eat(0, 39) < 144 && PF::eat(0, 83) < 162 && PF::eat(0, 127) < 180 && PF::eat(0, 171) < 198, "unit 0: accumulators read before they restart");
static_assert(PF::eat(1, 44) >= 216 + 38 && PF::eat(1, 88) >= 216 + 56 && PF::eat(1, 132) >= 216 + 74 && PF::eat(1, 171) < 288 + 54, "unit 1");
static_assert(PF::eat(0, 153) > 144 + 30 && PF::eat(0, 153) < 213 && PF::eat(1, 65) > 216 + 40 && PF::eat(1, 65) < 285, "early weight waits follow the last request");
static_assert(PF::eat(2, 44) >= 360 + 38 && PF::eat(2, 88) >= 360 + 56 && PF::eat(2, 132) >= 360 + 74, "unit 2");

struct Args { const char* x; const char* wqkv; const char* wo; const char* wg; const char* wu; const char* wd; char* ws; char* out; };

template <bool QKV>
DI void body(const Args& A, unsigned smem, unsigned tag, int tok0, int col0, int grp, int share, bool spare, int wave, int lane, unsigned& tch0) {
  using P = Plan<QKV>;
  constexpr int NCG = P::NCG, KS = P::KS, NSLOT = P::NSLOT, IPS = P::IPS, NIT = P::NIT, SPU = P::SPU, SB = P::SB;
  constexpr int PL1 = QKV ? 3 : 4;
  constexpr unsigned RS = QKV ? 3u * D * 2u : F * 2u, WB = QKV ? D * 3 * D * 2 : D * F * 2;
  char* ws = A.ws;
  const int bid = blockIdx.x, isU = wave >> 2, w3 = wave & 3;
  const rsrc_t sF = mk_rsrc(ws + WS_FL, FL_B), sxn = mk_rsrc(ws + WS_XN, XN_B);
  unsigned wv;
  {
    const int cgl = ((lane & 7) >> 1) ^ ((lane >> 4) & 3);
    wv = (unsigned)((16 * (w3 & 1) + 4 * (w3 >> 1) + 8 * (lane >> 5) + ((lane >> 3) & 3)) * RS + col0 * 2 + (QKV && cgl == 3 ? 2 : cgl) * 32 + (lane & 1) * 16);
  }
  const char* wA = QKV ? A.wqkv : A.wg;
  const char* wB = QKV ? A.wqkv + 96 : A.wu;
  const rsrc_t sw = mk_rsrc(isU ? wB : wA, QKV && isU ? WB - 96 : WB);
  const unsigned m0b = smem + isU * (SB / 2) + w3 * 1024;
  auto w1 = [&](auto ST, auto B, unsigned m0, rsrc_t srd) {
    constexpr int a = CV(ST), b = CV(B), su = a / SPU;
    constexpr unsigned sucol = QKV ? (su == 0 ? 1152u : su == 1 ? 2304u : 0u) : su * 128u;
    dma16<(a % NSLOT) * SB + b * 4096, 32 * (KS * (a % SPU) + b) * RS + sucol>(m0, wv, srd);
  };
  if (isU && !spare) {
    const rsrc_t sa = mk_rsrc(wA, WB);
    sfor<NSLOT>([&](auto ST) { sfor<KS>([&](auto B) { w1(ST, B, m0b - SB / 2, sa); w1(ST, B, m0b, sw); }); });
  }
  const int t16 = (tok0 >> 4) + 2 * wave;
  const int xcd = bid & 7, b2 = xcd >> 1, rt2 = (xcd & 1) * 8 + (bid >> 5), nt2 = (bid >> 3) & 3, tg = wave & 3, kh = wave >> 2;
  const int u16 = rt2 * 8 + 2 * tg, tile = rt2 * 4 + nt2;
  const rsrc_t sh = mk_rsrc(ws + WS_H, HBUF_B), sWd = mk_rsrc(A.wd, F * D * 2), sS = mk_rsrc(ws + WS_SLAB, SLAB_B);
  const unsigned m0c = spin(smem + wave * 2304), ws2 = spin((36 * b2 + kh) * (32 * 1152));
  const unsigned hb[2] = {spin(((u16 + kh) * KSTOT + kh) * 1024), spin(((u16 + 1 - kh) * KSTOT + kh) * 1024)};
  const unsigned hq = spin(36 * b2 * 1024), oso = spin((u16 + kh) * 16 * 1152 + nt2 * 288), hst = spin((t16 * KSTOT + (col0 >> 5)) * 1024);
  const unsigned guest = spin(tg ^ b2), sl = spin((tile * 8 + wave) * 4 * 5120);
  unsigned ow2[3], on2[3];
  sfor<3>([&](auto I) { const unsigned j = 6 * b2 + kh + 2 * I, g = (j * 43) >> 7; ow2[I] = spin((grp_col0(g) + 32 * (j - 3 * g)) * 1152); on2[I] = spin(j < 21 ? 0x7fffffffu : 0u); });
  const unsigned l16 = lane * 16, tab = smem + LDS_X + wave * 2048 + l16;
  const bool wdo = (bid >> 3) * 8 + wave >= 162;
  unsigned ab[NCG], xto;
  {
    const unsigned l = lane;
    u32x4 t0, t1;
    sfor<3>([&](auto I) { const unsigned n = 64 * I + l, r = n / 18; t0[(int)I] = (8 * tg + r) * 1152 + nt2 * 288 + (n - 18 * r) * 16 - 1024 * I; });
    const unsigned fa = FL_H + (((rt2 * 4 + tg) * 24 + 6 * b2) * 4 + l) * FS, fb = FL_O + ((u16 + ((l >> 1) & 1)) * 8 + 2 * b2 + (b2 < 3 ? l & 1 : 0)) * FS;
    t0[3] = pick<unsigned>(l < 24, fa, fb);
    t1[0] = smem + (kh * 32 + (l >> 2)) * 288 + (l & 3) * 8;
    t1[1] = (l & 15) * 1152 + oso + (l >> 4) * 8;
    if constexpr (QKV) { t1[2] = (((tok0 + 16) / 240) * 64 + l) * RS + col0 * 2 + (!isU && w3 < 3 ? 94 * w3 : 0x80000000u); t1[3] = 0; }
    else {
      t1[2] = pick<unsigned>(l < 18, (((tok0 >> 8) & 3) * 72 + w3 * 18 + l) * RS + col0 * 2, 0x80000000u);
      t1[3] = pick<unsigned>(l < 22, (wdo ? grp_col0(2 * b2) * 1152 - 162 * 8192 : b2 * 1152 * 1152) + ((bid >> 3) * 8 + wave) * 8192 + l * 128, 0x80000000u);
    }
    *(LDS u32x4*)tab = t0; *(LDS u32x4*)(tab + 1024) = t1;
    const int g = lane >> 4, qq = (lane >> 2) & 3, p = lane & 3, c = (2 * g + (qq >> 1)) & 3;
    if constexpr (QKV) sfor<3>([&](auto C) { ab[C] = smem + (4 * g + qq) * 128 + ((CV(C) ^ c) * 32) + 8 * p; pin(ab[C]); });
    else sfor<8>([&](auto J) { ab[J] = smem + (4 * g + qq) * 128 + (((CV(J) >> 1) ^ c) * 32) + 8 * p + (CV(J) & 1) * (SB / 2); pin(ab[J]); });
    constexpr int NSH = QKV ? 7 : 6, PER = (16 * 120 + NSH - 1) / NSH;
    const unsigned id = wave * 40 + l, ln = share * PER + id, tg16 = ln / 120;
    const int tt = (tok0 >> 4) + (int)tg16;
    const unsigned xa = (unsigned)(tt * 18432) + 3072 + (ln - tg16 * 120) * 128;
    xto = pick<unsigned>(l < 40 && id < PER && tg16 < 16 && tt >= 0, xa, 0x80000000u); pin(xto);
  }
  if (wave == 0) {
    if (spare) {
      sfor<4>([&](auto I) { unsigned f; const unsigned fo = FL_X + FS * (64 * I + lane); load4_sc1(f, fo, sF); flags_wait<0>(f, fo, tag, ~0ull, sF); });
      publish(tag, FL_EPOCH, sF);
    } else {
      const int fi = (tok0 >> 3) + (lane & 31);
      const unsigned fo = FL_X + FS * (fi < 0 ? 0 : fi > 255 ? 255 : fi);
      unsigned f; load4_sc1(f, fo, sF); flags_wait<0, 1>(f, fo, tag, ~0ull, sF);
    }
  }
  asm volatile("s_barrier" ::: "memory");
  STAMP(4);
  u32x4 xf[18][2], hf[HD][2], hp[2], t0;
  unsigned fl, sw2, cur, ooff, tch[3], tk[3][2];
  u32x2 xres[9];
  auto ldtab = [&](int i) { return *(volatile LDS unsigned*)(tab + 1024 + 4 * i); };
  auto vmem = [&](auto K, auto AA, auto BB, auto NN) {
    constexpr int kind = CV(K), a = CV(AA), b = CV(BB);
    if constexpr (kind == W1) w1(AA, BB, m0b, sw);
    else if constexpr (kind == XF) {
      const int tt = t16 + (a & 1);
      load16_sc1<(a / 2) * 1024>(xf[a / 2][a & 1], l16, (unsigned)(tt * 18432), QKV ? mk_rsrc(ws + WS_XN, tt >= 0 && tt < 128 ? XN_B : 0u) : sxn);
    } else if constexpr (kind == TOUCH) {
      if constexpr (QKV) load4(tch[0], tch[0], mk_rsrc(wA, WB));
      else if constexpr (b == 0) load4s<288 * (a % 2) * RS + (a / 2) * 128>(tch[0], tch[0], sw);
      else load4s<a * 22 * 128>(tch[1], tch[1], mk_rsrc(wdo ? A.wo : A.wd, wdo ? D * D * 2 : F * D * 2));
    } else if constexpr (kind == XRES) load8_if<32 * a>(xres[a], ooff, mk_rsrc(A.x, S * D * 2), guest);
    else if constexpr (kind == W2 || kind == H2) {
      constexpr bool isO = a >= 12 && a < 15;
      constexpr int e = a < 12 ? a : a - 3, ko = 6 * (e % 6) + 2 * (e / 6), io = isO ? a - 12 : 0;
      if constexpr (kind == W2) {
        const rsrc_t r = isO ? mk_rsrc(A.wo, on2[io] & (D * D * 2)) : sWd;
        if constexpr (b == 0) sw2 = dma_w2a<(a % 8) * SLOT2, isO ? 0 : ko * 32 * 1152>(m0c, t0[0], isO ? ow2[io] : ws2, r);
        else if constexpr (b == 1) dma_w2b(t0[1], sw2, r);
        else dma_w2c(t0[2], sw2, r);
      } else if constexpr (isO) load16_sc1<(144 + 2 * io) * 1024>(hf[a % HD][b], l16, hb[b] + 6 * 1024 * b2, mk_rsrc(ws + WS_H, on2[io] & HBUF_B));
      else load16_sc1<ko * 1024>(hf[a % HD][b], l16, hb[b] + hq, sh);
    } else if constexpr (kind == SAMPLE) load4_sc1(fl, t0[3], sF);
    else if constexpr (kind == CHECK) flags_wait<CV(NN), QKV ? 8 : 2>(fl, t0[3], tag, a == 0 ? 0x111111ull : a == 1 ? 0x222222ull : a == 2 ? 0xf000000ull : 0x444444ull, sF);
    else if constexpr (kind == PUB) publish(tag, (unsigned)(FL_H + ((((tok0 >> 5) + wave) * 24 + col0 / 192) * 4 + a) * FS), sF);
  };
  f32x4 acc[NCG][2], acc2[9][2];
  bf16x4 al[DA], ah[DA];
  auto aread = [&](auto N, auto T) {
    constexpr int n = CV(N);
    bf16x4 r;
    if constexpr (n >= NIT) r = trread(cur, ((((n - NIT) / 9) % 8) % 3) * SLOT2 + ((n - NIT) % 9) * 32 + CV(T) * 16 * 288);
    else if constexpr (QKV) r = trread(ab[P::jof(n) % 3], ((n / IPS) & 1) * SB + (P::jof(n) / 3) * (SB / 2) + (P::sof(n) % KS) * 4096 + CV(T) * 2048);
    else r = trread(n < 144 ? ab[P::jof(n)] : cur, (P::sof(n) % KS) * 4096 + CV(T) * 2048);
    if constexpr (CV(T) == 0) al[n % DA] = r; else ah[n % DA] = r;
  };
  auto rebase = [&](auto M) {
    constexpr int m = CV(M);
    if constexpr (m >= NIT) cur = v_addk<(((m - NIT) / 9) % 8) == 0 ? (unsigned)(-6 * SLOT2) : 3u * SLOT2>(cur);
    else if constexpr (QKV) ab[P::jof(m)] = v_addk<(m / IPS) % 4 == 0 ? (unsigned)(-2 * SB) : 2u * SB>(ab[P::jof(m)]);
    else if constexpr (m < 144) ab[P::jof(m)] = v_addk<SB>(ab[P::jof(m)]);
    else cur = v_addk<(unsigned)((((m / IPS) & 1) - 1) * SB + (P::jof(m) & 1) * (SB / 2))>(ab[P::jof(m) & ~1]);
  };
  float ee, eg, eh[2];
  const float k2 = __builtin_bit_cast(float, spin(__builtin_bit_cast(unsigned, LOG2E * LOG2E)));
  auto epi = [&](auto SU, auto E) {
    constexpr int e = CV(E), w = e / 11, ph = e % 11, p = w >> 2, th = (w >> 1) & 1, r = 2 * (w & 1) + (ph >= 4 && ph != 5);
    if constexpr (ph == 0 || ph == 4) { if constexpr (ph == 4) eh[0] = ee; ee = v_expn(acc[2 * p][th][r]); }
    if constexpr (ph == 1 || ph == 6) eg = v_mul(acc[2 * p][th][r], acc[2 * p + 1][th][r]);
    if constexpr (ph == 2 || ph == 7) ee = v_fmak(ee, k2);
    if constexpr (ph == 3 || ph == 8) ee = v_rcp(ee);
    if constexpr (ph == 5) eh[0] = v_mul(eg, eh[0]);
    if constexpr (ph == 9) eh[1] = v_mul(eg, ee);
    if constexpr (ph == 10) {
      hp[th][2 * (p & 1) + (w & 1)] = v_cvtpk(eh[0], eh[1]);
      if constexpr ((p & 1) && (w & 1)) store16_sc1<(p >> 1) * 1024>(hp[th], sh, l16, hst + (th * KSTOT + 2 * CV(SU)) * 1024);
    }
  };
  auto filler = [&](auto N, auto T) {
    constexpr int n = CV(N), t = CV(T);
    if constexpr (t == 1 && P::rebase(n + 4)) rebase(ic<n + 4>{});
    else if constexpr (!QKV) sfor<3>([&](auto SU) { constexpr int e = P::eidx(CV(SU), n, t); if constexpr (e >= 0) epi(SU, ic<e>{}); });
    GAP();
  };
  u32x2 pk[6][2];
  const unsigned stg = smem + wave * 6144 + lane * 8;
  auto epiq = [&](auto SU, auto HB, auto J) {
    constexpr int su = CV(SU), j = CV(J);
    if constexpr (j >= 2 && j < 8) {
      constexpr int e = j - 2, cg = 3 * CV(HB) + (e >> 1), th = e & 1;
      pin(acc[cg][th]);
      const f32x4 a = acc[cg][th];
      u32x2 t = {cvtpk(a[0], a[1]), cvtpk(a[2], a[3])};
      if constexpr (su == 0) *(LDS u32x2*)(stg + LDS_K + th * 3072 + 512 * cg) = t;
      else { pin(t); pk[cg][th] = t; }
    }
  };
  if (!QKV || !spare) {
    sfor<P::NPRO>([&](auto I) { vmem(ic<XF>{}, I, ic<0>{}, ic<0>{}); });
    load4_sc1(tch[2], xto, sxn);
    GAP(); wait_barrier<P::wait1(0, XF, 1)>(); GAP();
    pin(tch0);
    STAMP(5);
    sfor<DA - 1>([&](auto N) { aread(N, ic<0>{}); aread(N, ic<1>{}); });
    GAP();
    sfor<NIT>([&](auto N) {
      constexpr int n = CV(N), st = n / IPS, it = n % IPS, j = P::jof(n), sg = P::sof(n), su = st / SPU, c = st % SPU, m = n + DA - 1;
      constexpr bool more = m < NIT || !QKV;
      if constexpr (QKV && n == NIT - (DA - 1)) { lds_barrier(); GAP(); }
      constexpr bool early = !QKV && (st == 2 || st == 3);
      if constexpr (early && n == P::eat(st - 2, st == 2 ? 153 : 65) - 1) { wait_vm<P::wait1(n, W1, st + 1)>(); GAP(); }
      if constexpr (it == P::BAR && st < P::NST - 1) { wait_barrier<early ? 63 : P::wait1(n, W1, st + 1)>(); GAP(); }
      if constexpr (!QKV && n == NIT - (DA - 1)) { cur = ooff; wait_barrier<cmin(P::wait1(n, W2, 1), P::wait1(n, H2, 0))>(); GAP(); }
      { constexpr int pp = (it + DA - 1) % IPS; if constexpr (pp % (IPS / PL1) == 0) { __builtin_amdgcn_s_setprio(PL1 - 1 - pp / (IPS / PL1)); GAP(); } }
      if constexpr (su == 0 && j == 0 && n > 0) { wait_vm<P::wait1(n, XF, 2 * sg + 1)>(); GAP(); }
      const bf16x8 a = cat8(al[n % DA], ah[n % DA]);
      constexpr bool swp = QKV && su == 1;
      sfor<2>([&](auto T) {
        if constexpr (more) { aread(ic<m>{}, T); GAP(); }
        const bf16x8 b = as_bf(xf[sg][T]);
        if constexpr (sg == 0) mfma_new(acc[j][T], swp ? b : a, swp ? a : b); else mfma_acc(acc[j][T], swp ? b : a, swp ? a : b);
        GAP();
        filler(N, T);
      });
      if constexpr (QKV) {
        if constexpr (c == 5 && it >= IPS / 2) epiq(ic<su>{}, ic<0>{}, ic<it - IPS / 2>{});
        if constexpr (c == 0 && it < IPS / 2 && su > 0) epiq(ic<su - 1>{}, ic<1>{}, ic<it>{});
        if constexpr (st == 16 && it < 12) *(LDS u32x2*)(stg + LDS_V + (it % 2) * 3072 + 512 * (it / 2)) = pk[it / 2][it % 2];
        if constexpr (n == 6 * IPS) tch[0] = ldtab(2);
        if constexpr (n == 11 * IPS) pin(tch[0]);
        if constexpr (n == 14 * IPS) t0 = *(volatile LDS u32x4*)tab;
      } else {
        if constexpr (st >= 1 && st <= 3 && it == 24) { tch[0] = ldtab(2); tch[1] = ldtab(3); }
        if constexpr (st >= 1 && st <= 3 && it == 41) { tk[st - 1][0] = tch[0]; tk[st - 1][1] = tch[1]; }
        if constexpr (n == 200 || n == 230 || n == 354) { constexpr int q = n == 200 ? 0 : n == 230 ? 1 : 2; pin(tk[q][0]); pin(tk[q][1]); }
        if constexpr (n == 320) t0 = *(volatile LDS u32x4*)tab;
        if constexpr (n == NIT - 10) ooff = ldtab(0);
      }
      if constexpr (n == 36) pin(tch[2]);
      constexpr Op o = P::at1(n);
      if constexpr (o.kind != NONE) vmem(ic<o.kind>{}, ic<o.a>{}, ic<o.b>{}, ic<P::wait1(n, SAMPLE, 9)>{});
      GAP();
      if constexpr (n % (NIT / 9) == NIT / 9 - 1) STAMP(6 + n / (NIT / 9));
    });
    if constexpr (QKV) {
      mfma_drain();
      sfor<9>([&](auto J) { epiq(ic<2>{}, ic<1>{}, J); });
      const int i16 = lane & 15, q4 = lane >> 4, odd = lane & 1;
      const int lo = grp == 6 ? 6 : grp < 4 ? 2 * grp : 0, hlo = grp == 6 ? 1 : 0, hhi = grp < 4 || grp == 6 ? 5 : 4;
      const bool cur0 = 4 * q4 + odd <= i16, cur1 = 4 * q4 + 2 + odd <= i16;
      const unsigned s0 = odd ? 0x01000c0cu : 0x0c0c0100u, s1 = odd ? 0x03020c0cu : 0x0c0c0302u;
      const unsigned sel[4] = {cur0 ? 0x0c0c0c0cu : s0, cur1 ? 0x0c0c0c0cu : s1, cur0 ? s0 : 0x0c0c0c0cu, cur1 ? s1 : 0x0c0c0c0cu};
      constexpr float SC = 0.23570226039551584f / LOG2E;
      auto ld2 = [&](unsigned a0, unsigned a1) { const u32x2 u = *(volatile LDS u32x2*)a0, v = *(volatile LDS u32x2*)a1; return as_bf(u32x4{u[0], u[1], v[0], v[1]}); };
      const unsigned prv0 = wave == 0 ? stg : stg - 3072;
      const float cp = t16 < 0 ? -1.0e30f : 0.f;
      f32x4 sp[10], sc[10];
      float l0[10], l1[10];
      bf16x8 pb[10], kb[5][3], vb[2][6];
      sfor<5>([&](auto H) {
        kb[H][0] = ld2(prv0 + LDS_K + 512 * H, prv0 + LDS_K + 512 * H + 512); kb[H][1] = ld2(stg + LDS_K + 512 * H, stg + LDS_K + 512 * H + 512);
        kb[H][2] = ld2(stg + LDS_K + 3072 + 512 * H, stg + LDS_K + 3072 + 512 * H + 512);
      });
      GAP();
      auto pf = [&](auto I) { constexpr Op o = P::pre2(CV(I)); GAP(); vmem(ic<o.kind>{}, ic<o.a>{}, ic<o.b>{}, ic<0>{}); GAP(); };
      pf(ic<0>{});
      sfor<5>([&](auto HH) {
        constexpr int hd = CV(HH);
        const int sh0 = lo + 2 * hd;
        sfor<2>([&](auto TH) {
          const bf16x8 qb = as_bf(u32x4{4 * q4 >= sh0 ? pk[hd][TH][0] : 0u, 4 * q4 + 2 >= sh0 ? pk[hd][TH][1] : 0u,
                                        4 * q4 <= sh0 ? pk[hd + 1][TH][0] : 0u, 4 * q4 + 2 <= sh0 ? pk[hd + 1][TH][1] : 0u});
          sp[2 * hd + TH] = CV(TH) ? mfma(kb[hd][1], qb, f32x4{cp, cp, cp, cp}) : mfma(kb[hd][0], qb, f32x4{});
          sc[2 * hd + TH] = mfma(kb[hd][1 + TH], qb, f32x4{});
        });
        pf(ic<1 + hd>{});
      });
      GAP();
      sfor<6>([&](auto J) { vb[0][J] = ld2(prv0 + LDS_V + 512 * J, stg + LDS_V + 512 * J); vb[1][J] = ld2(stg + LDS_V + 512 * J, stg + LDS_V + 3072 + 512 * J); });
      GAP();
      lds_barrier();
      float mx[10];
      sfor<10>([&](auto E) {
        l0[E] = pick<float>(cur0, pick<float>(odd, sc[E][1], sc[E][0]), pick<float>(odd, sp[E][1], sp[E][0])) * SC;
        l1[E] = pick<float>(cur1, pick<float>(odd, sc[E][3], sc[E][2]), pick<float>(odd, sp[E][3], sp[E][2])) * SC;
        mx[E] = vmax(l0[E], l1[E]); pin(mx[E]); pin(l0[E]); pin(l1[E]);
        if constexpr (CV(E) % 5 == 0) pf(ic<6 + CV(E) / 5>{});
      });
      GAP();
      xq10<true>(mx);
      pf(ic<8>{});
      sfor<10>([&](auto E) { l0[E] = __builtin_amdgcn_exp2f(l0[E] - mx[E]); l1[E] = __builtin_amdgcn_exp2f(l1[E] - mx[E]); mx[E] = l0[E] + l1[E]; }); GAP();
      pf(ic<9>{});
      xq10<false>(mx);
      pf(ic<10>{});
      sfor<10>([&](auto E) {
        constexpr int hd = CV(E) >> 1;
        const float inv = __builtin_amdgcn_rcpf(mx[E]) * ((hd >= hlo && hd < hhi) ? 1.f / LOG2E : 0.f);
        const unsigned pp = cvtpk(l0[E] * inv, l1[E] * inv);
        pb[E] = as_bf(u32x4{__builtin_amdgcn_perm(pp, pp, sel[0]), __builtin_amdgcn_perm(pp, pp, sel[1]),
                            __builtin_amdgcn_perm(pp, pp, sel[2]), __builtin_amdgcn_perm(pp, pp, sel[3])});
      });
      pf(ic<11>{});
      sfor<10>([&](auto E) { sp[E] = mfma(vb[CV(E) & 1][CV(E) >> 1], pb[E], f32x4{}); sc[E] = mfma(vb[CV(E) & 1][(CV(E) >> 1) + 1], pb[E], f32x4{}); });
      GAP();
      const bool on0 = wave > 0 && t16 < 128, on1 = t16 + 1 < 128;
      sfor<2>([&](auto TH) {
        unsigned ow[12];
        sfor<12>([&](auto E) {
          constexpr int j = CV(E) >> 1, e = CV(E) & 1;
          unsigned a = 0u, b = 0u;
          if constexpr (j < 5) a = cvtpk(sp[2 * j + TH][2 * e], sp[2 * j + TH][2 * e + 1]);
          if constexpr (j > 0) b = cvtpk(sc[2 * j - 2 + TH][2 * e], sc[2 * j - 2 + TH][2 * e + 1]);
          ow[E] = 4 * q4 + 2 * e >= lo + 2 * j ? a : b;
        });
        const rsrc_t so = mk_rsrc(ws + WS_H, (CV(TH) ? on1 : on0) ? HBUF_B : 0u);
        sfor<3>([&](auto M) {
          store16_sc1<1024 * CV(M)>(u32x4{ow[4 * M], ow[4 * M + 1], ow[4 * M + 2], ow[4 * M + 3]}, so, l16, ((t16 + TH) * KSTOT + 144 + 3 * grp) * 1024);
        });
      });
      STAMP(15);
      vmem(ic<SAMPLE>{}, ic<1>{}, ic<0>{}, ic<0>{});
      publish2(tag, (unsigned)(FL_O + (t16 * 8 + grp) * FS), mk_rsrc(ws + WS_FL, on0 ? FL_B : 0u), (unsigned)(FL_O + (t16 * 8 + 8 + grp) * FS), mk_rsrc(ws + WS_FL, on1 ? FL_B : 0u));
      vmem(ic<CHECK>{}, ic<1>{}, ic<0>{}, ic<63>{});
      sfor<6>([&](auto I) { pin(hf[I][0]); pin(hf[I][1]); });
      STAMP(16);
    }
  } else {
    wait_vm<0>(); pin(tch0);
    t0 = *(volatile LDS u32x4*)tab;
    vmem(ic<SAMPLE>{}, ic<0>{}, ic<0>{}, ic<0>{}); vmem(ic<CHECK>{}, ic<0>{}, ic<0>{}, ic<0>{});
    sfor<24>([&](auto I) { constexpr Op o = P::pre2(CV(I)); vmem(ic<o.kind>{}, ic<o.a>{}, ic<o.b>{}, ic<0>{}); });
    vmem(ic<CHECK>{}, ic<1>{}, ic<0>{}, ic<0>{});
    sfor<6>([&](auto I) { pin(hf[I][0]); pin(hf[I][1]); });
  }
  if constexpr (QKV) {
    cur = ldtab(0);
    GAP(); lds_barrier(); GAP();
    sfor<DA - 1>([&](auto N) { aread(ic<NIT + CV(N)>{}, ic<0>{}); aread(ic<NIT + CV(N)>{}, ic<1>{}); });
    GAP();
    STAMP(17);
  }
  sfor<NIT2>([&](auto M) {
    constexpr int m = CV(M), n = NIT + m, st = m / 9, i = m % 9;
    if constexpr (i == 9 - (DA - 1) && st % 2 == 1) { wait_barrier<P::wait2(m, W2, st + 2)>(); GAP(); }
    { constexpr int pp = (m + DA - 1) % 18; if constexpr (pp % 6 == 0) { __builtin_amdgcn_s_setprio(2 - pp / 6); GAP(); } }
    if constexpr (i == 0 && st > 0) { wait_vm<P::wait2(m, H2, st)>(); GAP(); }
    const bf16x8 a = cat8(al[n % DA], ah[n % DA]);
    sfor<2>([&](auto T) {
      if constexpr (m + DA - 1 < NIT2) { aread(ic<n + DA - 1>{}, T); GAP(); }
      if constexpr (st == 0) mfma_new(acc2[i][T], a, as_bf(hf[st % HD][T])); else mfma_acc(acc2[i][T], a, as_bf(hf[st % HD][T]));
      GAP();
      filler(ic<n>{}, T);
    });
    if constexpr (m == 150) ooff = ldtab(1);
    constexpr Op o = P::at2(m);
    if constexpr (o.kind != NONE) { vmem(ic<o.kind>{}, ic<o.a>{}, ic<o.b>{}, ic<P::wait2(m, SAMPLE, 9)>{}); GAP(); }
    if constexpr (m % 27 == 26) STAMP(18 + m / 27);
  });
  mfma_drain();
  sfor<9>([&](auto I) { pin(acc2[I][0]); pin(acc2[I][1]); });
  lane = lane_now();
  const unsigned xr = smem + ((wave ^ 4) < 6 ? 5 * SLOT2 + (wave ^ 4) * 9216 : ((wave ^ 4) - 6) * 9216);
  unsigned xw = smem + (wave < 6 ? 5 * SLOT2 + wave * 9216 : (wave - 6) * 9216) + l16, xl = xr + l16;
  pin(xw); pin(xl);
  sfor<9>([&](auto I) { *(LDS f32x4*)(xw + I * 1024) = acc2[I][1]; });
  lds_barrier();
  sfor<9>([&](auto I) { acc2[I][0] += *(LDS f32x4*)(xl + I * 1024); });
  STAMP(26);
  if (guest) {
    auto pk2 = [&](f32x4 v, int e) { return cvtpk(v[2 * e], v[2 * e + 1]); };
    sfor<4>([&](auto I) {
      store16_sc1<1024 * CV(I)>(u32x4{pk2(acc2[2 * I][0], 0), pk2(acc2[2 * I][0], 1), pk2(acc2[2 * I + 1][0], 0), pk2(acc2[2 * I + 1][0], 1)}, sS, l16, sl + b2 * 5120);
    });
    __builtin_amdgcn_raw_buffer_store_b64(u32x2{pk2(acc2[8][0], 0), pk2(acc2[8][0], 1)}, sS, 4096 + lane * 8, sl + b2 * 5120, SC1);
    publish(tag, (unsigned)(FL_S + ((tile * 8 + wave) * 4 + b2) * FS), sF);
    STAMP(27);
    return;
  }
  const unsigned one = (lane >> 4) == ((lane >> 2) & 3) ? (lane & 1 ? 0x3f800000u : 0x3f80u) : 0u, e0 = lane & 2 ? 0u : one, e1 = lane & 2 ? one : 0u;
  const bf16x8 elo = as_bf(u32x4{e0, e1, 0u, 0u}), ehi = as_bf(u32x4{0u, 0u, e0, e1});
  unsigned fin[5][2];
  {
    const unsigned fo = FL_S + ((tile * 8 + wave) * 4 + (lane & 3)) * FS;
    unsigned f; load4_sc1(f, fo, sF);
    wait_vm<1>();
    sfor<9>([&](auto I) { pin(xres[I]); acc2[I][0] = mfma(elo, as_bf(u32x4{xres[I][0], xres[I][1], 0u, 0u}), acc2[I][0]); });
    sfor<5>([&](auto I) {
      const unsigned n = 64 * I + lane, t = n / 18, c = n - 18 * t;
      fin[I][0] = t * 304 + c * 16; fin[I][1] = n < 288 ? t * 1152 + c * 16 : 0x80000000u; pin(fin[I][0]); pin(fin[I][1]);
    });
    flags_wait<0, 1>(f, fo, tag, 0xfull & ~(1ull << b2), sF);
  }
  STAMP(28);
  u32x4 pv[3][4]; u32x2 pw[3];
  sfor<3>([&](auto J) {
    const unsigned so = sl + ((b2 + 1 + J) & 3) * 5120;
    sfor<4>([&](auto I) { pv[J][I] = __builtin_amdgcn_raw_buffer_load_b128(sS, l16 + 1024 * I, so, SC1); });
    pw[J] = __builtin_amdgcn_raw_buffer_load_b64(sS, 4096 + lane * 8, so, SC1);
  });
  GAP();
  const unsigned tw = xr + (lane & 15) * 304 + (lane >> 4) * 8;
  sfor<3>([&](auto J) {
    sfor<8>([&](auto I) { acc2[I][0] = mfma(CV(I) & 1 ? ehi : elo, as_bf(pv[J][CV(I) >> 1]), acc2[I][0]); });
    acc2[8][0] = mfma(elo, as_bf(u32x4{pw[J][0], pw[J][1], 0u, 0u}), acc2[8][0]);
  });
  sfor<9>([&](auto I) { const f32x4 v = acc2[I][0]; *(LDS u32x2*)(tw + 32 * I) = u32x2{cvtpk(v[0], v[1]), cvtpk(v[2], v[3])}; });
  asm volatile("" ::: "memory");
  const rsrc_t sO = mk_rsrc(A.out, S * D * 2);
  u32x4 ov[5];
  sfor<5>([&](auto I) { ov[I] = *(LDS u32x4*)(xr + fin[I][0]); });
  sfor<5>([&](auto I) { store16(ov[I], sO, fin[I][1], oso); });
  STAMP(29);
  if constexpr (QKV) asm volatile(".Lpsk_end_a:"); else asm volatile(".Lpsk_end_b:");
}

__global__ __launch_bounds__(512, 2) void fused(Args A) {
  __shared__ __attribute__((aligned(256))) char smem_[LDS_B];
  const unsigned smem = (unsigned)(size_t)(LDS char*)smem_;
  const int wave = rfl(threadIdx.x >> 6), lane = threadIdx.x & 63, bid = blockIdx.x, xcd = bid & 7, r = bid >> 3;
  char* ws = A.ws;
  STAMP(0);
  u32x4 v; unsigned v9, ep, tc[2];
  const rsrc_t sF = mk_rsrc(ws + WS_FL, FL_B);
  {
    const rsrc_t sx = mk_rsrc(A.x, S * D * 2);
    const unsigned xo = (unsigned)((bid * 8 + wave) * 1152);
    load16(v, xo + lane * 16, sx);
    asm volatile("buffer_load_ushort %0, %1, %2, 0 offen offset:1024" : "=v"(v9) : "v"(xo + lane * 2), "s"(sx));
    load4_sc1(ep, FL_EPOCH, sF);
  }
  asm volatile("s_barrier" ::: "memory");
  const bool qkv = r >= 24;
  const int item = xcd * 8 + r - 24, t9 = item / 7, g = item - 7 * t9;
  const int tok0 = qkv ? t9 * 240 - 16 : ((xcd & 1) * 4 + r / 6) * 256, col0 = qkv ? grp_col0(g) : ((xcd >> 1) * 6 + r % 6) * 192;
  {
    u64 pc; unsigned na, nb;
    asm volatile("s_getpc_b64 %0\n.Lpsk_begin:\n\ts_mov_b32 %1, .Lpsk_end_a-.Lpsk_begin\n\ts_mov_b32 %2, .Lpsk_end_b-.Lpsk_begin" : "=s"(pc), "=s"(na), "=s"(nb));
    const int w3 = wave & 3, q4 = (tok0 >> 8) & 3;
    unsigned vo;
    if (qkv) vo = (t9 * 64 + lane) * (3 * D * 2) + col0 * 2 + (wave < 4 ? 2304 : 1152) - 94 + 94 * w3;
    else { const unsigned id = (w3 - 1) * 64 + lane; vo = pick<unsigned>(id < 144, (q4 * 144 + id) * (F * 2), (q4 * 72 + id - 144) * (F * 2) + 128) + col0 * 2; }
    if (wave == 4) vo = ((r & 15) * 64 + lane) * 128;
    const unsigned nby = wave == 4 ? (na > nb ? na : nb) : w3 == 0 || item == 63 ? 0u : qkv ? D * 3 * D * 2 : D * F * 2;
    const char* base = wave == 4 ? (const char*)pc : qkv ? A.wqkv : wave < 4 ? A.wg : A.wu;
    load4(tc[0], vo, mk_rsrc(base, nby));
  }
  wait_vm<1>();
  pin(v); pin(v9); pin(ep);
  STAMP(1);
  const unsigned tag = rfl(ep) * 1664525u + 1013904223u;
  {
    float f[9];
    sfor<4>([&](auto E) { f[2 * E] = bflo(v[(int)E]); f[2 * E + 1] = bfhi(v[(int)E]); });
    f[8] = bflo(v9);
    const float mean = wave_sum(((f[0] + f[1]) + (f[2] + f[3])) + ((f[4] + f[5]) + (f[6] + f[7])) + f[8]) * (1.0f / D);
    float q = 0.f;
    sfor<9>([&](auto E) { const float d = f[E] - mean; q += d * d; });
    const float var = wave_sum(q) * (1.0f / D);
    const float mb = rbf(mean), rs = LOG2E * __builtin_amdgcn_rcpf(rbf(__builtin_amdgcn_sqrtf(rbf(rbf(var) + 1e-5f))));
    sfor<4>([&](auto E) { v[(int)E] = cvtpk((f[2 * E] - mb) * rs, (f[2 * E + 1] - mb) * rs); });
    *(LDS u32x4*)(smem + LDS_X + lane * 128 + wave * 16) = v;
    *(LDS unsigned short*)(smem + LDS_X + 8192 + (lane >> 3) * 128 + wave * 16 + (lane & 7) * 2) = (unsigned short)cvtpk((f[8] - mb) * rs, 0.f);
  }
  lds_barrier();
  STAMP(2);
  if (wave == 0) {
    u32x4 t[9];
    sfor<9>([&](auto I) { t[I] = *(LDS u32x4*)(smem + LDS_X + 1024 * I + lane * 16); });
    const unsigned o = (unsigned)((bid >> 1) * 18432 + (bid & 1) * 128 + (lane >> 3) * 256 + (lane & 7) * 16);
    const rsrc_t sxn = mk_rsrc(ws + WS_XN, XN_B);
    sfor<9>([&](auto I) { store16_sc1<0>(t[I], sxn, o, 2048 * I); });
    publish(tag, FL_X + FS * bid, sF);
    STAMP(3);
  }
  asm volatile("s_barrier" ::: "memory");
  if (!qkv) body<false>(A, smem, tag, tok0, col0, 0, r % 6, false, wave, lane, tc[0]);
  else body<true>(A, smem, tag, tok0, col0, g, g, item == 63, wave, lane, tc[0]);
}

}
extern "C" void kernel_launch(void* const* d_in, const int* in_sizes, int n_in, void* d_out, int out_size, void* d_ws, size_t ws_size,
                              hipStream_t stream) {
  psk::Args A;
  A.x = (const char*)d_in[0]; A.wqkv = (const char*)d_in[1]; A.wo = (const char*)d_in[2]; A.wg = (const char*)d_in[4];
  A.wu = (const char*)d_in[5]; A.wd = (const char*)d_in[6]; A.ws = (char*)d_ws; A.out = (char*)d_out;
  psk::fused<<<256, 512, 0, stream>>>(A);
}
```

```cpp
#include <hip/hip_runtime.h>
#include <utility>
#pragma clang diagnostic ignored "-Wint-to-pointer-cast"

typedef float    f32x4  __attribute__((ext_vector_type(4)));
typedef float    f32x2  __attribute__((ext_vector_type(2)));
typedef __bf16   bf16x8 __attribute__((ext_vector_type(8)));
typedef __bf16   bf16x4 __attribute__((ext_vector_type(4)));
typedef __bf16   bf16x2 __attribute__((ext_vector_type(2)));
typedef unsigned u32x4  __attribute__((ext_vector_type(4)));
typedef unsigned u32x2  __attribute__((ext_vector_type(2)));
typedef unsigned long long u64;
typedef __amdgpu_buffer_rsrc_t rsrc_t;
#define LDS __attribute__((address_space(3)))
#define DI __device__ __forceinline__
#define GAP() __builtin_amdgcn_sched_barrier(0)
#define CV(X) (decltype(X)::value)

#define XT 0
#define NPROV 6
#define LT 8
#define PB 52
#define SM 67
namespace psk {

constexpr int S = 2048, D = 576, F = 4608;
constexpr int KSTOT = 165;
constexpr unsigned HBUF_B = 128u * KSTOT * 1024u;
constexpr unsigned SLAB_B = 32u * 8u * 8u * 8192u;
constexpr unsigned XN_B = S * D * 2u;
constexpr unsigned FS = 128;
constexpr unsigned FL_EPOCH = 0, FL_X = FS, FL_H = FL_X + 256 * FS, FL_O = FL_H + 64 * 24 * 4 * FS, FL_S = FL_O + 128 * 8 * FS, FL_B = FL_S + 64 * 8 * 4 * FS;
constexpr size_t WS_H = 0, WS_SLAB = WS_H + HBUF_B, WS_XN = WS_SLAB + SLAB_B, WS_FL = WS_XN + XN_B, WS_DBG = WS_FL + FL_B;
constexpr int SC1 = 16;
constexpr unsigned SPIN_CAP = 1u << 18;

template <int N> using ic = std::integral_constant<int, N>;
template <class Fn, int... Is> DI void sfor_impl(Fn&& f, std::integer_sequence<int, Is...>) { (f(ic<Is>{}), ...); }
template <int N, class Fn> DI void sfor(Fn&& f) { sfor_impl(f, std::make_integer_sequence<int, N>{}); }

DI unsigned rfl(unsigned x) { return __builtin_amdgcn_readfirstlane(x); }
DI rsrc_t mk_rsrc(const void* p, unsigned bytes) { return __builtin_amdgcn_make_buffer_rsrc((void*)p, (short)0, (int)bytes, 0x00020000); }
DI unsigned cvtpk(float lo, float hi) { f32x2 v = {lo, hi}; return __builtin_bit_cast(unsigned, __builtin_convertvector(v, bf16x2)); }
DI float rbf(float x) { return __builtin_bit_cast(float, cvtpk(x, x) << 16); }
DI float bflo(unsigned v) { return __builtin_bit_cast(float, v << 16); }
DI float bfhi(unsigned v) { return __builtin_bit_cast(float, v & 0xffff0000u); }
DI bf16x4 trread(unsigned addr, int off) { return __builtin_amdgcn_ds_read_tr16_b64_v4bf16((LDS bf16x4*)(addr + off)); }
DI bf16x8 cat8(bf16x4 a, bf16x4 b) { return __builtin_shufflevector(a, b, 0, 1, 2, 3, 4, 5, 6, 7); }
DI bf16x8 as_bf(u32x4 v) { return __builtin_bit_cast(bf16x8, v); }
DI f32x4 mfma(bf16x8 a, bf16x8 b, f32x4 c) { return __builtin_amdgcn_mfma_f32_16x16x32_bf16(a, b, c, 0, 0, 0); }
DI void mfma_acc(f32x4& c, bf16x8 a, bf16x8 b) { asm volatile("v_mfma_f32_16x16x32_bf16 %0, %1, %2, %0" : "+v"(c) : "v"(a), "v"(b)); }
DI void mfma_new(f32x4& c, bf16x8 a, bf16x8 b) { asm volatile("v_mfma_f32_16x16x32_bf16 %0, %1, %2, 0" : "=v"(c) : "v"(a), "v"(b)); }
DI void mfma_drain() { asm volatile("s_nop 7\n\ts_nop 1"); GAP(); }
template <class T> DI void pin(T& x) { asm volatile("" : "+v"(x)); }
DI unsigned lane_now() { unsigned l; asm volatile("v_mbcnt_lo_u32_b32 %0, -1, 0\n\tv_mbcnt_hi_u32_b32 %0, -1, %0" : "=v"(l)); return l; }
DI unsigned spin(unsigned x) { asm volatile("" : "+s"(x)); return x; }
template <int N> DI void wait_vm() { if constexpr (N < 63) asm volatile("s_waitcnt vmcnt(%0)" ::"i"(N) : "memory"); }
template <int N> DI void wait_barrier() { asm volatile("s_waitcnt vmcnt(%0) lgkmcnt(0)\n\ts_barrier" ::"i"(N) : "memory"); }
DI void lds_barrier() { asm volatile("s_waitcnt lgkmcnt(0)\n\ts_barrier" ::: "memory"); }
DI float v_mul(float a, float b) { float r; asm volatile("v_mul_f32 %0, %1, %2" : "=v"(r) : "v"(a), "v"(b)); return r; }
DI float v_expn(float a) { float r; asm volatile("v_exp_f32_e64 %0, -%1" : "=v"(r) : "v"(a)); return r; }
DI float v_fmak(float a, float k) { float r; asm volatile("v_fma_f32 %0, %1, %2, %2" : "=v"(r) : "v"(a), "s"(k)); return r; }
template <unsigned K> DI unsigned v_addk(unsigned a) { unsigned r; asm volatile("v_add_u32 %0, %2, %1" : "=v"(r) : "v"(a), "i"(K)); return r; }
DI float v_rcp(float a) { float r; asm volatile("v_rcp_f32 %0, %1" : "=v"(r) : "v"(a)); return r; }
DI unsigned v_cvtpk(float a, float b) { unsigned r; asm volatile("v_cvt_pk_bf16_f32 %0, %1, %2" : "=v"(r) : "v"(a), "v"(b)); return r; }

#define STAMP(i) do {} while (0)

template <int LOFF, unsigned GOFF> DI void dma16(unsigned m0base, unsigned voff, rsrc_t srd) {
  unsigned t;
  asm volatile("s_add_u32 m0, %1, %2\n\ts_mov_b32 %0, %3\n\tbuffer_load_dwordx4 %4, %5, %0 offen lds"
               : "=&s"(t) : "s"(m0base), "i"(LOFF), "i"(GOFF), "v"(voff), "s"(srd) : "scc", "memory");
}
template <int LOFF, int GOFF> DI unsigned dma_w2a(unsigned m0base, unsigned voff, unsigned sbase, rsrc_t srd) {
  unsigned so;
  asm volatile("s_add_u32 m0, %1, %2\n\ts_add_u32 %0, %5, %6\n\tbuffer_load_dwordx4 %3, %4, %0 offen lds"
               : "=&s"(so) : "s"(m0base), "i"(LOFF), "v"(voff), "s"(srd), "s"(sbase), "i"(GOFF) : "scc", "memory");
  return so;
}
DI void dma_w2b(unsigned voff, unsigned so, rsrc_t srd) { asm volatile("buffer_load_dwordx4 %0, %1, %2 offen offset:1024 lds" ::"v"(voff), "s"(srd), "s"(so) : "memory"); }
DI void dma_w2c(unsigned voff, unsigned so, rsrc_t srd) {
  asm volatile("s_mov_b64 exec, 0xffff\n\tbuffer_load_dwordx4 %0, %1, %2 offen offset:2048 lds\n\ts_mov_b64 exec, -1" ::"v"(voff), "s"(srd), "s"(so) : "memory");
}
template <int GOFF> DI void load16_sc1(u32x4& d, unsigned voff, unsigned sbase, rsrc_t srd) {
  unsigned t;
  asm volatile("s_add_u32 %1, %3, %4\n\tbuffer_load_dwordx4 %0, %2, %5, %1 offen sc1" : "=v"(d), "=&s"(t) : "v"(voff), "s"(sbase), "i"(GOFF), "s"(srd) : "scc");
}
template <int OFF> DI void store16_sc1(u32x4 d, rsrc_t srd, unsigned voff, unsigned soff) {
  asm volatile("buffer_store_dwordx4 %0, %1, %2, %3 offen offset:%4 sc1\n\ts_nop 1" ::"v"(d), "v"(voff), "s"(srd), "s"(soff), "i"(OFF) : "memory");
}
DI void store16(u32x4 d, rsrc_t srd, unsigned voff, unsigned soff) {
  asm volatile("buffer_store_dwordx4 %0, %1, %2, %3 offen sc1\n\ts_nop 1" ::"v"(d), "v"(voff), "s"(srd), "s"(soff) : "memory");
}
DI void load16(u32x4& d, unsigned voff, rsrc_t srd) { asm volatile("buffer_load_dwordx4 %0, %1, %2, 0 offen" : "=v"(d) : "v"(voff), "s"(srd)); }
DI void load4(unsigned& d, unsigned voff, rsrc_t srd) { asm volatile("buffer_load_dword %0, %1, %2, 0 offen" : "=v"(d) : "v"(voff), "s"(srd)); }
template <unsigned SOFF> DI void load4s(unsigned& d, unsigned voff, rsrc_t srd) {
  unsigned t;
  asm volatile("s_mov_b32 %1, %4\n\tbuffer_load_dword %0, %2, %3, %1 offen" : "=v"(d), "=&s"(t) : "v"(voff), "s"(srd), "i"(SOFF));
}
DI void load4_sc1(unsigned& d, unsigned voff, rsrc_t srd) { asm volatile("buffer_load_dword %0, %1, %2, 0 offen sc1" : "=v"(d) : "v"(voff), "s"(srd)); }
template <int N, int SLEEP = 2> DI void flags_wait(unsigned& f, unsigned voff, unsigned tag, u64 mask, rsrc_t srd) {
  unsigned cnt;
  asm volatile("s_waitcnt vmcnt(%6)\n\ts_mov_b32 %1, %7\n"
               "1:\n\tv_cmp_ne_u32 vcc, %3, %0\n\ts_nop 1\n\ts_and_b64 vcc, vcc, %4\n\ts_cbranch_scc0 2f\n\t"
               "s_sleep %8\n\tbuffer_load_dword %0, %2, %5, 0 offen sc1\n\ts_waitcnt vmcnt(0)\n\ts_sub_u32 %1, %1, 1\n\ts_cbranch_scc0 1b\n"
               "2:"
               : "+v"(f), "=&s"(cnt) : "v"(voff), "s"(tag), "s"(mask), "s"(srd), "i"(N), "i"(SPIN_CAP), "i"(SLEEP) : "vcc", "scc", "memory");
}
DI void publish(unsigned tag, unsigned soff, rsrc_t srd) {
  unsigned t;
  asm volatile("v_mov_b32 %0, %1\n\ts_waitcnt vmcnt(0)\n\ts_mov_b64 exec, 1\n\tbuffer_store_dword %0, off, %3, %2 sc1\n\ts_mov_b64 exec, -1"
               : "=&v"(t) : "s"(tag), "s"(soff), "s"(srd) : "memory");
}

DI void gather7(u32x4 (&p)[7], u32x4 (&q)[7], unsigned& f, unsigned fo, unsigned v0, unsigned v1, unsigned tag, unsigned src0, unsigned s0, rsrc_t sp, rsrc_t sq, rsrc_t sf) {
  unsigned pend, rdy, t, cnt;
  asm volatile("s_mov_b32 %15, 0x7f\n\ts_mov_b32 %18, %28\n"
               "1:\n\tv_cmp_eq_u32 vcc, %22, %14\n\ts_nop 1\n\ts_and_b32 %16, vcc_lo, %15\n\ts_andn2_b32 %15, %15, %16\n\t"
               "s_bitcmp1_b32 %16, 0\n\ts_cbranch_scc0 3f\n\ts_sub_u32 %17, %23, 0\n\ts_and_b32 %17, %17, 7\n\ts_lshl_b32 %17, %17, 16\n\ts_add_u32 %17, %17, %24\n\t"
               "buffer_load_dwordx4 %0, %20, %25, %17 offen offset:0 sc1\n\tbuffer_load_dwordx4 %7, %21, %26, %17 offen offset:3072 sc1\n3:\n\t"
               "s_bitcmp1_b32 %16, 1\n\ts_cbranch_scc0 3f\n\ts_sub_u32 %17, %23, 1\n\ts_and_b32 %17, %17, 7\n\ts_lshl_b32 %17, %17, 16\n\ts_add_u32 %17, %17, %24\n\t"
               "buffer_load_dwordx4 %1, %20, %25, %17 offen offset:1024 sc1\n\tbuffer_load_dwordx4 %8, %21, %26, %17 offen offset:3072 sc1\n3:\n\t"
               "s_bitcmp1_b32 %16, 2\n\ts_cbranch_scc0 3f\n\ts_sub_u32 %17, %23, 2\n\ts_and_b32 %17, %17, 7\n\ts_lshl_b32 %17, %17, 16\n\ts_add_u32 %17, %17, %24\n\t"
               "buffer_load_dwordx4 %2, %20, %25, %17 offen offset:2048 sc1\n\tbuffer_load_dwordx4 %9, %21, %26, %17 offen offset:3072 sc1\n3:\n\t"
               "s_bitcmp1_b32 %16, 3\n\ts_cbranch_scc0 3f\n\ts_sub_u32 %17, %23, 3\n\ts_and_b32 %17, %17, 7\n\ts_lshl_b32 %17, %17, 16\n\ts_add_u32 %17, %17, %24\n\t"
               "buffer_load_dwordx4 %3, %20, %25, %17 offen offset:3072 sc1\n\tbuffer_load_dwordx4 %10, %21, %26, %17 offen offset:3072 sc1\n3:\n\t"
               "s_bitcmp1_b32 %16, 4\n\ts_cbranch_scc0 3f\n\ts_sub_u32 %17, %23, 4\n\ts_and_b32 %17, %17, 7\n\ts_lshl_b32 %17, %17, 16\n\ts_add_u32 %17, %17, %24\n\t"
               "buffer_load_dwordx4 %4, %21, %25, %17 offen offset:0 sc1\n\tbuffer_load_dwordx4 %11, %21, %26, %17 offen offset:3072 sc1\n3:\n\t"
               "s_bitcmp1_b32 %16, 5\n\ts_cbranch_scc0 3f\n\ts_sub_u32 %17, %23, 5\n\ts_and_b32 %17, %17, 7\n\ts_lshl_b32 %17, %17, 16\n\ts_add_u32 %17, %17, %24\n\t"
               "buffer_load_dwordx4 %5, %21, %25, %17 offen offset:1024 sc1\n\tbuffer_load_dwordx4 %12, %21, %26, %17 offen offset:3072 sc1\n3:\n\t"
               "s_bitcmp1_b32 %16, 6\n\ts_cbranch_scc0 3f\n\ts_sub_u32 %17, %23, 6\n\ts_and_b32 %17, %17, 7\n\ts_lshl_b32 %17, %17, 16\n\ts_add_u32 %17, %17, %24\n\t"
               "buffer_load_dwordx4 %6, %21, %25, %17 offen offset:2048 sc1\n\tbuffer_load_dwordx4 %13, %21, %26, %17 offen offset:3072 sc1\n3:\n\t"
               "s_cmp_eq_u32 %15, 0\n\ts_cbranch_scc1 2f\n\ts_sleep 1\n\tbuffer_load_dword %14, %19, %27, 0 offen sc1\n\ts_waitcnt vmcnt(0)\n\t"
               "s_sub_u32 %18, %18, 1\n\ts_cbranch_scc0 1b\n2:\n\ts_waitcnt vmcnt(0)"
               : "=&v"(p[0]), "=&v"(p[1]), "=&v"(p[2]), "=&v"(p[3]), "=&v"(p[4]), "=&v"(p[5]), "=&v"(p[6]), "=&v"(q[0]), "=&v"(q[1]), "=&v"(q[2]), "=&v"(q[3]), "=&v"(q[4]), "=&v"(q[5]), "=&v"(q[6]), "+v"(f), "=&s"(pend), "=&s"(rdy), "=&s"(t), "=&s"(cnt)
               : "v"(fo), "v"(v0), "v"(v1), "s"(tag), "s"(src0), "s"(s0), "s"(sp), "s"(sq), "s"(sf), "i"(SPIN_CAP) : "vcc", "scc", "memory");
}
DI void publish2(unsigned tag, unsigned s0, rsrc_t r0, unsigned s1, rsrc_t r1) {
  unsigned t;
  asm volatile("v_mov_b32 %0, %1\n\ts_waitcnt vmcnt(0)\n\ts_mov_b64 exec, 1\n\tbuffer_store_dword %0, off, %3, %2 sc1\n\tbuffer_store_dword %0, off, %5, %4 sc1\n\ts_mov_b64 exec, -1"
               : "=&v"(t) : "s"(tag), "s"(s0), "s"(r0), "s"(s1), "s"(r1) : "memory");
}
DI void load8_if(u32x2& d, unsigned voff, unsigned soff, rsrc_t srd, unsigned off) {
  asm volatile("s_cmp_lg_u32 %4, 0\n\ts_cbranch_scc1 1f\n\tbuffer_load_dwordx2 %0, %1, %2, %3 offen\n1:" : "=v"(d) : "v"(voff), "s"(srd), "s"(soff), "s"(off) : "scc");
}

template <class T> DI T pick(bool c, T a, T b) { return c ? a : b; }
DI unsigned vand(unsigned a, unsigned b) { return a & b; }
DI float fma4(float a, float b, float c, float d, const float (&k)[4]) {
  float r = a * k[0]; pin(r); r = __builtin_fmaf(b, k[1], r); r = __builtin_fmaf(c, k[2], r); return __builtin_fmaf(d, k[3], r);
}
DI float vmax(float a, float b) { float r; asm("v_max_f32 %0, %1, %2" : "=v"(r) : "v"(a), "v"(b)); return r; }
template <bool MAX> DI float x16(float v) {
  const unsigned u = __builtin_bit_cast(unsigned, v);
  auto a = __builtin_amdgcn_permlane16_swap(u, u, false, false);
  const float p = __builtin_bit_cast(float, (unsigned)a[0]), q = __builtin_bit_cast(float, (unsigned)a[1]);
  return MAX ? vmax(p, q) : p + q;
}
template <bool MAX> DI float x32(float v) {
  const unsigned u = __builtin_bit_cast(unsigned, v);
  auto a = __builtin_amdgcn_permlane32_swap(u, u, false, false);
  const float p = __builtin_bit_cast(float, (unsigned)a[0]), q = __builtin_bit_cast(float, (unsigned)a[1]);
  return MAX ? vmax(p, q) : p + q;
}
template <int W> DI void swp(float& a, float& b) {
  const unsigned x = __builtin_bit_cast(unsigned, a), y = __builtin_bit_cast(unsigned, b);
  if constexpr (W == 16) { auto r = __builtin_amdgcn_permlane16_swap(x, y, false, false); a = __builtin_bit_cast(float, (unsigned)r[0]); b = __builtin_bit_cast(float, (unsigned)r[1]); }
  else { auto r = __builtin_amdgcn_permlane32_swap(x, y, false, false); a = __builtin_bit_cast(float, (unsigned)r[0]); b = __builtin_bit_cast(float, (unsigned)r[1]); }
}
template <bool MAX> DI void xq10(float (&m)[10]) {
  auto op = [](float p, float q) { return MAX ? vmax(p, q) : p + q; };
  float a[6], c[3], d[6];
  sfor<5>([&](auto I) { swp<16>(m[2 * I], m[2 * I + 1]); }); GAP();
  sfor<5>([&](auto I) { a[I] = op(m[2 * I], m[2 * I + 1]); pin(a[I]); }); a[5] = a[4]; pin(a[5]); GAP();
  sfor<3>([&](auto I) { swp<32>(a[2 * I], a[2 * I + 1]); }); GAP();
  sfor<3>([&](auto I) { c[I] = op(a[2 * I], a[2 * I + 1]); if constexpr (!MAX) c[I] = __builtin_amdgcn_rcpf(c[I]); pin(c[I]); }); GAP();
  sfor<2>([&](auto I) { d[2 * I] = c[I]; d[2 * I + 1] = c[I]; pin(d[2 * I + 1]); }); d[4] = c[2]; GAP();
  sfor<2>([&](auto I) { swp<32>(d[2 * I], d[2 * I + 1]); }); GAP();
  sfor<5>([&](auto I) { m[2 * I] = d[I]; m[2 * I + 1] = d[I]; pin(m[2 * I + 1]); }); GAP();
  sfor<5>([&](auto I) { swp<16>(m[2 * I], m[2 * I + 1]); }); GAP();
}
template <int CTRL> DI float dpp_add(float v) { return v + __builtin_bit_cast(float, __builtin_amdgcn_update_dpp(0, __builtin_bit_cast(int, v), CTRL, 0xf, 0xf, true)); }
DI float wave_sum(float v) { return x32<false>(x16<false>(dpp_add<0x140>(dpp_add<0x141>(dpp_add<0x4e>(dpp_add<0xb1>(v)))))); }
__host__ __device__ constexpr int grp_col0(int g) { return 8 * (int)((0x3C362D21160B00ull >> (8 * g)) & 0xff); }

constexpr int LDS_V = 2 * 24576, LDS_K = 4 * 24576;
constexpr int LDS_X = 6 * 24576, LDS_B = LDS_X + 16384;
constexpr int SLOT2 = 64 * 288, NIT2 = 21 * 9, DA = 4;
constexpr float LOG2E = 1.4426950408889634f;
constexpr int cmin(int a, int b) { return a < b ? a : b; }

enum { NONE, W1, XF, TOUCH, W2, H2, SAMPLE,   CHECK, PUB, XRES };
struct Op { int kind, a, b; };
template <bool QKV> struct Plan {
  static constexpr int NCG = QKV ? 6 : 8, KS = QKV ? 3 : 9, NST = 54 / KS, SPU = 18 / KS, NSLOT = QKV ? 4 : 2;
  static constexpr int SB = KS * 8192, IPS = KS * NCG, NIT = NST * IPS, BAR = IPS - (DA - 1), NPRO = NPROV, NPRE2 = 24;
  static constexpr bool kmajor(int n) { return QKV ? ((n / IPS) % 6 != 5 && (n < 6 * IPS || (n / IPS) % 6 != 0)) : n < 96; }
  static constexpr int jof(int n) { return kmajor(n) ? n % NCG : (!QKV && n < 144) ? (n - 96) / 6 : (n % IPS) / KS; }
  static constexpr int sof(int n) { return kmajor(n) ? (n % (18 * NCG)) / NCG : (!QKV && n < 144) ? 12 + n % 6 : KS * ((n / IPS) % SPU) + n % KS; }
  static constexpr bool rebase(int m) {
    if (m >= NIT) return m - NIT == 50 || m - NIT == 51;
    if (QKV) return m >= IPS && (m / IPS) % 2 == 0 && (kmajor(m) ? m % IPS < 3 : (m % IPS) % 3 == 0 && m % IPS < 9);
    return m < 144 ? (m >= 72 && m < 80) : m % 9 == 0;
  }
  static constexpr int TM = 174 - 8 * LT;
  static constexpr int st2(int m) { return m < 54 ? m / 9 : m < TM ? 6 + (m - 54) / 8 : m < 174 ? 21 - LT + (m - TM) % LT : 6 + m - 174; }
  static constexpr int ti2(int m) { return m < 54 ? m % 9 : m < 174 ? ((m < TM ? (m - 54) % 8 : (m - TM) / LT) + 8) % 9 : 7; }
  static constexpr bool first2(int m) { return m < 54 ? m % 9 == 0 : m < TM ? (m - 54) % 8 == 0 : m < TM + LT; }
  static constexpr int lds2(int k) { return (k < 12 ? k : k < 18 ? k - 12 : k - 6) * (SLOT2 / 2); }
  static constexpr int bar2(int m) { return m == 67 ? 7 : m == 99 ? 10 : -1; }
  static constexpr int estart(int su) { return su == 0 ? 108 : 144 * su + 92; }
  static constexpr int eidx(int su, int n, int t) {
    if (n < estart(su) || n > estart(su) + 110 || (t == 1 && rebase(n + 4))) return -1;
    int c = 0;
    for (int i = estart(su); i < n; ++i) c += rebase(i + 4) ? 1 : 2;
    return c + t < 176 ? c + t : -1;
  }
  static constexpr int eat(int su, int e) { for (int n = estart(su);; ++n) if (eidx(su, n, 0) == e || eidx(su, n, 1) == e) return n; }
  static constexpr Op pre2(int i) { return i < 12 ? Op{W2, i / 3, i % 3} : Op{H2, (i - 12) / 2, i & 1}; }
  static constexpr Op at1(int n) {
    const int st = n / IPS, it = n % IPS;
    if (QKV) {
      if (n % 2 == 0 && NPRO + n / 2 < 36) return Op{XF, NPRO + n / 2, 0};
      if (st > 0 && it < 6 && it % 2 == 1 && st + 3 < NST) return Op{W1, st + 3, it / 2};
      if (st == 6 && it == 9) return Op{TOUCH, 0, 0};
      if (st == 14 && it == 6) return Op{SAMPLE, 0, 0};
      if (st == 15 && it == 12) return Op{CHECK, 0, 0};
      if (n > 15 * IPS + 12 && n % 3 == 1 && (n - 15 * IPS - 13) / 3 < 12) return pre2(12 + (n - 15 * IPS - 13) / 3);
      return Op{NONE, 0, 0};
    }
    if (n % 3 == 0 && NPRO + n / 3 < 36) return Op{XF, NPRO + n / 3, 0};
    if (st >= 1 && st <= 4) { const int i0 = st == 3 ? 10 : 1; if (it >= i0 && (it - i0) % 3 == 0 && (it - i0) / 3 < 9) return Op{W1, st + 1, (it - i0) / 3}; }
    if (n == 224) return Op{PUB, 0, 0};
    if (st >= 1 && st <= 3 && it == (st == 3 ? 37 : 31)) return Op{TOUCH, st + 2, 0};
    if (st >= 1 && st <= 3 && it == (st == 3 ? 39 : 33)) return Op{TOUCH, st - 1, 1};
    if (n == 330) return Op{SAMPLE, 0, 0};
    if (n == 352) return Op{PUB, 1, 0};
    if (n == 353) return Op{CHECK, 0, 0};
    if (n == 406) return Op{SAMPLE, 1, 0};
    if (n == 431) return Op{CHECK, 1, 0};
    if (n >= 358 && n % 5 < 4 && n % 5 != 1 && n % 5 != 2) { const int e = (n - 358) / 5 * 2 + ((n - 358) % 5 ? 1 : 0); if (e < 24) return pre2(e); }
    return Op{NONE, 0, 0};
  }
  static constexpr Op at2(int m) {
    if (QKV) {
      if (m == 12) return Op{PUB, 3, 0};
      if (m == 13) return Op{CHECK, 1, 0};
      if (m >= 14 && m < 56 && m % 7 != 2 && m % 7 != 4 && m % 7 != 6) {
        const int e = (m - 14) / 7 * 2 + (m % 7 > 1); return (m % 7 == 0 || m % 7 == 3) ? Op{H2, 6 + e / 2, e & 1} : Op{W2, 4 + e / 3, e % 3};
      }
    } else if (m < 42 && m % 7 != 2 && m % 7 != 4 && m % 7 != 6) {
      const int e = m / 7 * 2 + (m % 7 > 1); return (m % 7 == 0 || m % 7 == 3) ? Op{H2, 6 + e / 2, e & 1} : Op{W2, 4 + e / 3, e % 3};
    }
    if (!QKV && m == PB) return Op{PUB, 2, 0};
    if (!QKV && m == PB + 1) return Op{TOUCH, 0, 2};
    if (m == SM) return Op{SAMPLE, 2, 0};
    if (m >= 68 && m < 86 && m % 2 == 0) return Op{W2, 8 + (m - 68) / 6, ((m - 68) / 2) % 3};
    if (m == SM + 20) return Op{CHECK, 2, 0};
    if (m > SM + 20 && m <= SM + 38) return Op{H2, 12 + (m - SM - 21) / 2, (m - SM - 21) & 1};
    if (m >= 140 && m < 148 && m % 2 == 0) return Op{XRES, (m - 140) / 2, 0};
    return Op{NONE, 0, 0};
  }
  static constexpr int wait1(int n0, int kind, int amax) {
    int cnt = 0, need = -1;
    for (int i = -NPRO - 1; i < n0; ++i) {
      const Op o = i < -1 ? Op{XF, i + NPRO + 1, 0} : i < 0 ? Op{TOUCH, 9, 0} : at1(i);
      if (o.kind == PUB) need = -1;
      if (o.kind == NONE || o.kind > SAMPLE) continue;
      ++cnt;
      if (o.kind == kind && o.a <= amax) need = cnt;
    }
    return need < 0 || cnt - need > 62 ? 63 : cnt - need;
  }
  static constexpr int wait2(int m0, int kind, int amax) {
    int cnt = 0, need = -1;
    for (int i = -NPRE2; i < m0; ++i) {
      const Op o = i < 0 ? pre2(i + NPRE2) : at2(i);
      if (o.kind == PUB) need = -1;
      if (o.kind == NONE || o.kind > SAMPLE) continue;
      ++cnt;
      if (o.kind == kind && o.a <= amax) need = cnt;
    }
    return need < 0 || cnt - need > 62 ? 63 : cnt - need;
  }
};
using PF = Plan<false>;
static_assert(PF::eat(0, 175) < 224 - 16 && PF::eat(1, 175) < 352 - 16 && PF::eat(2, 175) < PF::NIT + PB - 8, "a unit is published after its last store");
static_assert(PF::eat(0, 44) >= 96 + 26 && PF::eat(0, 88) >= 96 + 38 && PF::eat(0, 132) >= 96 + 50, "unit 0: accumulators complete before they are read");
static_assert(PF::eat(0, 39) < 144 && PF::eat(0, 83) < 162 && PF::eat(0, 127) < 180 && PF::eat(0, 171) < 198, "unit 0: accumulators read before they restart");
static_assert(PF::eat(1, 44) >= 216 + 38 && PF::eat(1, 88) >= 216 + 56 && PF::eat(1, 132) >= 216 + 74 && PF::eat(1, 171) < 288 + 54, "unit 1");
static_assert(PF::eat(0, 153) > 144 + 30 && PF::eat(0, 153) < 213 && PF::eat(1, 65) > 216 + 40 && PF::eat(1, 65) < 285, "early weight waits follow the last request");
static_assert(PF::eat(2, 44) >= 360 + 38 && PF::eat(2, 88) >= 360 + 56 && PF::eat(2, 132) >= 360 + 74, "unit 2");

struct Args { const char* x; const char* wqkv; const char* wo; const char* wg; const char* wu; const char* wd; char* ws; char* out; };

template <bool QKV>
DI void body(const Args& A, unsigned smem, unsigned tag, int tok0, int col0, int grp, int share, bool spare, int wave, int lane, unsigned& tch0) {
  using P = Plan<QKV>;
  constexpr int NCG = P::NCG, KS = P::KS, NSLOT = P::NSLOT, IPS = P::IPS, NIT = P::NIT, SPU = P::SPU, SB = P::SB;
  constexpr int PL1 = QKV ? 3 : 4;
  constexpr unsigned RS = QKV ? 3u * D * 2u : F * 2u, WB = QKV ? D * 3 * D * 2 : D * F * 2;
  char* ws = A.ws;
  const int bid = blockIdx.x, isU = wave >> 2, w3 = wave & 3;
  const rsrc_t sF = mk_rsrc(ws + WS_FL, FL_B), sxn = mk_rsrc(ws + WS_XN, XN_B);
  unsigned wv;
  {
    const int cgl = ((lane & 7) >> 1) ^ ((lane >> 4) & 3);
    wv = (unsigned)((16 * (w3 & 1) + 4 * (w3 >> 1) + 8 * (lane >> 5) + ((lane >> 3) & 3)) * RS + col0 * 2 + (QKV && cgl == 3 ? 2 : cgl) * 32 + (lane & 1) * 16);
  }
  const char* wA = QKV ? A.wqkv : A.wg;
  const char* wB = QKV ? A.wqkv + 96 : A.wu;
  const rsrc_t sw = mk_rsrc(isU ? wB : wA, QKV && isU ? WB - 96 : WB);
  const unsigned m0b = smem + isU * (SB / 2) + w3 * 1024;
  auto w1 = [&](auto ST, auto B, unsigned m0, rsrc_t srd) {
    constexpr int a = CV(ST), b = CV(B), su = a / SPU;
    constexpr unsigned sucol = QKV ? (su == 0 ? 1152u : su == 1 ? 2304u : 0u) : su * 128u;
    dma16<(a % NSLOT) * SB + b * 4096, 32 * (KS * (a % SPU) + b) * RS + sucol>(m0, wv, srd);
  };
  if (isU && !spare) {
    const rsrc_t sa = mk_rsrc(wA, WB);
    sfor<NSLOT>([&](auto ST) { sfor<KS>([&](auto B) { w1(ST, B, m0b - SB / 2, sa); w1(ST, B, m0b, sw); }); });
  }
  const int t16 = (tok0 >> 4) + 2 * wave;
  const int b2 = bid & 7, tile = bid >> 3, rt2 = QKV ? tile - 24 : tok0 >> 8, nt2 = QKV ? 3 : tile - 3 * rt2, u16 = rt2 * 16 + 2 * wave;
  const rsrc_t sh = mk_rsrc(ws + WS_H, HBUF_B), sWd = mk_rsrc(A.wd, F * D * 2), sS = mk_rsrc(ws + WS_SLAB, SLAB_B);
  const unsigned m0c = spin(smem + wave * 2304), ws2 = spin(576 * b2 * 1152);
  const unsigned hb[2] = {spin((u16 * KSTOT + 18 * b2) * 1024), spin(((u16 + 1) * KSTOT + 18 * b2) * 1024)};
  const unsigned ho[2] = {spin(hb[0] - 15 * 1024 * b2), spin(hb[1] - 15 * 1024 * b2)};
  const unsigned xs[2] = {spin(t16 * 18432), spin(t16 * 18432 + 18432)}, xz[2] = {spin(t16 >= 0 && t16 < 128 ? XN_B : 0u), spin(t16 + 1 < 128 ? XN_B : 0u)};
  const unsigned oso = spin(u16 * 16 * 1152 + nt2 * 288), hst = spin((t16 * KSTOT + (col0 >> 5)) * 1024);
  const unsigned guest = spin(wave ^ b2), sl = spin(((tile * 8 + b2) * 8 + wave) * 8192);
  const unsigned ow2 = spin(grp_col0(b2) * 1152), onh = spin(b2 < 7 ? HBUF_B : 0u);
  const unsigned onw[2] = {spin(b2 < 7 ? D * D * 2 : 0), spin(b2 < 7 && wave < 4 ? D * D * 2 : 0)};
  const unsigned om = spin(b2 < 7 ? 0x3444 : 0x444);
  const unsigned fq[2] = {spin(FL_O + (t16 * 8 + grp) * FS), spin(FL_O + (t16 * 8 + 8 + grp) * FS)};
  const unsigned fn[2] = {spin(wave > 0 && t16 < 128 ? FL_B : 0u), spin(t16 + 1 < 128 ? FL_B : 0u)};
  const unsigned l16 = lane * 16, tab = smem + LDS_X + wave * 2048 + l16;
  const bool wdo = (bid >> 3) * 8 + wave >= 162;
  unsigned ab[NCG], xto;
  {
    const unsigned l = lane;
    u32x4 t0, t1;
    sfor<3>([&](auto I) { const unsigned n = 64 * I + l, r = n / 18, c = n - 18 * r;
      t0[(int)I] = (8 * wave + r) * 1152 + nt2 * 288 + pick<unsigned>(c < 16, (c + 2 * b2 + 2) & 15, c) * 16 - 1024 * I; });
    const unsigned fa = FL_H + (((rt2 * 8 + wave) * 24 + 3 * b2) * 4 + l) * FS, fb = FL_O + ((u16 + (l & 1)) * 8 + b2) * FS;
    t0[3] = pick<unsigned>(l < 12, fa, fb);
    t1[0] = smem + (l >> 2) * 288 + (l & 3) * 8;
    t1[1] = (l & 15) * 1152 + (l >> 4) * 8;
    if constexpr (QKV) { t1[2] = (((tok0 + 16) / 240) * 64 + l) * RS + col0 * 2 + (!isU && w3 < 3 ? 94 * w3 : 0x80000000u); t1[3] = 0; }
    else {
      t1[2] = pick<unsigned>(l < 9, ((tok0 >> 8) * 36 + w3 * 9 + l) * RS + col0 * 2, 0x80000000u);
      t1[3] = pick<unsigned>(l < 11, (wdo ? ow2 - 162 * 4096 : ws2) + ((bid >> 3) * 8 + wave) * 4096 + l * 128, 0x80000000u);
    }
    *(LDS u32x4*)tab = t0; *(LDS u32x4*)(tab + 1024) = t1;
    const int g = lane >> 4, qq = (lane >> 2) & 3, p = lane & 3, c = (2 * g + (qq >> 1)) & 3;
    if constexpr (QKV) sfor<3>([&](auto C) { ab[C] = smem + (4 * g + qq) * 128 + ((CV(C) ^ c) * 32) + 8 * p; pin(ab[C]); });
    else sfor<8>([&](auto J) { ab[J] = smem + (4 * g + qq) * 128 + (((CV(J) >> 1) ^ c) * 32) + 8 * p + (CV(J) & 1) * (SB / 2); pin(ab[J]); });
    const unsigned ln = share * 512 + wave * 64 + l, tg16 = ln / 96;
    xto = QKV ? 0x80000000u : ((tok0 >> 4) + tg16) * 18432 + 3072 + (ln - tg16 * 96) * 128; pin(xto);
  }
  if (wave == 0) {
    if (spare) {
      sfor<4>([&](auto I) { unsigned f; const unsigned fo = FL_X + FS * (64 * I + lane); load4_sc1(f, fo, sF); flags_wait<0>(f, fo, tag, ~0ull, sF); });
      publish(tag, FL_EPOCH, sF);
    } else {
      const int fi = (tok0 >> 3) + (lane & 31);
      const unsigned fo = FL_X + FS * (fi < 0 ? 0 : fi > 255 ? 255 : fi);
      unsigned f; load4_sc1(f, fo, sF); flags_wait<0, 1>(f, fo, tag, ~0ull, sF);
    }
  }
  asm volatile("s_barrier" ::: "memory");
  STAMP(4);
  u32x4 xf[18][2], hf[21][2], hp[2], gp, t0;
  unsigned fl, sw2, cur, ooff, tch[3], tk[3][2];
  u32x2 xres[4];
  unsigned cb[3];
  auto ldtab = [&](int i) { return *(volatile LDS unsigned*)(tab + 1024 + 4 * i); };
  auto vmem = [&](auto K, auto AA, auto BB, auto NN) {
    constexpr int kind = CV(K), a = CV(AA), b = CV(BB);
    if constexpr (kind == W1) w1(AA, BB, m0b, sw);
    else if constexpr (kind == XF) {
      load16_sc1<(a / 2) * 1024>(xf[a / 2][a & 1], l16, xs[a & 1], QKV ? mk_rsrc(ws + WS_XN, xz[a & 1]) : sxn);
    } else if constexpr (kind == TOUCH) {
      if constexpr (b == 2) load4_sc1(tch[0], (l16 & 0xf0) * 8 + (l16 & 0x100) * (KSTOT * 4) + (l16 >> 9) * 0x80000000u + hst + 4096, sh);
      else if constexpr (QKV) load4(tch[0], tch[0], mk_rsrc(wA, WB));
      else if constexpr (b == 0) load4s<288 * (a % 2) * RS + (a / 2) * 128>(tch[0], tch[0], sw);
      else load4s<a * 11 * 128>(tch[1], tch[1], mk_rsrc(wdo ? A.wo : A.wd, wdo ? D * D * 2 : F * D * 2));
    } else if constexpr (kind == XRES) load8_if(xres[a], ooff, oso + (a & 1) * 16 * 1152 + (a < 2 ? 32 * b2 : 256), mk_rsrc(A.x, S * D * 2), a < 2 ? 0u : guest);
    else if constexpr (kind == W2) {
      constexpr bool isO = a == 6 || a == 7;
      constexpr int e = a < 6 ? a : a - 2, row = isO ? 64 * (a - 6) : 192 * (e % 3) + 64 * (e / 3);
      const rsrc_t r = isO ? mk_rsrc(A.wo, onw[a & 1]) : sWd;
      if constexpr (b == 0) sw2 = dma_w2a<(a % 8) * SLOT2, row * 1152>(m0c, t0[0], isO ? ow2 : ws2, r);
      else if constexpr (b == 1) dma_w2b(t0[1], sw2, r);
      else dma_w2c(t0[2], sw2, r);
    } else if constexpr (kind == H2) {
      if constexpr (a >= 18) load16_sc1<(126 + a) * 1024>(hf[a][b], l16, ho[b], mk_rsrc(ws + WS_H, onh));
      else load16_sc1<(6 * ((a % 6) / 2) + 2 * (a / 6) + (a & 1)) * 1024>(hf[a][b], l16, hb[b], sh);
    } else if constexpr (kind == SAMPLE) load4_sc1(fl, t0[3], sF);
    else if constexpr (kind == CHECK) flags_wait<CV(NN), QKV ? 8 : 2>(fl, t0[3], tag, a == 0 ? 0x111ull : a == 1 ? 0x222ull : (u64)om, sF);
    else if constexpr (kind == PUB && QKV) publish2(tag, fq[0], mk_rsrc(ws + WS_FL, fn[0]), fq[1], mk_rsrc(ws + WS_FL, fn[1]));
    else if constexpr (kind == PUB) publish(tag, (unsigned)(FL_H + ((((tok0 >> 5) + wave) * 24 + col0 / 192) * 4 + a) * FS), sF);
  };
  f32x4 acc[NCG][2], acc2[9][2];
  bf16x4 al[DA], ah[DA];
  auto aread = [&](auto N, auto T) {
    constexpr int n = CV(N);
    bf16x4 r;
    if constexpr (n >= NIT) { constexpr int o = P::lds2(P::st2(n - NIT)); r = trread(cb[o / (3 * SLOT2)], o % (3 * SLOT2) + P::ti2(n - NIT) * 32 + CV(T) * 16 * 288); }
    else if constexpr (QKV) r = trread(ab[P::jof(n) % 3], ((n / IPS) & 1) * SB + (P::jof(n) / 3) * (SB / 2) + (P::sof(n) % KS) * 4096 + CV(T) * 2048);
    else r = trread(n < 144 ? ab[P::jof(n)] : cur, (P::sof(n) % KS) * 4096 + CV(T) * 2048);
    if constexpr (CV(T) == 0) al[n % DA] = r; else ah[n % DA] = r;
  };
  auto rebase = [&](auto M) {
    constexpr int m = CV(M);
    if constexpr (m >= NIT) cb[m - NIT - 49] = v_addk<(m - NIT - 49) * 3u * SLOT2>(cb[0]);
    else if constexpr (QKV) ab[P::jof(m)] = v_addk<(m / IPS) % 4 == 0 ? (unsigned)(-2 * SB) : 2u * SB>(ab[P::jof(m)]);
    else if constexpr (m < 144) ab[P::jof(m)] = v_addk<SB>(ab[P::jof(m)]);
    else cur = v_addk<(unsigned)((((m / IPS) & 1) - 1) * SB + (P::jof(m) & 1) * (SB / 2))>(ab[P::jof(m) & ~1]);
  };
  float ee, eg, eh[2];
  const float k2 = __builtin_bit_cast(float, spin(__builtin_bit_cast(unsigned, LOG2E * LOG2E)));
  auto epi = [&](auto SU, auto E) {
    constexpr int e = CV(E), w = e / 11, ph = e % 11, p = w >> 2, th = (w >> 1) & 1, r = 2 * (w & 1) + (ph >= 4 && ph != 5);
    if constexpr (ph == 0 || ph == 4) { if constexpr (ph == 4) eh[0] = ee; ee = v_expn(acc[2 * p][th][r]); }
    if constexpr (ph == 1 || ph == 6) eg = v_mul(acc[2 * p][th][r], acc[2 * p + 1][th][r]);
    if constexpr (ph == 2 || ph == 7) ee = v_fmak(ee, k2);
    if constexpr (ph == 3 || ph == 8) ee = v_rcp(ee);
    if constexpr (ph == 5) eh[0] = v_mul(eg, eh[0]);
    if constexpr (ph == 9) eh[1] = v_mul(eg, ee);
    if constexpr (ph == 10) {
      hp[th][2 * (p & 1) + (w & 1)] = v_cvtpk(eh[0], eh[1]);
      if constexpr ((p & 1) && (w & 1)) store16_sc1<(p >> 1) * 1024>(hp[th], sh, l16, hst + (th * KSTOT + 2 * CV(SU)) * 1024);
    }
  };
  auto filler = [&](auto N, auto T) {
    constexpr int n = CV(N), t = CV(T);
    if constexpr (t == 1 && P::rebase(n + 4)) rebase(ic<n + 4>{});
    else if constexpr (!QKV) sfor<3>([&](auto SU) { constexpr int e = P::eidx(CV(SU), n, t); if constexpr (e >= 0) epi(SU, ic<e>{}); });
    GAP();
  };
  u32x2 pk[6][2];
  const unsigned stg = smem + wave * 6144 + lane * 8;
  auto epiq = [&](auto SU, auto HB, auto J, auto T) {
    constexpr int j = CV(J), t = CV(T);
    if constexpr (j >= 2 && j < 8) {
      constexpr int e = j - 2, cg = 3 * CV(HB) + (e >> 1), th = e & 1;
      pk[cg][th][t] = v_cvtpk(acc[cg][th][2 * t], acc[cg][th][2 * t + 1]);
      if constexpr (CV(SU) == 0 && t == 1) *(LDS u32x2*)(stg + LDS_K + th * 3072 + 512 * cg) = pk[cg][th];
      GAP();
    }
  };
  if (!QKV || !spare) {
    sfor<P::NPRO>([&](auto I) { vmem(ic<XF>{}, I, ic<0>{}, ic<0>{}); });
    GAP(); wait_barrier<P::NPRO - 2>(); GAP();
    load4_sc1(tch[2], XT ? xto : 0x80000000u, sxn);
    pin(tch0);
    STAMP(5);
    __builtin_amdgcn_s_setprio(PL1 - 1);
    sfor<DA - 1>([&](auto N) { aread(N, ic<0>{}); aread(N, ic<1>{}); });
    GAP();
    sfor<NIT>([&](auto N) {
      constexpr int n = CV(N), st = n / IPS, it = n % IPS, j = P::jof(n), sg = P::sof(n), su = st / SPU, c = st % SPU, m = n + DA - 1;
      constexpr bool more = m < NIT || !QKV;
      if constexpr (QKV && n == NIT - (DA - 1)) { lds_barrier(); GAP(); }
      constexpr bool early = !QKV && (st == 2 || st == 3);
      if constexpr (early && n == P::eat(st - 2, st == 2 ? 153 : 65) - 1) { wait_vm<P::wait1(n, W1, st + 1)>(); GAP(); }
      if constexpr (it == P::BAR && st < P::NST - 1) { wait_barrier<early ? 63 : P::wait1(n, W1, st + 1)>(); GAP(); }
      if constexpr (!QKV && n == NIT - (DA - 1)) { wait_barrier<cmin(P::wait1(n, W2, 3), P::wait1(n, H2, 0))>(); GAP(); }
      { constexpr int pp = (it + DA - 1) % IPS; if constexpr (pp % (IPS / PL1) == 0) { __builtin_amdgcn_s_setprio(PL1 - 1 - pp / (IPS / PL1)); GAP(); } }
      if constexpr (su == 0 && j == 0 && n > 0) { wait_vm<P::wait1(n, XF, 2 * sg + 1)>(); GAP(); }
      const bf16x8 a = cat8(al[n % DA], ah[n % DA]);
      constexpr bool swp = QKV && su == 1;
      sfor<2>([&](auto T) {
        if constexpr (more) { aread(ic<m>{}, T); GAP(); }
        const bf16x8 b = as_bf(xf[sg][T]);
        if constexpr (sg == 0) mfma_new(acc[j][T], swp ? b : a, swp ? a : b); else mfma_acc(acc[j][T], swp ? b : a, swp ? a : b);
        GAP();
        filler(N, T);
        if constexpr (QKV && c == 5 && it >= IPS / 2) epiq(ic<su>{}, ic<0>{}, ic<it - IPS / 2>{}, T);
        if constexpr (QKV && c == 0 && it < IPS / 2 && su > 0) epiq(ic<su - 1>{}, ic<1>{}, ic<it>{}, T);
      });
      if constexpr (QKV) {
        if constexpr (st == 16 && it < 12) *(LDS u32x2*)(stg + LDS_V + (it % 2) * 3072 + 512 * (it / 2)) = pk[it / 2][it % 2];
        if constexpr (n == 6 * IPS) tch[0] = ldtab(2);
        if constexpr (n == 11 * IPS) pin(tch[0]);
        if constexpr (n == 14 * IPS) t0 = *(volatile LDS u32x4*)tab;
      } else {
        if constexpr (st >= 1 && st <= 3 && it == 24) { tch[0] = ldtab(2); tch[1] = ldtab(3); }
        if constexpr (st >= 1 && st <= 3 && it == 41) { tk[st - 1][0] = tch[0]; tk[st - 1][1] = tch[1]; }
        if constexpr (n == 200 || n == 230 || n == 354) { constexpr int q = n == 200 ? 0 : n == 230 ? 1 : 2; pin(tk[q][0]); pin(tk[q][1]); }
        if constexpr (n == 320) t0 = *(volatile LDS u32x4*)tab;
        if constexpr (n == NIT - 10) cb[0] = ldtab(0);
      }
      if constexpr (n == 60) pin(tch[2]);
      constexpr Op o = P::at1(n);
      if constexpr (o.kind != NONE) vmem(ic<o.kind>{}, ic<o.a>{}, ic<o.b>{}, ic<P::wait1(n, SAMPLE, 9)>{});
      GAP();
      if constexpr (n % (NIT / 9) == NIT / 9 - 1) STAMP(6 + n / (NIT / 9));
    });
    if constexpr (QKV) {
      mfma_drain();
      sfor<18>([&](auto J) { epiq(ic<2>{}, ic<1>{}, ic<CV(J) / 2>{}, ic<CV(J) % 2>{}); });
      const int i16 = lane & 15, q4 = lane >> 4, odd = lane & 1;
      const int lo = grp == 6 ? 6 : grp < 4 ? 2 * grp : 0, hlo = grp == 6 ? 1 : 0, hhi = grp < 4 || grp == 6 ? 5 : 4;
      const bool cur0 = 4 * q4 + odd <= i16, cur1 = 4 * q4 + 2 + odd <= i16;
      const unsigned s0 = odd ? 0x01000c0cu : 0x0c0c0100u, s1 = odd ? 0x03020c0cu : 0x0c0c0302u;
      const unsigned sel[4] = {cur0 ? 0x0c0c0c0cu : s0, cur1 ? 0x0c0c0c0cu : s1, cur0 ? s0 : 0x0c0c0c0cu, cur1 ? s1 : 0x0c0c0c0cu};
      constexpr float SC = 0.23570226039551584f / LOG2E;
      auto ld2 = [&](unsigned a0, unsigned a1) { const u32x2 u = *(volatile LDS u32x2*)a0, v = *(volatile LDS u32x2*)a1; return as_bf(u32x4{u[0], u[1], v[0], v[1]}); };
      const unsigned prv0 = wave == 0 ? stg : stg - 3072;
      const float cp = t16 < 0 ? -1.0e30f : 0.f;
      unsigned ge[7], lt[7];
      sfor<7>([&](auto I) { lt[I] = (unsigned)((4 * q4 - lo - 2 * (CV(I) - 1)) >> 31); ge[I] = ~lt[I]; pin(lt[I]); pin(ge[I]); });
      float ca[4], cb[4];
      sfor<4>([&](auto I) { const bool par = odd == (CV(I) & 1); ca[I] = par && cur0 == (CV(I) < 2) ? SC : 0.f; cb[I] = par && cur1 == (CV(I) < 2) ? SC : 0.f; pin(ca[I]); pin(cb[I]); });
      f32x4 sp[10], sc[10];
      float l0[10], l1[10];
      bf16x8 pb[10], kb[5][3], vb[2][6];
      sfor<5>([&](auto H) {
        kb[H][0] = ld2(prv0 + LDS_K + 512 * H, prv0 + LDS_K + 512 * H + 512); kb[H][1] = ld2(stg + LDS_K + 512 * H, stg + LDS_K + 512 * H + 512);
        kb[H][2] = ld2(stg + LDS_K + 3072 + 512 * H, stg + LDS_K + 3072 + 512 * H + 512);
      });
      GAP();
      auto pf = [&](auto I) { constexpr Op o = P::pre2(CV(I)); GAP(); vmem(ic<o.kind>{}, ic<o.a>{}, ic<o.b>{}, ic<0>{}); GAP(); };
      pf(ic<0>{});
      sfor<5>([&](auto HH) {
        constexpr int hd = CV(HH);
        sfor<2>([&](auto TH) {
          const bf16x8 qb = as_bf(u32x4{vand(pk[hd][TH][0], ge[hd + 1]), vand(pk[hd][TH][1], ge[hd]), vand(pk[hd + 1][TH][0], lt[hd + 2]), vand(pk[hd + 1][TH][1], lt[hd + 1])});
          sp[2 * hd + TH] = CV(TH) ? mfma(kb[hd][1], qb, f32x4{cp, cp, cp, cp}) : mfma(kb[hd][0], qb, f32x4{});
          sc[2 * hd + TH] = mfma(kb[hd][1 + TH], qb, f32x4{});
        });
        pf(ic<1 + hd>{});
      });
      GAP();
      sfor<6>([&](auto J) { vb[0][J] = ld2(prv0 + LDS_V + 512 * J, stg + LDS_V + 512 * J); vb[1][J] = ld2(stg + LDS_V + 512 * J, stg + LDS_V + 3072 + 512 * J); });
      GAP();
      lds_barrier();
      float mx[10];
      sfor<10>([&](auto E) {
        l0[E] = fma4(sc[E][0], sc[E][1], sp[E][0], sp[E][1], ca);
        l1[E] = fma4(sc[E][2], sc[E][3], sp[E][2], sp[E][3], cb);
        mx[E] = vmax(l0[E], l1[E]); pin(mx[E]); pin(l0[E]); pin(l1[E]);
        if constexpr (CV(E) % 5 == 0) pf(ic<6 + CV(E) / 5>{});
      });
      GAP();
      xq10<true>(mx);
      pf(ic<8>{});
      sfor<10>([&](auto E) { l0[E] = __builtin_amdgcn_exp2f(l0[E] - mx[E]); l1[E] = __builtin_amdgcn_exp2f(l1[E] - mx[E]); mx[E] = l0[E] + l1[E]; }); GAP();
      pf(ic<9>{});
      xq10<false>(mx);
      pf(ic<10>{});
      sfor<10>([&](auto E) {
        constexpr int hd = CV(E) >> 1;
        const float inv = mx[E] * ((hd >= hlo && hd < hhi) ? 1.f / LOG2E : 0.f);
        const unsigned pp = cvtpk(l0[E] * inv, l1[E] * inv);
        pb[E] = as_bf(u32x4{__builtin_amdgcn_perm(pp, pp, sel[0]), __builtin_amdgcn_perm(pp, pp, sel[1]),
                            __builtin_amdgcn_perm(pp, pp, sel[2]), __builtin_amdgcn_perm(pp, pp, sel[3])});
      });
      pf(ic<11>{});
      sfor<10>([&](auto E) { sp[E] = mfma(vb[CV(E) & 1][CV(E) >> 1], pb[E], f32x4{}); sc[E] = mfma(vb[CV(E) & 1][(CV(E) >> 1) + 1], pb[E], f32x4{}); });
      GAP();
      wait_vm<0>();
      sfor<6>([&](auto I) { pin(hf[I][0]); pin(hf[I][1]); });
      sfor<2>([&](auto TH) {
        unsigned ow[12];
        sfor<12>([&](auto E) {
          constexpr int j = CV(E) >> 1, e = CV(E) & 1;
          unsigned a = 0u, b = 0u;
          if constexpr (j < 5) a = cvtpk(sp[2 * j + TH][2 * e], sp[2 * j + TH][2 * e + 1]);
          if constexpr (j > 0) b = cvtpk(sc[2 * j - 2 + TH][2 * e], sc[2 * j - 2 + TH][2 * e + 1]);
          ow[E] = 4 * q4 + 2 * e >= lo + 2 * j ? a : b;
        });
        const rsrc_t so = mk_rsrc(ws + WS_H, fn[TH] ? HBUF_B : 0u);
        sfor<3>([&](auto M) {
          store16_sc1<1024 * CV(M)>(u32x4{ow[4 * M], ow[4 * M + 1], ow[4 * M + 2], ow[4 * M + 3]}, so, l16, ((t16 + TH) * KSTOT + 144 + 3 * grp) * 1024);
        });
      });
      STAMP(16);
    }
  } else {
    wait_vm<0>(); pin(tch0);
    t0 = *(volatile LDS u32x4*)tab;
    vmem(ic<SAMPLE>{}, ic<0>{}, ic<0>{}, ic<0>{}); vmem(ic<CHECK>{}, ic<0>{}, ic<0>{}, ic<0>{});
    sfor<24>([&](auto I) { constexpr Op o = P::pre2(CV(I)); vmem(ic<o.kind>{}, ic<o.a>{}, ic<o.b>{}, ic<0>{}); });
    wait_vm<0>();
    sfor<6>([&](auto I) { pin(hf[I][0]); pin(hf[I][1]); });
  }
  if constexpr (QKV) {
    cb[0] = ldtab(0);
    vmem(ic<SAMPLE>{}, ic<1>{}, ic<0>{}, ic<0>{});
    GAP(); lds_barrier(); GAP();
    sfor<DA - 1>([&](auto N) { aread(ic<NIT + CV(N)>{}, ic<0>{}); aread(ic<NIT + CV(N)>{}, ic<1>{}); });
    __builtin_amdgcn_s_setprio(3);
    GAP();
    STAMP(17);
  }
  sfor<NIT2>([&](auto M) {
    constexpr int m = CV(M), n = NIT + m, st = P::st2(m), i = P::ti2(m);
    if constexpr (P::bar2(m) >= 0) { wait_barrier<P::wait2(m, W2, P::bar2(m))>(); GAP(); }
    {
      constexpr int B2 = 99, L1 = (B2 - 67 + 3) / 4, L2 = (189 - B2 + 3) / 4;
      constexpr int pr = m < 67 ? ((m + 3) % 18 == 0 ? 3 - (m + 3) / 18 : -1) : m < B2 ? ((m - 67) % L1 == 0 ? 3 - (m - 67) / L1 : -1) : ((m - B2) % L2 == 0 ? 3 - (m - B2) / L2 : -1);
      if constexpr (pr >= 0) { __builtin_amdgcn_s_setprio(pr); GAP(); }
    }
    if constexpr (P::first2(m) && st > 0) { wait_vm<P::wait2(m, H2, st)>(); GAP(); }
    const bf16x8 a = cat8(al[n % DA], ah[n % DA]);
    sfor<2>([&](auto T) {
      if constexpr (m + DA - 1 < NIT2) { aread(ic<n + DA - 1>{}, T); GAP(); }
      if constexpr (st == 0) mfma_new(acc2[i][T], a, as_bf(hf[st][T])); else mfma_acc(acc2[i][T], a, as_bf(hf[st][T]));
      GAP();
      filler(ic<n>{}, T);
      constexpr int C0 = P::TM + LT + 1;
      if constexpr (m >= C0 && m < 177 && (m - C0) % LT < 2) {
        constexpr int k = (m - C0) / LT, tl = (k + 8) % 9, th = (m - C0) % LT;
        gp[2 * th + T] = v_cvtpk(acc2[tl][th][2 * T], acc2[tl][th][2 * T + 1]);
        if constexpr (th == 1 && CV(T) == 1) store16_sc1<0>(gp, sS, l16, sl + ((k + 7) % 8) * 1024);
        GAP();
      }
    });
    if constexpr (m == 134) { ooff = ldtab(1); if constexpr (!QKV) pin(tch[0]); }
    constexpr Op o = P::at2(m);
    if constexpr (o.kind != NONE) { vmem(ic<o.kind>{}, ic<o.a>{}, ic<o.b>{}, ic<P::wait2(m, SAMPLE, 9)>{}); GAP(); }
    if constexpr (m % 27 == 26) STAMP(18 + m / 27);
  });
  unsigned fo = FL_S + (tile * 8 + wave) * 8 * FS + ((16 * (b2 + 7) - l16) & 0x70) * (FS / 16), f;
  load4_sc1(f, fo, sF);
  publish(tag, (unsigned)(FL_S + ((tile * 8 + wave) * 8 + b2) * FS), sF);
  STAMP(26);
  mfma_drain();
  sfor<4>([&](auto I) { pin(acc2[7 + CV(I) / 2][CV(I) & 1]); pin(xres[I]); });
  lane = lane_now();
  const unsigned one = (lane >> 4) == ((lane >> 2) & 3) ? (lane & 1 ? 0x3f800000u : 0x3f80u) : 0u, e0 = lane & 2 ? 0u : one, e1 = lane & 2 ? one : 0u;
  const bf16x8 elo = as_bf(u32x4{e0, e1, 0u, 0u}), ehi = as_bf(u32x4{0u, 0u, e0, e1});
  const bf16x8 xb = as_bf(u32x4{xres[0][0], xres[0][1], xres[1][0], xres[1][1]});
  f32x4 o0 = mfma(elo, xb, acc2[7][0]), o1 = mfma(ehi, xb, acc2[7][1]);
  const unsigned xr = smem + LDS_X + wave * 2048;
  unsigned tw = xr + (lane & 15) * 48 + (lane >> 4) * 8, trd = xr + (lane >> 1) * 48 + (lane & 1) * 16, tst = (lane >> 1) * 1152 + (lane & 1) * 16;
  pin(tw); pin(trd); pin(tst); pin(o0); pin(o1);
  const unsigned s0 = spin((tile * 64 + wave) * 8192);
  u32x4 pv[7], pw[7];
  gather7(pv, pw, f, fo, l16, l16 + 4096, tag, b2 + 7, s0, sS, mk_rsrc(ws + WS_SLAB, guest ? 0u : SLAB_B), sF);
  STAMP(28);
  const rsrc_t sO = mk_rsrc(A.out, S * D * 2);
  auto finish = [&](const u32x4 (&p)[7], unsigned so) {
    sfor<7>([&](auto J) { o0 = mfma(elo, as_bf(p[J]), o0); o1 = mfma(ehi, as_bf(p[J]), o1); });
    *(LDS u32x2*)tw = u32x2{cvtpk(o0[0], o0[1]), cvtpk(o0[2], o0[3])}; *(LDS u32x2*)(tw + 768) = u32x2{cvtpk(o1[0], o1[1]), cvtpk(o1[2], o1[3])};
    asm volatile("" ::: "memory");
    store16(*(LDS u32x4*)trd, sO, tst, so);
  };
  finish(pv, oso + 32 * b2);
  if (!guest) {
    const bf16x8 x8 = as_bf(u32x4{xres[2][0], xres[2][1], xres[3][0], xres[3][1]});
    o0 = mfma(elo, x8, acc2[8][0]); o1 = mfma(ehi, x8, acc2[8][1]);
    finish(pw, oso + 256);
  }
  STAMP(29);
  if constexpr (QKV) asm volatile(".Lpsk_end_a:"); else asm volatile(".Lpsk_end_b:");
}

__global__ __launch_bounds__(512, 2) void fused(Args A) {
  __shared__ __attribute__((aligned(256))) char smem_[LDS_B];
  const unsigned smem = (unsigned)(size_t)(LDS char*)smem_;
  const int wave = rfl(threadIdx.x >> 6), lane = threadIdx.x & 63, bid = blockIdx.x, xcd = bid & 7, r = bid >> 3;
  char* ws = A.ws;
  STAMP(0);
  u32x4 v; unsigned v9, ep, tc[2];
  const rsrc_t sF = mk_rsrc(ws + WS_FL, FL_B);
  {
    const rsrc_t sx = mk_rsrc(A.x, S * D * 2);
    const unsigned xo = (unsigned)((bid * 8 + wave) * 1152);
    load16(v, xo + lane * 16, sx);
    asm volatile("buffer_load_ushort %0, %1, %2, 0 offen offset:1024" : "=v"(v9) : "v"(xo + lane * 2), "s"(sx));
    load4_sc1(ep, FL_EPOCH, sF);
  }
  asm volatile("s_barrier" ::: "memory");
  const bool qkv = r >= 24;
  const int item = xcd * 8 + r - 24, t9 = item / 7, g = item - 7 * t9;
  const int tok0 = qkv ? t9 * 240 - 16 : (r / 3) * 256, col0 = qkv ? grp_col0(g) : (xcd * 3 + r % 3) * 192;
  {
    u64 pc; unsigned na, nb;
    asm volatile("s_getpc_b64 %0\n.Lpsk_begin:\n\ts_mov_b32 %1, .Lpsk_end_a-.Lpsk_begin\n\ts_mov_b32 %2, .Lpsk_end_b-.Lpsk_begin" : "=s"(pc), "=s"(na), "=s"(nb));
    const int w3 = wave & 3, q8 = tok0 >> 8;
    unsigned vo;
    if (qkv) vo = (t9 * 64 + lane) * (3 * D * 2) + col0 * 2 + (wave < 4 ? 2304 : 1152) - 94 + 94 * w3;
    else { const unsigned id = (w3 - 1) * 64 + lane; vo = pick<unsigned>(id < 72, (q8 * 72 + id) * (F * 2), pick<unsigned>(id < 108, (q8 * 36 + id - 72) * (F * 2) + 128, 0x80000000u)) + col0 * 2; }
    if (wave == 4) vo = ((r & 15) * 64 + lane) * 128;
    const unsigned nby = wave == 4 ? (na > nb ? na : nb) : w3 == 0 || item == 63 ? 0u : qkv ? D * 3 * D * 2 : D * F * 2;
    const char* base = wave == 4 ? (const char*)pc : qkv ? A.wqkv : wave < 4 ? A.wg : A.wu;
    load4(tc[0], vo, mk_rsrc(base, nby));
  }
  wait_vm<1>();
  pin(v); pin(v9); pin(ep);
  STAMP(1);
  const unsigned tag = rfl(ep) * 1664525u + 1013904223u;
  {
    float f[9];
    sfor<4>([&](auto E) { f[2 * E] = bflo(v[(int)E]); f[2 * E + 1] = bfhi(v[(int)E]); });
    f[8] = bflo(v9);
    const float mean = wave_sum(((f[0] + f[1]) + (f[2] + f[3])) + ((f[4] + f[5]) + (f[6] + f[7])) + f[8]) * (1.0f / D);
    float q = 0.f;
    sfor<9>([&](auto E) { const float d = f[E] - mean; q += d * d; });
    const float var = wave_sum(q) * (1.0f / D);
    const float mb = rbf(mean), rs = LOG2E * __builtin_amdgcn_rcpf(rbf(__builtin_amdgcn_sqrtf(rbf(rbf(var) + 1e-5f))));
    sfor<4>([&](auto E) { v[(int)E] = cvtpk((f[2 * E] - mb) * rs, (f[2 * E + 1] - mb) * rs); });
    *(LDS u32x4*)(smem + LDS_X + lane * 128 + wave * 16) = v;
    *(LDS unsigned short*)(smem + LDS_X + 8192 + (lane >> 3) * 128 + wave * 16 + (lane & 7) * 2) = (unsigned short)cvtpk((f[8] - mb) * rs, 0.f);
  }
  lds_barrier();
  STAMP(2);
  if (wave == 0) {
    u32x4 t[9];
    sfor<9>([&](auto I) { t[I] = *(LDS u32x4*)(smem + LDS_X + 1024 * I + lane * 16); });
    const unsigned o = (unsigned)((bid >> 1) * 18432 + (bid & 1) * 128 + (lane >> 3) * 256 + (lane & 7) * 16);
    const rsrc_t sxn = mk_rsrc(ws + WS_XN, XN_B);
    sfor<9>([&](auto I) { store16_sc1<0>(t[I], sxn, o, 2048 * I); });
    publish(tag, FL_X + FS * bid, sF);
    STAMP(3);
  }
  asm volatile("s_barrier" ::: "memory");
  if (!qkv) body<false>(A, smem, tag, tok0, col0, 0, r % 3, false, wave, lane, tc[0]);
  else body<true>(A, smem, tag, tok0, col0, g, g, item == 63, wave, lane, tc[0]);
}

}
extern "C" void kernel_launch(void* const* d_in, const int* in_sizes, int n_in, void* d_out, int out_size, void* d_ws, size_t ws_size,
                              hipStream_t stream) {
  psk::Args A;
  A.x = (const char*)d_in[0]; A.wqkv = (const char*)d_in[1]; A.wo = (const char*)d_in[2]; A.wg = (const char*)d_in[4];
  A.wu = (const char*)d_in[5]; A.wd = (const char*)d_in[6]; A.ws = (char*)d_ws; A.out = (char*)d_out;
  psk::fused<<<256, 512, 0, stream>>>(A);
}
```

```cpp
#define PADI 99
#define PADT 0
#include <hip/hip_runtime.h>
#include <utility>
#pragma clang diagnostic ignored "-Wint-to-pointer-cast"

typedef float    f32x4  __attribute__((ext_vector_type(4)));
typedef float    f32x2  __attribute__((ext_vector_type(2)));
typedef __bf16   bf16x8 __attribute__((ext_vector_type(8)));
typedef __bf16   bf16x4 __attribute__((ext_vector_type(4)));
typedef __bf16   bf16x2 __attribute__((ext_vector_type(2)));
typedef unsigned u32x4  __attribute__((ext_vector_type(4)));
typedef unsigned u32x2  __attribute__((ext_vector_type(2)));
typedef unsigned long long u64;
typedef __amdgpu_buffer_rsrc_t rsrc_t;
#define LDS __attribute__((address_space(3)))
#define DI __device__ __forceinline__
#define GAP() __builtin_amdgcn_sched_barrier(0)
#define CV(X) (decltype(X)::value)

#define XT 0
#define NPROV 6
#define LT 8
#define PB 52
#define SM 67
namespace psk {

constexpr int S = 2048, D = 576, F = 4608;
constexpr int KSTOT = 165;
constexpr unsigned HBUF_B = 128u * KSTOT * 1024u;
constexpr unsigned SLAB_B = 32u * 8u * 8u * 8192u;
constexpr unsigned XN_B = S * D * 2u;
constexpr unsigned FS = 128;
constexpr unsigned FL_EPOCH = 0, FL_X = FS, FL_H = FL_X + 256 * FS, FL_O = FL_H + 64 * 24 * 4 * FS, FL_S = FL_O + 128 * 8 * FS, FL_B = FL_S + 64 * 8 * 4 * FS;
constexpr size_t WS_H = 0, WS_SLAB = WS_H + HBUF_B, WS_XN = WS_SLAB + SLAB_B, WS_FL = WS_XN + XN_B, WS_DBG = WS_FL + FL_B;
constexpr int SC1 = 16;
constexpr unsigned SPIN_CAP = 1u << 18;

template <int N> using ic = std::integral_constant<int, N>;
template <class Fn, int... Is> DI void sfor_impl(Fn&& f, std::integer_sequence<int, Is...>) { (f(ic<Is>{}), ...); }
template <int N, class Fn> DI void sfor(Fn&& f) { sfor_impl(f, std::make_integer_sequence<int, N>{}); }

DI unsigned rfl(unsigned x) { return __builtin_amdgcn_readfirstlane(x); }
DI rsrc_t mk_rsrc(const void* p, unsigned bytes) { return __builtin_amdgcn_make_buffer_rsrc((void*)p, (short)0, (int)bytes, 0x00020000); }
DI unsigned cvtpk(float lo, float hi) { f32x2 v = {lo, hi}; return __builtin_bit_cast(unsigned, __builtin_convertvector(v, bf16x2)); }
DI float rbf(float x) { return __builtin_bit_cast(float, cvtpk(x, x) << 16); }
DI float bflo(unsigned v) { return __builtin_bit_cast(float, v << 16); }
DI float bfhi(unsigned v) { return __builtin_bit_cast(float, v & 0xffff0000u); }
DI bf16x4 trread(unsigned addr, int off) { return __builtin_amdgcn_ds_read_tr16_b64_v4bf16((LDS bf16x4*)(addr + off)); }
DI bf16x8 cat8(bf16x4 a, bf16x4 b) { return __builtin_shufflevector(a, b, 0, 1, 2, 3, 4, 5, 6, 7); }
DI bf16x8 as_bf(u32x4 v) { return __builtin_bit_cast(bf16x8, v); }
DI f32x4 mfma(bf16x8 a, bf16x8 b, f32x4 c) { return __builtin_amdgcn_mfma_f32_16x16x32_bf16(a, b, c, 0, 0, 0); }
DI void mfma_acc(f32x4& c, bf16x8 a, bf16x8 b) { asm volatile("v_mfma_f32_16x16x32_bf16 %0, %1, %2, %0" : "+v"(c) : "v"(a), "v"(b)); }
DI void mfma_new(f32x4& c, bf16x8 a, bf16x8 b) { asm volatile("v_mfma_f32_16x16x32_bf16 %0, %1, %2, 0" : "=v"(c) : "v"(a), "v"(b)); }
DI void mfma_drain() { asm volatile("s_nop 7\n\ts_nop 1"); GAP(); }
template <class T> DI void pin(T& x) { asm volatile("" : "+v"(x)); }
DI unsigned lane_now() { unsigned l; asm volatile("v_mbcnt_lo_u32_b32 %0, -1, 0\n\tv_mbcnt_hi_u32_b32 %0, -1, %0" : "=v"(l)); return l; }
DI unsigned spin(unsigned x) { asm volatile("" : "+s"(x)); return x; }
template <int N> DI void wait_vm() { if constexpr (N < 63) asm volatile("s_waitcnt vmcnt(%0)" ::"i"(N) : "memory"); }
template <int N> DI void wait_barrier() { asm volatile("s_waitcnt vmcnt(%0) lgkmcnt(0)\n\ts_barrier" ::"i"(N) : "memory"); }
DI void lds_barrier() { asm volatile("s_waitcnt lgkmcnt(0)\n\ts_barrier" ::: "memory"); }
DI float v_mul(float a, float b) { float r; asm volatile("v_mul_f32 %0, %1, %2" : "=v"(r) : "v"(a), "v"(b)); return r; }
DI float v_expn(float a) { float r; asm volatile("v_exp_f32_e64 %0, -%1" : "=v"(r) : "v"(a)); return r; }
DI float v_fmak(float a, float k) { float r; asm volatile("v_fma_f32 %0, %1, %2, %2" : "=v"(r) : "v"(a), "s"(k)); return r; }
template <unsigned K> DI unsigned v_addk(unsigned a) { unsigned r; asm volatile("v_add_u32 %0, %2, %1" : "=v"(r) : "v"(a), "i"(K)); return r; }
DI float v_rcp(float a) { float r; asm volatile("v_rcp_f32 %0, %1" : "=v"(r) : "v"(a)); return r; }
DI unsigned v_cvtpk(float a, float b) { unsigned r; asm volatile("v_cvt_pk_bf16_f32 %0, %1, %2" : "=v"(r) : "v"(a), "v"(b)); return r; }

DI void padto(u64 t0, unsigned ticks) {
  unsigned a, b; u64 t;
  GAP();
  asm volatile("s_memrealtime %0\n\ts_waitcnt lgkmcnt(0)" : "=s"(t)::"memory");
  while ((unsigned)(t - t0) < ticks) { asm volatile("s_sleep 1\n\ts_memrealtime %0\n\ts_waitcnt lgkmcnt(0)" : "=s"(t)::"memory"); }
  (void)a; (void)b; GAP();
}
#define STAMP(i) do { if ((i) == PADI) padto(tstart, PADT); } while (0)

template <int LOFF, unsigned GOFF> DI void dma16(unsigned m0base, unsigned voff, rsrc_t srd) {
  unsigned t;
  asm volatile("s_add_u32 m0, %1, %2\n\ts_mov_b32 %0, %3\n\tbuffer_load_dwordx4 %4, %5, %0 offen lds"
               : "=&s"(t) : "s"(m0base), "i"(LOFF), "i"(GOFF), "v"(voff), "s"(srd) : "scc", "memory");
}
template <int LOFF, int GOFF> DI unsigned dma_w2a(unsigned m0base, unsigned voff, unsigned sbase, rsrc_t srd) {
  unsigned so;
  asm volatile("s_add_u32 m0, %1, %2\n\ts_add_u32 %0, %5, %6\n\tbuffer_load_dwordx4 %3, %4, %0 offen lds"
               : "=&s"(so) : "s"(m0base), "i"(LOFF), "v"(voff), "s"(srd), "s"(sbase), "i"(GOFF) : "scc", "memory");
  return so;
}
DI void dma_w2b(unsigned voff, unsigned so, rsrc_t srd) { asm volatile("buffer_load_dwordx4 %0, %1, %2 offen offset:1024 lds" ::"v"(voff), "s"(srd), "s"(so) : "memory"); }
DI void dma_w2c(unsigned voff, unsigned so, rsrc_t srd) {
  asm volatile("s_mov_b64 exec, 0xffff\n\tbuffer_load_dwordx4 %0, %1, %2 offen offset:2048 lds\n\ts_mov_b64 exec, -1" ::"v"(voff), "s"(srd), "s"(so) : "memory");
}
template <int GOFF> DI void load16_sc1(u32x4& d, unsigned voff, unsigned sbase, rsrc_t srd) {
  unsigned t;
  asm volatile("s_add_u32 %1, %3, %4\n\tbuffer_load_dwordx4 %0, %2, %5, %1 offen sc1" : "=v"(d), "=&s"(t) : "v"(voff), "s"(sbase), "i"(GOFF), "s"(srd) : "scc");
}
template <int OFF> DI void store16_sc1(u32x4 d, rsrc_t srd, unsigned voff, unsigned soff) {
  asm volatile("buffer_store_dwordx4 %0, %1, %2, %3 offen offset:%4 sc1\n\ts_nop 1" ::"v"(d), "v"(voff), "s"(srd), "s"(soff), "i"(OFF) : "memory");
}
DI void store16(u32x4 d, rsrc_t srd, unsigned voff, unsigned soff) {
  asm volatile("buffer_store_dwordx4 %0, %1, %2, %3 offen sc1\n\ts_nop 1" ::"v"(d), "v"(voff), "s"(srd), "s"(soff) : "memory");
}
DI void load16(u32x4& d, unsigned voff, rsrc_t srd) { asm volatile("buffer_load_dwordx4 %0, %1, %2, 0 offen" : "=v"(d) : "v"(voff), "s"(srd)); }
DI void load4(unsigned& d, unsigned voff, rsrc_t srd) { asm volatile("buffer_load_dword %0, %1, %2, 0 offen" : "=v"(d) : "v"(voff), "s"(srd)); }
template <unsigned SOFF> DI void load4s(unsigned& d, unsigned voff, rsrc_t srd) {
  unsigned t;
  asm volatile("s_mov_b32 %1, %4\n\tbuffer_load_dword %0, %2, %3, %1 offen" : "=v"(d), "=&s"(t) : "v"(voff), "s"(srd), "i"(SOFF));
}
DI void load4_sc1(unsigned& d, unsigned voff, rsrc_t srd) { asm volatile("buffer_load_dword %0, %1, %2, 0 offen sc1" : "=v"(d) : "v"(voff), "s"(srd)); }
template <int N, int SLEEP = 2> DI void flags_wait(unsigned& f, unsigned voff, unsigned tag, u64 mask, rsrc_t srd) {
  unsigned cnt;
  asm volatile("s_waitcnt vmcnt(%6)\n\ts_mov_b32 %1, %7\n"
               "1:\n\tv_cmp_ne_u32 vcc, %3, %0\n\ts_nop 1\n\ts_and_b64 vcc, vcc, %4\n\ts_cbranch_scc0 2f\n\t"
               "s_sleep %8\n\tbuffer_load_dword %0, %2, %5, 0 offen sc1\n\ts_waitcnt vmcnt(0)\n\ts_sub_u32 %1, %1, 1\n\ts_cbranch_scc0 1b\n"
               "2:"
               : "+v"(f), "=&s"(cnt) : "v"(voff), "s"(tag), "s"(mask), "s"(srd), "i"(N), "i"(SPIN_CAP), "i"(SLEEP) : "vcc", "scc", "memory");
}
DI void publish(unsigned tag, unsigned soff, rsrc_t srd) {
  unsigned t;
  asm volatile("v_mov_b32 %0, %1\n\ts_waitcnt vmcnt(0)\n\ts_mov_b64 exec, 1\n\tbuffer_store_dword %0, off, %3, %2 sc1\n\ts_mov_b64 exec, -1"
               : "=&v"(t) : "s"(tag), "s"(soff), "s"(srd) : "memory");
}

DI void gather7(u32x4 (&p)[7], u32x4 (&q)[7], unsigned& f, unsigned fo, unsigned v0, unsigned v1, unsigned tag, unsigned src0, unsigned s0, rsrc_t sp, rsrc_t sq, rsrc_t sf) {
  unsigned pend, rdy, t, cnt;
  asm volatile("s_mov_b32 %15, 0x7f\n\ts_mov_b32 %18, %28\n"
               "1:\n\tv_cmp_eq_u32 vcc, %22, %14\n\ts_nop 1\n\ts_and_b32 %16, vcc_lo, %15\n\ts_andn2_b32 %15, %15, %16\n\t"
               "s_bitcmp1_b32 %16, 0\n\ts_cbranch_scc0 3f\n\ts_sub_u32 %17, %23, 0\n\ts_and_b32 %17, %17, 7\n\ts_lshl_b32 %17, %17, 16\n\ts_add_u32 %17, %17, %24\n\t"
               "buffer_load_dwordx4 %0, %20, %25, %17 offen offset:0 sc1\n\tbuffer_load_dwordx4 %7, %21, %26, %17 offen offset:3072 sc1\n3:\n\t"
               "s_bitcmp1_b32 %16, 1\n\ts_cbranch_scc0 3f\n\ts_sub_u32 %17, %23, 1\n\ts_and_b32 %17, %17, 7\n\ts_lshl_b32 %17, %17, 16\n\ts_add_u32 %17, %17, %24\n\t"
               "buffer_load_dwordx4 %1, %20, %25, %17 offen offset:1024 sc1\n\tbuffer_load_dwordx4 %8, %21, %26, %17 offen offset:3072 sc1\n3:\n\t"
               "s_bitcmp1_b32 %16, 2\n\ts_cbranch_scc0 3f\n\ts_sub_u32 %17, %23, 2\n\ts_and_b32 %17, %17, 7\n\ts_lshl_b32 %17, %17, 16\n\ts_add_u32 %17, %17, %24\n\t"
               "buffer_load_dwordx4 %2, %20, %25, %17 offen offset:2048 sc1\n\tbuffer_load_dwordx4 %9, %21, %26, %17 offen offset:3072 sc1\n3:\n\t"
               "s_bitcmp1_b32 %16, 3\n\ts_cbranch_scc0 3f\n\ts_sub_u32 %17, %23, 3\n\ts_and_b32 %17, %17, 7\n\ts_lshl_b32 %17, %17, 16\n\ts_add_u32 %17, %17, %24\n\t"
               "buffer_load_dwordx4 %3, %20, %25, %17 offen offset:3072 sc1\n\tbuffer_load_dwordx4 %10, %21, %26, %17 offen offset:3072 sc1\n3:\n\t"
               "s_bitcmp1_b32 %16, 4\n\ts_cbranch_scc0 3f\n\ts_sub_u32 %17, %23, 4\n\ts_and_b32 %17, %17, 7\n\ts_lshl_b32 %17, %17, 16\n\ts_add_u32 %17, %17, %24\n\t"
               "buffer_load_dwordx4 %4, %21, %25, %17 offen offset:0 sc1\n\tbuffer_load_dwordx4 %11, %21, %26, %17 offen offset:3072 sc1\n3:\n\t"
               "s_bitcmp1_b32 %16, 5\n\ts_cbranch_scc0 3f\n\ts_sub_u32 %17, %23, 5\n\ts_and_b32 %17, %17, 7\n\ts_lshl_b32 %17, %17, 16\n\ts_add_u32 %17, %17, %24\n\t"
               "buffer_load_dwordx4 %5, %21, %25, %17 offen offset:1024 sc1\n\tbuffer_load_dwordx4 %12, %21, %26, %17 offen offset:3072 sc1\n3:\n\t"
               "s_bitcmp1_b32 %16, 6\n\ts_cbranch_scc0 3f\n\ts_sub_u32 %17, %23, 6\n\ts_and_b32 %17, %17, 7\n\ts_lshl_b32 %17, %17, 16\n\ts_add_u32 %17, %17, %24\n\t"
               "buffer_load_dwordx4 %6, %21, %25, %17 offen offset:2048 sc1\n\tbuffer_load_dwordx4 %13, %21, %26, %17 offen offset:3072 sc1\n3:\n\t"
               "s_cmp_eq_u32 %15, 0\n\ts_cbranch_scc1 2f\n\ts_sleep 1\n\tbuffer_load_dword %14, %19, %27, 0 offen sc1\n\ts_waitcnt vmcnt(0)\n\t"
               "s_sub_u32 %18, %18, 1\n\ts_cbranch_scc0 1b\n2:\n\ts_waitcnt vmcnt(0)"
               : "=&v"(p[0]), "=&v"(p[1]), "=&v"(p[2]), "=&v"(p[3]), "=&v"(p[4]), "=&v"(p[5]), "=&v"(p[6]), "=&v"(q[0]), "=&v"(q[1]), "=&v"(q[2]), "=&v"(q[3]), "=&v"(q[4]), "=&v"(q[5]), "=&v"(q[6]), "+v"(f), "=&s"(pend), "=&s"(rdy), "=&s"(t), "=&s"(cnt)
               : "v"(fo), "v"(v0), "v"(v1), "s"(tag), "s"(src0), "s"(s0), "s"(sp), "s"(sq), "s"(sf), "i"(SPIN_CAP) : "vcc", "scc", "memory");
}
DI void publish2(unsigned tag, unsigned s0, rsrc_t r0, unsigned s1, rsrc_t r1) {
  unsigned t;
  asm volatile("v_mov_b32 %0, %1\n\ts_waitcnt vmcnt(0)\n\ts_mov_b64 exec, 1\n\tbuffer_store_dword %0, off, %3, %2 sc1\n\tbuffer_store_dword %0, off, %5, %4 sc1\n\ts_mov_b64 exec, -1"
               : "=&v"(t) : "s"(tag), "s"(s0), "s"(r0), "s"(s1), "s"(r1) : "memory");
}
DI void load8_if(u32x2& d, unsigned voff, unsigned soff, rsrc_t srd, unsigned off) {
  asm volatile("s_cmp_lg_u32 %4, 0\n\ts_cbranch_scc1 1f\n\tbuffer_load_dwordx2 %0, %1, %2, %3 offen\n1:" : "=v"(d) : "v"(voff), "s"(srd), "s"(soff), "s"(off) : "scc");
}

template <class T> DI T pick(bool c, T a, T b) { return c ? a : b; }
DI unsigned vand(unsigned a, unsigned b) { return a & b; }
DI float fma4(float a, float b, float c, float d, const float (&k)[4]) {
  float r = a * k[0]; pin(r); r = __builtin_fmaf(b, k[1], r); r = __builtin_fmaf(c, k[2], r); return __builtin_fmaf(d, k[3], r);
}
DI float vmax(float a, float b) { float r; asm("v_max_f32 %0, %1, %2" : "=v"(r) : "v"(a), "v"(b)); return r; }
template <bool MAX> DI float x16(float v) {
  const unsigned u = __builtin_bit_cast(unsigned, v);
  auto a = __builtin_amdgcn_permlane16_swap(u, u, false, false);
  const float p = __builtin_bit_cast(float, (unsigned)a[0]), q = __builtin_bit_cast(float, (unsigned)a[1]);
  return MAX ? vmax(p, q) : p + q;
}
template <bool MAX> DI float x32(float v) {
  const unsigned u = __builtin_bit_cast(unsigned, v);
  auto a = __builtin_amdgcn_permlane32_swap(u, u, false, false);
  const float p = __builtin_bit_cast(float, (unsigned)a[0]), q = __builtin_bit_cast(float, (unsigned)a[1]);
  return MAX ? vmax(p, q) : p + q;
}
template <int W> DI void swp(float& a, float& b) {
  const unsigned x = __builtin_bit_cast(unsigned, a), y = __builtin_bit_cast(unsigned, b);
  if constexpr (W == 16) { auto r = __builtin_amdgcn_permlane16_swap(x, y, false, false); a = __builtin_bit_cast(float, (unsigned)r[0]); b = __builtin_bit_cast(float, (unsigned)r[1]); }
  else { auto r = __builtin_amdgcn_permlane32_swap(x, y, false, false); a = __builtin_bit_cast(float, (unsigned)r[0]); b = __builtin_bit_cast(float, (unsigned)r[1]); }
}
template <bool MAX> DI void xq10(float (&m)[10]) {
  auto op = [](float p, float q) { return MAX ? vmax(p, q) : p + q; };
  float a[6], c[3], d[6];
  sfor<5>([&](auto I) { swp<16>(m[2 * I], m[2 * I + 1]); }); GAP();
  sfor<5>([&](auto I) { a[I] = op(m[2 * I], m[2 * I + 1]); pin(a[I]); }); a[5] = a[4]; pin(a[5]); GAP();
  sfor<3>([&](auto I) { swp<32>(a[2 * I], a[2 * I + 1]); }); GAP();
  sfor<3>([&](auto I) { c[I] = op(a[2 * I], a[2 * I + 1]); if constexpr (!MAX) c[I] = __builtin_amdgcn_rcpf(c[I]); pin(c[I]); }); GAP();
  sfor<2>([&](auto I) { d[2 * I] = c[I]; d[2 * I + 1] = c[I]; pin(d[2 * I + 1]); }); d[4] = c[2]; GAP();
  sfor<2>([&](auto I) { swp<32>(d[2 * I], d[2 * I + 1]); }); GAP();
  sfor<5>([&](auto I) { m[2 * I] = d[I]; m[2 * I + 1] = d[I]; pin(m[2 * I + 1]); }); GAP();
  sfor<5>([&](auto I) { swp<16>(m[2 * I], m[2 * I + 1]); }); GAP();
}
template <int CTRL> DI float dpp_add(float v) { return v + __builtin_bit_cast(float, __builtin_amdgcn_update_dpp(0, __builtin_bit_cast(int, v), CTRL, 0xf, 0xf, true)); }
DI float wave_sum(float v) { return x32<false>(x16<false>(dpp_add<0x140>(dpp_add<0x141>(dpp_add<0x4e>(dpp_add<0xb1>(v)))))); }
__host__ __device__ constexpr int grp_col0(int g) { return 8 * (int)((0x3C362D21160B00ull >> (8 * g)) & 0xff); }

constexpr int LDS_V = 2 * 24576, LDS_K = 4 * 24576;
constexpr int LDS_X = 6 * 24576, LDS_B = LDS_X + 16384;
constexpr int SLOT2 = 64 * 288, NIT2 = 21 * 9, DA = 4;
constexpr float LOG2E = 1.4426950408889634f;
constexpr int cmin(int a, int b) { return a < b ? a : b; }

enum { NONE, W1, XF, TOUCH, W2, H2, SAMPLE,   CHECK, PUB, XRES };
struct Op { int kind, a, b; };
template <bool QKV> struct Plan {
  static constexpr int NCG = QKV ? 6 : 8, KS = QKV ? 3 : 9, NST = 54 / KS, SPU = 18 / KS, NSLOT = QKV ? 4 : 2;
  static constexpr int SB = KS * 8192, IPS = KS * NCG, NIT = NST * IPS, BAR = IPS - (DA - 1), NPRO = NPROV, NPRE2 = 24;
  static constexpr bool kmajor(int n) { return QKV ? ((n / IPS) % 6 != 5 && (n < 6 * IPS || (n / IPS) % 6 != 0)) : n < 96; }
  static constexpr int jof(int n) { return kmajor(n) ? n % NCG : (!QKV && n < 144) ? (n - 96) / 6 : (n % IPS) / KS; }
  static constexpr int sof(int n) { return kmajor(n) ? (n % (18 * NCG)) / NCG : (!QKV && n < 144) ? 12 + n % 6 : KS * ((n / IPS) % SPU) + n % KS; }
  static constexpr bool rebase(int m) {
    if (m >= NIT) return m - NIT == 50 || m - NIT == 51;
    if (QKV) return m >= IPS && (m / IPS) % 2 == 0 && (kmajor(m) ? m % IPS < 3 : (m % IPS) % 3 == 0 && m % IPS < 9);
    return m < 144 ? (m >= 72 && m < 80) : m % 9 == 0;
  }
  static constexpr int TM = 174 - 8 * LT;
  static constexpr int st2(int m) { return m < 54 ? m / 9 : m < TM ? 6 + (m - 54) / 8 : m < 174 ? 21 - LT + (m - TM) % LT : 6 + m - 174; }
  static constexpr int ti2(int m) { return m < 54 ? m % 9 : m < 174 ? ((m < TM ? (m - 54) % 8 : (m - TM) / LT) + 8) % 9 : 7; }
  static constexpr bool first2(int m) { return m < 54 ? m % 9 == 0 : m < TM ? (m - 54) % 8 == 0 : m < TM + LT; }
  static constexpr int lds2(int k) { return (k < 12 ? k : k < 18 ? k - 12 : k - 6) * (SLOT2 / 2); }
  static constexpr int bar2(int m) { return m == 67 ? 7 : m == 99 ? 10 : -1; }
  static constexpr int estart(int su) { return su == 0 ? 108 : 144 * su + 92; }
  static constexpr int eidx(int su, int n, int t) {
    if (n < estart(su) || n > estart(su) + 110 || (t == 1 && rebase(n + 4))) return -1;
    int c = 0;
    for (int i = estart(su); i < n; ++i) c += rebase(i + 4) ? 1 : 2;
    return c + t < 176 ? c + t : -1;
  }
  static constexpr int eat(int su, int e) { for (int n = estart(su);; ++n) if (eidx(su, n, 0) == e || eidx(su, n, 1) == e) return n; }
  static constexpr Op pre2(int i) { return i < 12 ? Op{W2, i / 3, i % 3} : Op{H2, (i - 12) / 2, i & 1}; }
  static constexpr Op at1(int n) {
    const int st = n / IPS, it = n % IPS;
    if (QKV) {
      if (n % 2 == 0 && NPRO + n / 2 < 36) return Op{XF, NPRO + n / 2, 0};
      if (st > 0 && it < 6 && it % 2 == 1 && st + 3 < NST) return Op{W1, st + 3, it / 2};
      if (st == 6 && it == 9) return Op{TOUCH, 0, 0};
      if (st == 14 && it == 6) return Op{SAMPLE, 0, 0};
      if (st == 15 && it == 12) return Op{CHECK, 0, 0};
      if (n > 15 * IPS + 12 && n % 3 == 1 && (n - 15 * IPS - 13) / 3 < 12) return pre2(12 + (n - 15 * IPS - 13) / 3);
      return Op{NONE, 0, 0};
    }
    if (n % 3 == 0 && NPRO + n / 3 < 36) return Op{XF, NPRO + n / 3, 0};
    if (st >= 1 && st <= 4) { const int i0 = st == 3 ? 10 : 1; if (it >= i0 && (it - i0) % 3 == 0 && (it - i0) / 3 < 9) return Op{W1, st + 1, (it - i0) / 3}; }
    if (n == 224) return Op{PUB, 0, 0};
    if (st >= 1 && st <= 3 && it == (st == 3 ? 37 : 31)) return Op{TOUCH, st + 2, 0};
    if (st >= 1 && st <= 3 && it == (st == 3 ? 39 : 33)) return Op{TOUCH, st - 1, 1};
    if (n == 330) return Op{SAMPLE, 0, 0};
    if (n == 352) return Op{PUB, 1, 0};
    if (n == 353) return Op{CHECK, 0, 0};
    if (n == 406) return Op{SAMPLE, 1, 0};
    if (n == 431) return Op{CHECK, 1, 0};
    if (n >= 358 && n % 5 < 4 && n % 5 != 1 && n % 5 != 2) { const int e = (n - 358) / 5 * 2 + ((n - 358) % 5 ? 1 : 0); if (e < 24) return pre2(e); }
    return Op{NONE, 0, 0};
  }
  static constexpr Op at2(int m) {
    if (QKV) {
      if (m == 12) return Op{PUB, 3, 0};
      if (m == 13) return Op{CHECK, 1, 0};
      if (m >= 14 && m < 56 && m % 7 != 2 && m % 7 != 4 && m % 7 != 6) {
        const int e = (m - 14) / 7 * 2 + (m % 7 > 1); return (m % 7 == 0 || m % 7 == 3) ? Op{H2, 6 + e / 2, e & 1} : Op{W2, 4 + e / 3, e % 3};
      }
    } else if (m < 42 && m % 7 != 2 && m % 7 != 4 && m % 7 != 6) {
      const int e = m / 7 * 2 + (m % 7 > 1); return (m % 7 == 0 || m % 7 == 3) ? Op{H2, 6 + e / 2, e & 1} : Op{W2, 4 + e / 3, e % 3};
    }
    if (!QKV && m == PB) return Op{PUB, 2, 0};
    if (!QKV && m == PB + 1) return Op{TOUCH, 0, 2};
    if (m == SM) return Op{SAMPLE, 2, 0};
    if (m >= 68 && m < 86 && m % 2 == 0) return Op{W2, 8 + (m - 68) / 6, ((m - 68) / 2) % 3};
    if (m == SM + 20) return Op{CHECK, 2, 0};
    if (m > SM + 20 && m <= SM + 38) return Op{H2, 12 + (m - SM - 21) / 2, (m - SM - 21) & 1};
    if (m >= 140 && m < 148 && m % 2 == 0) return Op{XRES, (m - 140) / 2, 0};
    return Op{NONE, 0, 0};
  }
  static constexpr int wait1(int n0, int kind, int amax) {
    int cnt = 0, need = -1;
    for (int i = -NPRO - 1; i < n0; ++i) {
      const Op o = i < -1 ? Op{XF, i + NPRO + 1, 0} : i < 0 ? Op{TOUCH, 9, 0} : at1(i);
      if (o.kind == PUB) need = -1;
      if (o.kind == NONE || o.kind > SAMPLE) continue;
      ++cnt;
      if (o.kind == kind && o.a <= amax) need = cnt;
    }
    return need < 0 || cnt - need > 62 ? 63 : cnt - need;
  }
  static constexpr int wait2(int m0, int kind, int amax) {
    int cnt = 0, need = -1;
    for (int i = -NPRE2; i < m0; ++i) {
      const Op o = i < 0 ? pre2(i + NPRE2) : at2(i);
      if (o.kind == PUB) need = -1;
      if (o.kind == NONE || o.kind > SAMPLE) continue;
      ++cnt;
      if (o.kind == kind && o.a <= amax) need = cnt;
    }
    return need < 0 || cnt - need > 62 ? 63 : cnt - need;
  }
};
using PF = Plan<false>;
static_assert(PF::eat(0, 175) < 224 - 16 && PF::eat(1, 175) < 352 - 16 && PF::eat(2, 175) < PF::NIT + PB - 8, "a unit is published after its last store");
static_assert(PF::eat(0, 44) >= 96 + 26 && PF::eat(0, 88) >= 96 + 38 && PF::eat(0, 132) >= 96 + 50, "unit 0: accumulators complete before they are read");
static_assert(PF::eat(0, 39) < 144 && PF::eat(0, 83) < 162 && PF::eat(0, 127) < 180 && PF::eat(0, 171) < 198, "unit 0: accumulators read before they restart");
static_assert(PF::eat(1, 44) >= 216 + 38 && PF::eat(1, 88) >= 216 + 56 && PF::eat(1, 132) >= 216 + 74 && PF::eat(1, 171) < 288 + 54, "unit 1");
static_assert(PF::eat(0, 153) > 144 + 30 && PF::eat(0, 153) < 213 && PF::eat(1, 65) > 216 + 40 && PF::eat(1, 65) < 285, "early weight waits follow the last request");
static_assert(PF::eat(2, 44) >= 360 + 38 && PF::eat(2, 88) >= 360 + 56 && PF::eat(2, 132) >= 360 + 74, "unit 2");

struct Args { const char* x; const char* wqkv; const char* wo; const char* wg; const char* wu; const char* wd; char* ws; char* out; };

template <bool QKV>
DI void body(const Args& A, unsigned smem, unsigned tag, int tok0, int col0, int grp, int share, bool spare, int wave, int lane, unsigned& tch0, u64 tstart) {
  using P = Plan<QKV>;
  constexpr int NCG = P::NCG, KS = P::KS, NSLOT = P::NSLOT, IPS = P::IPS, NIT = P::NIT, SPU = P::SPU, SB = P::SB;
  constexpr int PL1 = QKV ? 3 : 4;
  constexpr unsigned RS = QKV ? 3u * D * 2u : F * 2u, WB = QKV ? D * 3 * D * 2 : D * F * 2;
  char* ws = A.ws;
  const int bid = blockIdx.x, isU = wave >> 2, w3 = wave & 3;
  const rsrc_t sF = mk_rsrc(ws + WS_FL, FL_B), sxn = mk_rsrc(ws + WS_XN, XN_B);
  unsigned wv;
  {
    const int cgl = ((lane & 7) >> 1) ^ ((lane >> 4) & 3);
    wv = (unsigned)((16 * (w3 & 1) + 4 * (w3 >> 1) + 8 * (lane >> 5) + ((lane >> 3) & 3)) * RS + col0 * 2 + (QKV && cgl == 3 ? 2 : cgl) * 32 + (lane & 1) * 16);
  }
  const char* wA = QKV ? A.wqkv : A.wg;
  const char* wB = QKV ? A.wqkv + 96 : A.wu;
  const rsrc_t sw = mk_rsrc(isU ? wB : wA, QKV && isU ? WB - 96 : WB);
  const unsigned m0b = smem + isU * (SB / 2) + w3 * 1024;
  auto w1 = [&](auto ST, auto B, unsigned m0, rsrc_t srd) {
    constexpr int a = CV(ST), b = CV(B), su = a / SPU;
    constexpr unsigned sucol = QKV ? (su == 0 ? 1152u : su == 1 ? 2304u : 0u) : su * 128u;
    dma16<(a % NSLOT) * SB + b * 4096, 32 * (KS * (a % SPU) + b) * RS + sucol>(m0, wv, srd);
  };
  if (isU && !spare) {
    const rsrc_t sa = mk_rsrc(wA, WB);
    sfor<NSLOT>([&](auto ST) { sfor<KS>([&](auto B) { w1(ST, B, m0b - SB / 2, sa); w1(ST, B, m0b, sw); }); });
  }
  const int t16 = (tok0 >> 4) + 2 * wave;
  const int b2 = bid & 7, tile = bid >> 3, rt2 = QKV ? tile - 24 : tok0 >> 8, nt2 = QKV ? 3 : tile - 3 * rt2, u16 = rt2 * 16 + 2 * wave;
  const rsrc_t sh = mk_rsrc(ws + WS_H, HBUF_B), sWd = mk_rsrc(A.wd, F * D * 2), sS = mk_rsrc(ws + WS_SLAB, SLAB_B);
  const unsigned m0c = spin(smem + wave * 2304), ws2 = spin(576 * b2 * 1152);
  const unsigned hb[2] = {spin((u16 * KSTOT + 18 * b2) * 1024), spin(((u16 + 1) * KSTOT + 18 * b2) * 1024)};
  const unsigned ho[2] = {spin(hb[0] - 15 * 1024 * b2), spin(hb[1] - 15 * 1024 * b2)};
  const unsigned xs[2] = {spin(t16 * 18432), spin(t16 * 18432 + 18432)}, xz[2] = {spin(t16 >= 0 && t16 < 128 ? XN_B : 0u), spin(t16 + 1 < 128 ? XN_B : 0u)};
  const unsigned oso = spin(u16 * 16 * 1152 + nt2 * 288), hst = spin((t16 * KSTOT + (col0 >> 5)) * 1024);
  const unsigned guest = spin(wave ^ b2), sl = spin(((tile * 8 + b2) * 8 + wave) * 8192);
  const unsigned ow2 = spin(grp_col0(b2) * 1152), onh = spin(b2 < 7 ? HBUF_B : 0u);
  const unsigned onw[2] = {spin(b2 < 7 ? D * D * 2 : 0), spin(b2 < 7 && wave < 4 ? D * D * 2 : 0)};
  const unsigned om = spin(b2 < 7 ? 0x3444 : 0x444);
  const unsigned fq[2] = {spin(FL_O + (t16 * 8 + grp) * FS), spin(FL_O + (t16 * 8 + 8 + grp) * FS)};
  const unsigned fn[2] = {spin(wave > 0 && t16 < 128 ? FL_B : 0u), spin(t16 + 1 < 128 ? FL_B : 0u)};
  const unsigned l16 = lane * 16, tab = smem + LDS_X + wave * 2048 + l16;
  const bool wdo = (bid >> 3) * 8 + wave >= 162;
  unsigned ab[NCG], xto;
  {
    const unsigned l = lane;
    u32x4 t0, t1;
    sfor<3>([&](auto I) { const unsigned n = 64 * I + l, r = n / 18, c = n - 18 * r;
      t0[(int)I] = (8 * wave + r) * 1152 + nt2 * 288 + pick<unsigned>(c < 16, (c + 2 * b2 + 2) & 15, c) * 16 - 1024 * I; });
    const unsigned fa = FL_H + (((rt2 * 8 + wave) * 24 + 3 * b2) * 4 + l) * FS, fb = FL_O + ((u16 + (l & 1)) * 8 + b2) * FS;
    t0[3] = pick<unsigned>(l < 12, fa, fb);
    t1[0] = smem + (l >> 2) * 288 + (l & 3) * 8;
    t1[1] = (l & 15) * 1152 + (l >> 4) * 8;
    if constexpr (QKV) { t1[2] = (((tok0 + 16) / 240) * 64 + l) * RS + col0 * 2 + (!isU && w3 < 3 ? 94 * w3 : 0x80000000u); t1[3] = 0; }
    else {
      t1[2] = pick<unsigned>(l < 9, ((tok0 >> 8) * 36 + w3 * 9 + l) * RS + col0 * 2, 0x80000000u);
      t1[3] = pick<unsigned>(l < 11, (wdo ? ow2 - 162 * 4096 : ws2) + ((bid >> 3) * 8 + wave) * 4096 + l * 128, 0x80000000u);
    }
    *(LDS u32x4*)tab = t0; *(LDS u32x4*)(tab + 1024) = t1;
    const int g = lane >> 4, qq = (lane >> 2) & 3, p = lane & 3, c = (2 * g + (qq >> 1)) & 3;
    if constexpr (QKV) sfor<3>([&](auto C) { ab[C] = smem + (4 * g + qq) * 128 + ((CV(C) ^ c) * 32) + 8 * p; pin(ab[C]); });
    else sfor<8>([&](auto J) { ab[J] = smem + (4 * g + qq) * 128 + (((CV(J) >> 1) ^ c) * 32) + 8 * p + (CV(J) & 1) * (SB / 2); pin(ab[J]); });
    const unsigned ln = share * 512 + wave * 64 + l, tg16 = ln / 96;
    xto = QKV ? 0x80000000u : ((tok0 >> 4) + tg16) * 18432 + 3072 + (ln - tg16 * 96) * 128; pin(xto);
  }
  if (wave == 0) {
    if (spare) {
      sfor<4>([&](auto I) { unsigned f; const unsigned fo = FL_X + FS * (64 * I + lane); load4_sc1(f, fo, sF); flags_wait<0>(f, fo, tag, ~0ull, sF); });
      publish(tag, FL_EPOCH, sF);
    } else {
      const int fi = (tok0 >> 3) + (lane & 31);
      const unsigned fo = FL_X + FS * (fi < 0 ? 0 : fi > 255 ? 255 : fi);
      unsigned f; load4_sc1(f, fo, sF); flags_wait<0, 1>(f, fo, tag, ~0ull, sF);
    }
  }
  asm volatile("s_barrier" ::: "memory");
  STAMP(4);
  u32x4 xf[18][2], hf[21][2], hp[2], gp, t0;
  unsigned fl, sw2, cur, ooff, tch[3], tk[3][2];
  u32x2 xres[4];
  unsigned cb[3];
  auto ldtab = [&](int i) { return *(volatile LDS unsigned*)(tab + 1024 + 4 * i); };
  auto vmem = [&](auto K, auto AA, auto BB, auto NN) {
    constexpr int kind = CV(K), a = CV(AA), b = CV(BB);
    if constexpr (kind == W1) w1(AA, BB, m0b, sw);
    else if constexpr (kind == XF) {
      load16_sc1<(a / 2) * 1024>(xf[a / 2][a & 1], l16, xs[a & 1], QKV ? mk_rsrc(ws + WS_XN, xz[a & 1]) : sxn);
    } else if constexpr (kind == TOUCH) {
      if constexpr (b == 2) load4_sc1(tch[0], (l16 & 0xf0) * 8 + (l16 & 0x100) * (KSTOT * 4) + (l16 >> 9) * 0x80000000u + hst + 4096, sh);
      else if constexpr (QKV) load4(tch[0], tch[0], mk_rsrc(wA, WB));
      else if constexpr (b == 0) load4s<288 * (a % 2) * RS + (a / 2) * 128>(tch[0], tch[0], sw);
      else load4s<a * 11 * 128>(tch[1], tch[1], mk_rsrc(wdo ? A.wo : A.wd, wdo ? D * D * 2 : F * D * 2));
    } else if constexpr (kind == XRES) load8_if(xres[a], ooff, oso + (a & 1) * 16 * 1152 + (a < 2 ? 32 * b2 : 256), mk_rsrc(A.x, S * D * 2), a < 2 ? 0u : guest);
    else if constexpr (kind == W2) {
      constexpr bool isO = a == 6 || a == 7;
      constexpr int e = a < 6 ? a : a - 2, row = isO ? 64 * (a - 6) : 192 * (e % 3) + 64 * (e / 3);
      const rsrc_t r = isO ? mk_rsrc(A.wo, onw[a & 1]) : sWd;
      if constexpr (b == 0) sw2 = dma_w2a<(a % 8) * SLOT2, row * 1152>(m0c, t0[0], isO ? ow2 : ws2, r);
      else if constexpr (b == 1) dma_w2b(t0[1], sw2, r);
      else dma_w2c(t0[2], sw2, r);
    } else if constexpr (kind == H2) {
      if constexpr (a >= 18) load16_sc1<(126 + a) * 1024>(hf[a][b], l16, ho[b], mk_rsrc(ws + WS_H, onh));
      else load16_sc1<(6 * ((a % 6) / 2) + 2 * (a / 6) + (a & 1)) * 1024>(hf[a][b], l16, hb[b], sh);
    } else if constexpr (kind == SAMPLE) load4_sc1(fl, t0[3], sF);
    else if constexpr (kind == CHECK) flags_wait<CV(NN), QKV ? 8 : 2>(fl, t0[3], tag, a == 0 ? 0x111ull : a == 1 ? 0x222ull : (u64)om, sF);
    else if constexpr (kind == PUB && QKV) publish2(tag, fq[0], mk_rsrc(ws + WS_FL, fn[0]), fq[1], mk_rsrc(ws + WS_FL, fn[1]));
    else if constexpr (kind == PUB) publish(tag, (unsigned)(FL_H + ((((tok0 >> 5) + wave) * 24 + col0 / 192) * 4 + a) * FS), sF);
  };
  f32x4 acc[NCG][2], acc2[9][2];
  bf16x4 al[DA], ah[DA];
  auto aread = [&](auto N, auto T) {
    constexpr int n = CV(N);
    bf16x4 r;
    if constexpr (n >= NIT) { constexpr int o = P::lds2(P::st2(n - NIT)); r = trread(cb[o / (3 * SLOT2)], o % (3 * SLOT2) + P::ti2(n - NIT) * 32 + CV(T) * 16 * 288); }
    else if constexpr (QKV) r = trread(ab[P::jof(n) % 3], ((n / IPS) & 1) * SB + (P::jof(n) / 3) * (SB / 2) + (P::sof(n) % KS) * 4096 + CV(T) * 2048);
    else r = trread(n < 144 ? ab[P::jof(n)] : cur, (P::sof(n) % KS) * 4096 + CV(T) * 2048);
    if constexpr (CV(T) == 0) al[n % DA] = r; else ah[n % DA] = r;
  };
  auto rebase = [&](auto M) {
    constexpr int m = CV(M);
    if constexpr (m >= NIT) cb[m - NIT - 49] = v_addk<(m - NIT - 49) * 3u * SLOT2>(cb[0]);
    else if constexpr (QKV) ab[P::jof(m)] = v_addk<(m / IPS) % 4 == 0 ? (unsigned)(-2 * SB) : 2u * SB>(ab[P::jof(m)]);
    else if constexpr (m < 144) ab[P::jof(m)] = v_addk<SB>(ab[P::jof(m)]);
    else cur = v_addk<(unsigned)((((m / IPS) & 1) - 1) * SB + (P::jof(m) & 1) * (SB / 2))>(ab[P::jof(m) & ~1]);
  };
  float ee, eg, eh[2];
  const float k2 = __builtin_bit_cast(float, spin(__builtin_bit_cast(unsigned, LOG2E * LOG2E)));
  auto epi = [&](auto SU, auto E) {
    constexpr int e = CV(E), w = e / 11, ph = e % 11, p = w >> 2, th = (w >> 1) & 1, r = 2 * (w & 1) + (ph >= 4 && ph != 5);
    if constexpr (ph == 0 || ph == 4) { if constexpr (ph == 4) eh[0] = ee; ee = v_expn(acc[2 * p][th][r]); }
    if constexpr (ph == 1 || ph == 6) eg = v_mul(acc[2 * p][th][r], acc[2 * p + 1][th][r]);
    if constexpr (ph == 2 || ph == 7) ee = v_fmak(ee, k2);
    if constexpr (ph == 3 || ph == 8) ee = v_rcp(ee);
    if constexpr (ph == 5) eh[0] = v_mul(eg, eh[0]);
    if constexpr (ph == 9) eh[1] = v_mul(eg, ee);
    if constexpr (ph == 10) {
      hp[th][2 * (p & 1) + (w & 1)] = v_cvtpk(eh[0], eh[1]);
      if constexpr ((p & 1) && (w & 1)) store16_sc1<(p >> 1) * 1024>(hp[th], sh, l16, hst + (th * KSTOT + 2 * CV(SU)) * 1024);
    }
  };
  auto filler = [&](auto N, auto T) {
    constexpr int n = CV(N), t = CV(T);
    if constexpr (t == 1 && P::rebase(n + 4)) rebase(ic<n + 4>{});
    else if constexpr (!QKV) sfor<3>([&](auto SU) { constexpr int e = P::eidx(CV(SU), n, t); if constexpr (e >= 0) epi(SU, ic<e>{}); });
    GAP();
  };
  u32x2 pk[6][2];
  const unsigned stg = smem + wave * 6144 + lane * 8;
  auto epiq = [&](auto SU, auto HB, auto J, auto T) {
    constexpr int j = CV(J), t = CV(T);
    if constexpr (j >= 2 && j < 8) {
      constexpr int e = j - 2, cg = 3 * CV(HB) + (e >> 1), th = e & 1;
      pk[cg][th][t] = v_cvtpk(acc[cg][th][2 * t], acc[cg][th][2 * t + 1]);
      if constexpr (CV(SU) == 0 && t == 1) *(LDS u32x2*)(stg + LDS_K + th * 3072 + 512 * cg) = pk[cg][th];
      GAP();
    }
  };
  if (!QKV || !spare) {
    sfor<P::NPRO>([&](auto I) { vmem(ic<XF>{}, I, ic<0>{}, ic<0>{}); });
    GAP(); wait_barrier<P::NPRO - 2>(); GAP();
    load4_sc1(tch[2], XT ? xto : 0x80000000u, sxn);
    pin(tch0);
    STAMP(5);
    __builtin_amdgcn_s_setprio(PL1 - 1);
    sfor<DA - 1>([&](auto N) { aread(N, ic<0>{}); aread(N, ic<1>{}); });
    GAP();
    sfor<NIT>([&](auto N) {
      constexpr int n = CV(N), st = n / IPS, it = n % IPS, j = P::jof(n), sg = P::sof(n), su = st / SPU, c = st % SPU, m = n + DA - 1;
      constexpr bool more = m < NIT || !QKV;
      if constexpr (QKV && n == NIT - (DA - 1)) { lds_barrier(); GAP(); }
      constexpr bool early = !QKV && (st == 2 || st == 3);
      if constexpr (early && n == P::eat(st - 2, st == 2 ? 153 : 65) - 1) { wait_vm<P::wait1(n, W1, st + 1)>(); GAP(); }
      if constexpr (it == P::BAR && st < P::NST - 1) { wait_barrier<early ? 63 : P::wait1(n, W1, st + 1)>(); GAP(); }
      if constexpr (!QKV && n == NIT - (DA - 1)) { wait_barrier<cmin(P::wait1(n, W2, 3), P::wait1(n, H2, 0))>(); GAP(); }
      { constexpr int pp = (it + DA - 1) % IPS; if constexpr (pp % (IPS / PL1) == 0) { __builtin_amdgcn_s_setprio(PL1 - 1 - pp / (IPS / PL1)); GAP(); } }
      if constexpr (su == 0 && j == 0 && n > 0) { wait_vm<P::wait1(n, XF, 2 * sg + 1)>(); GAP(); }
      const bf16x8 a = cat8(al[n % DA], ah[n % DA]);
      constexpr bool swp = QKV && su == 1;
      sfor<2>([&](auto T) {
        if constexpr (more) { aread(ic<m>{}, T); GAP(); }
        const bf16x8 b = as_bf(xf[sg][T]);
        if constexpr (sg == 0) mfma_new(acc[j][T], swp ? b : a, swp ? a : b); else mfma_acc(acc[j][T], swp ? b : a, swp ? a : b);
        GAP();
        filler(N, T);
        if constexpr (QKV && c == 5 && it >= IPS / 2) epiq(ic<su>{}, ic<0>{}, ic<it - IPS / 2>{}, T);
        if constexpr (QKV && c == 0 && it < IPS / 2 && su > 0) epiq(ic<su - 1>{}, ic<1>{}, ic<it>{}, T);
      });
      if constexpr (QKV) {
        if constexpr (st == 16 && it < 12) *(LDS u32x2*)(stg + LDS_V + (it % 2) * 3072 + 512 * (it / 2)) = pk[it / 2][it % 2];
        if constexpr (n == 6 * IPS) tch[0] = ldtab(2);
        if constexpr (n == 11 * IPS) pin(tch[0]);
        if constexpr (n == 14 * IPS) t0 = *(volatile LDS u32x4*)tab;
      } else {
        if constexpr (st >= 1 && st <= 3 && it == 24) { tch[0] = ldtab(2); tch[1] = ldtab(3); }
        if constexpr (st >= 1 && st <= 3 && it == 41) { tk[st - 1][0] = tch[0]; tk[st - 1][1] = tch[1]; }
        if constexpr (n == 200 || n == 230 || n == 354) { constexpr int q = n == 200 ? 0 : n == 230 ? 1 : 2; pin(tk[q][0]); pin(tk[q][1]); }
        if constexpr (n == 320) t0 = *(volatile LDS u32x4*)tab;
        if constexpr (n == NIT - 10) cb[0] = ldtab(0);
      }
      if constexpr (n == 60) pin(tch[2]);
      constexpr Op o = P::at1(n);
      if constexpr (o.kind != NONE) vmem(ic<o.kind>{}, ic<o.a>{}, ic<o.b>{}, ic<P::wait1(n, SAMPLE, 9)>{});
      GAP();
      if constexpr (n % (NIT / 9) == NIT / 9 - 1) STAMP(6 + n / (NIT / 9));
    });
    if constexpr (QKV) {
      mfma_drain();
      sfor<18>([&](auto J) { epiq(ic<2>{}, ic<1>{}, ic<CV(J) / 2>{}, ic<CV(J) % 2>{}); });
      const int i16 = lane & 15, q4 = lane >> 4, odd = lane & 1;
      const int lo = grp == 6 ? 6 : grp < 4 ? 2 * grp : 0, hlo = grp == 6 ? 1 : 0, hhi = grp < 4 || grp == 6 ? 5 : 4;
      const bool cur0 = 4 * q4 + odd <= i16, cur1 = 4 * q4 + 2 + odd <= i16;
      const unsigned s0 = odd ? 0x01000c0cu : 0x0c0c0100u, s1 = odd ? 0x03020c0cu : 0x0c0c0302u;
      const unsigned sel[4] = {cur0 ? 0x0c0c0c0cu : s0, cur1 ? 0x0c0c0c0cu : s1, cur0 ? s0 : 0x0c0c0c0cu, cur1 ? s1 : 0x0c0c0c0cu};
      constexpr float SC = 0.23570226039551584f / LOG2E;
      auto ld2 = [&](unsigned a0, unsigned a1) { const u32x2 u = *(volatile LDS u32x2*)a0, v = *(volatile LDS u32x2*)a1; return as_bf(u32x4{u[0], u[1], v[0], v[1]}); };
      const unsigned prv0 = wave == 0 ? stg : stg - 3072;
      const float cp = t16 < 0 ? -1.0e30f : 0.f;
      unsigned ge[7], lt[7];
      sfor<7>([&](auto I) { lt[I] = (unsigned)((4 * q4 - lo - 2 * (CV(I) - 1)) >> 31); ge[I] = ~lt[I]; pin(lt[I]); pin(ge[I]); });
      float ca[4], cb[4];
      sfor<4>([&](auto I) { const bool par = odd == (CV(I) & 1); ca[I] = par && cur0 == (CV(I) < 2) ? SC : 0.f; cb[I] = par && cur1 == (CV(I) < 2) ? SC : 0.f; pin(ca[I]); pin(cb[I]); });
      f32x4 sp[10], sc[10];
      float l0[10], l1[10];
      bf16x8 pb[10], kb[5][3], vb[2][6];
      sfor<5>([&](auto H) {
        kb[H][0] = ld2(prv0 + LDS_K + 512 * H, prv0 + LDS_K + 512 * H + 512); kb[H][1] = ld2(stg + LDS_K + 512 * H, stg + LDS_K + 512 * H + 512);
        kb[H][2] = ld2(stg + LDS_K + 3072 + 512 * H, stg + LDS_K + 3072 + 512 * H + 512);
      });
      GAP();
      auto pf = [&](auto I) { constexpr Op o = P::pre2(CV(I)); GAP(); vmem(ic<o.kind>{}, ic<o.a>{}, ic<o.b>{}, ic<0>{}); GAP(); };
      pf(ic<0>{});
      sfor<5>([&](auto HH) {
        constexpr int hd = CV(HH);
        sfor<2>([&](auto TH) {
          const bf16x8 qb = as_bf(u32x4{vand(pk[hd][TH][0], ge[hd + 1]), vand(pk[hd][TH][1], ge[hd]), vand(pk[hd + 1][TH][0], lt[hd + 2]), vand(pk[hd + 1][TH][1], lt[hd + 1])});
          sp[2 * hd + TH] = CV(TH) ? mfma(kb[hd][1], qb, f32x4{cp, cp, cp, cp}) : mfma(kb[hd][0], qb, f32x4{});
          sc[2 * hd + TH] = mfma(kb[hd][1 + TH], qb, f32x4{});
        });
        pf(ic<1 + hd>{});
      });
      GAP();
      sfor<6>([&](auto J) { vb[0][J] = ld2(prv0 + LDS_V + 512 * J, stg + LDS_V + 512 * J); vb[1][J] = ld2(stg + LDS_V + 512 * J, stg + LDS_V + 3072 + 512 * J); });
      GAP();
      lds_barrier();
      float mx[10];
      sfor<10>([&](auto E) {
        l0[E] = fma4(sc[E][0], sc[E][1], sp[E][0], sp[E][1], ca);
        l1[E] = fma4(sc[E][2], sc[E][3], sp[E][2], sp[E][3], cb);
        mx[E] = vmax(l0[E], l1[E]); pin(mx[E]); pin(l0[E]); pin(l1[E]);
        if constexpr (CV(E) % 5 == 0) pf(ic<6 + CV(E) / 5>{});
      });
      GAP();
      xq10<true>(mx);
      pf(ic<8>{});
      sfor<10>([&](auto E) { l0[E] = __builtin_amdgcn_exp2f(l0[E] - mx[E]); l1[E] = __builtin_amdgcn_exp2f(l1[E] - mx[E]); mx[E] = l0[E] + l1[E]; }); GAP();
      pf(ic<9>{});
      xq10<false>(mx);
      pf(ic<10>{});
      sfor<10>([&](auto E) {
        constexpr int hd = CV(E) >> 1;
        const float inv = mx[E] * ((hd >= hlo && hd < hhi) ? 1.f / LOG2E : 0.f);
        const unsigned pp = cvtpk(l0[E] * inv, l1[E] * inv);
        pb[E] = as_bf(u32x4{__builtin_amdgcn_perm(pp, pp, sel[0]), __builtin_amdgcn_perm(pp, pp, sel[1]),
                            __builtin_amdgcn_perm(pp, pp, sel[2]), __builtin_amdgcn_perm(pp, pp, sel[3])});
      });
      pf(ic<11>{});
      sfor<10>([&](auto E) { sp[E] = mfma(vb[CV(E) & 1][CV(E) >> 1], pb[E], f32x4{}); sc[E] = mfma(vb[CV(E) & 1][(CV(E) >> 1) + 1], pb[E], f32x4{}); });
      GAP();
      wait_vm<0>();
      sfor<6>([&](auto I) { pin(hf[I][0]); pin(hf[I][1]); });
      sfor<2>([&](auto TH) {
        unsigned ow[12];
        sfor<12>([&](auto E) {
          constexpr int j = CV(E) >> 1, e = CV(E) & 1;
          unsigned a = 0u, b = 0u;
          if constexpr (j < 5) a = cvtpk(sp[2 * j + TH][2 * e], sp[2 * j + TH][2 * e + 1]);
          if constexpr (j > 0) b = cvtpk(sc[2 * j - 2 + TH][2 * e], sc[2 * j - 2 + TH][2 * e + 1]);
          ow[E] = 4 * q4 + 2 * e >= lo + 2 * j ? a : b;
        });
        const rsrc_t so = mk_rsrc(ws + WS_H, fn[TH] ? HBUF_B : 0u);
        sfor<3>([&](auto M) {
          store16_sc1<1024 * CV(M)>(u32x4{ow[4 * M], ow[4 * M + 1], ow[4 * M + 2], ow[4 * M + 3]}, so, l16, ((t16 + TH) * KSTOT + 144 + 3 * grp) * 1024);
        });
      });
      STAMP(16);
    }
  } else {
    wait_vm<0>(); pin(tch0);
    t0 = *(volatile LDS u32x4*)tab;
    vmem(ic<SAMPLE>{}, ic<0>{}, ic<0>{}, ic<0>{}); vmem(ic<CHECK>{}, ic<0>{}, ic<0>{}, ic<0>{});
    sfor<24>([&](auto I) { constexpr Op o = P::pre2(CV(I)); vmem(ic<o.kind>{}, ic<o.a>{}, ic<o.b>{}, ic<0>{}); });
    wait_vm<0>();
    sfor<6>([&](auto I) { pin(hf[I][0]); pin(hf[I][1]); });
  }
  if constexpr (QKV) {
    cb[0] = ldtab(0);
    vmem(ic<SAMPLE>{}, ic<1>{}, ic<0>{}, ic<0>{});
    GAP(); lds_barrier(); GAP();
    sfor<DA - 1>([&](auto N) { aread(ic<NIT + CV(N)>{}, ic<0>{}); aread(ic<NIT + CV(N)>{}, ic<1>{}); });
    __builtin_amdgcn_s_setprio(3);
    GAP();
    STAMP(17);
  }
  sfor<NIT2>([&](auto M) {
    constexpr int m = CV(M), n = NIT + m, st = P::st2(m), i = P::ti2(m);
    if constexpr (P::bar2(m) >= 0) { wait_barrier<P::wait2(m, W2, P::bar2(m))>(); GAP(); }
    {
      constexpr int B2 = 99, L1 = (B2 - 67 + 3) / 4, L2 = (189 - B2 + 3) / 4;
      constexpr int pr = m < 67 ? ((m + 3) % 18 == 0 ? 3 - (m + 3) / 18 : -1) : m < B2 ? ((m - 67) % L1 == 0 ? 3 - (m - 67) / L1 : -1) : ((m - B2) % L2 == 0 ? 3 - (m - B2) / L2 : -1);
      if constexpr (pr >= 0) { __builtin_amdgcn_s_setprio(pr); GAP(); }
    }
    if constexpr (P::first2(m) && st > 0) { wait_vm<P::wait2(m, H2, st)>(); GAP(); }
    const bf16x8 a = cat8(al[n % DA], ah[n % DA]);
    sfor<2>([&](auto T) {
      if constexpr (m + DA - 1 < NIT2) { aread(ic<n + DA - 1>{}, T); GAP(); }
      if constexpr (st == 0) mfma_new(acc2[i][T], a, as_bf(hf[st][T])); else mfma_acc(acc2[i][T], a, as_bf(hf[st][T]));
      GAP();
      filler(ic<n>{}, T);
      constexpr int C0 = P::TM + LT + 1;
      if constexpr (m >= C0 && m < 177 && (m - C0) % LT < 2) {
        constexpr int k = (m - C0) / LT, tl = (k + 8) % 9, th = (m - C0) % LT;
        gp[2 * th + T] = v_cvtpk(acc2[tl][th][2 * T], acc2[tl][th][2 * T + 1]);
        if constexpr (th == 1 && CV(T) == 1) store16_sc1<0>(gp, sS, l16, sl + ((k + 7) % 8) * 1024);
        GAP();
      }
    });
    if constexpr (m == 134) { ooff = ldtab(1); if constexpr (!QKV) pin(tch[0]); }
    constexpr Op o = P::at2(m);
    if constexpr (o.kind != NONE) { vmem(ic<o.kind>{}, ic<o.a>{}, ic<o.b>{}, ic<P::wait2(m, SAMPLE, 9)>{}); GAP(); }
    if constexpr (m % 27 == 26) STAMP(18 + m / 27);
  });
  unsigned fo = FL_S + (tile * 8 + wave) * 8 * FS + ((16 * (b2 + 7) - l16) & 0x70) * (FS / 16), f;
  load4_sc1(f, fo, sF);
  publish(tag, (unsigned)(FL_S + ((tile * 8 + wave) * 8 + b2) * FS), sF);
  STAMP(26);
  mfma_drain();
  sfor<4>([&](auto I) { pin(acc2[7 + CV(I) / 2][CV(I) & 1]); pin(xres[I]); });
  lane = lane_now();
  const unsigned one = (lane >> 4) == ((lane >> 2) & 3) ? (lane & 1 ? 0x3f800000u : 0x3f80u) : 0u, e0 = lane & 2 ? 0u : one, e1 = lane & 2 ? one : 0u;
  const bf16x8 elo = as_bf(u32x4{e0, e1, 0u, 0u}), ehi = as_bf(u32x4{0u, 0u, e0, e1});
  const bf16x8 xb = as_bf(u32x4{xres[0][0], xres[0][1], xres[1][0], xres[1][1]});
  f32x4 o0 = mfma(elo, xb, acc2[7][0]), o1 = mfma(ehi, xb, acc2[7][1]);
  const unsigned xr = smem + LDS_X + wave * 2048;
  unsigned tw = xr + (lane & 15) * 48 + (lane >> 4) * 8, trd = xr + (lane >> 1) * 48 + (lane & 1) * 16, tst = (lane >> 1) * 1152 + (lane & 1) * 16;
  pin(tw); pin(trd); pin(tst); pin(o0); pin(o1);
  const unsigned s0 = spin((tile * 64 + wave) * 8192);
  u32x4 pv[7], pw[7];
  gather7(pv, pw, f, fo, l16, l16 + 4096, tag, b2 + 7, s0, sS, mk_rsrc(ws + WS_SLAB, guest ? 0u : SLAB_B), sF);
  STAMP(28);
  const rsrc_t sO = mk_rsrc(A.out, S * D * 2);
  auto finish = [&](const u32x4 (&p)[7], unsigned so) {
    sfor<7>([&](auto J) { o0 = mfma(elo, as_bf(p[J]), o0); o1 = mfma(ehi, as_bf(p[J]), o1); });
    *(LDS u32x2*)tw = u32x2{cvtpk(o0[0], o0[1]), cvtpk(o0[2], o0[3])}; *(LDS u32x2*)(tw + 768) = u32x2{cvtpk(o1[0], o1[1]), cvtpk(o1[2], o1[3])};
    asm volatile("" ::: "memory");
    store16(*(LDS u32x4*)trd, sO, tst, so);
  };
  finish(pv, oso + 32 * b2);
  if (!guest) {
    const bf16x8 x8 = as_bf(u32x4{xres[2][0], xres[2][1], xres[3][0], xres[3][1]});
    o0 = mfma(elo, x8, acc2[8][0]); o1 = mfma(ehi, x8, acc2[8][1]);
    finish(pw, oso + 256);
  }
  STAMP(29);
  if constexpr (QKV) asm volatile(".Lpsk_end_a:"); else asm volatile(".Lpsk_end_b:");
}

__global__ __launch_bounds__(512, 2) void fused(Args A) {
  __shared__ __attribute__((aligned(256))) char smem_[LDS_B];
  const unsigned smem = (unsigned)(size_t)(LDS char*)smem_;
  const int wave = rfl(threadIdx.x >> 6), lane = threadIdx.x & 63, bid = blockIdx.x, xcd = bid & 7, r = bid >> 3;
  char* ws = A.ws;
  u64 tstart; asm volatile("s_memrealtime %0\n\ts_waitcnt lgkmcnt(0)" : "=s"(tstart)::"memory");
  u32x4 v; unsigned v9, ep, tc[2];
  const rsrc_t sF = mk_rsrc(ws + WS_FL, FL_B);
  {
    const rsrc_t sx = mk_rsrc(A.x, S * D * 2);
    const unsigned xo = (unsigned)((bid * 8 + wave) * 1152);
    load16(v, xo + lane * 16, sx);
    asm volatile("buffer_load_ushort %0, %1, %2, 0 offen offset:1024" : "=v"(v9) : "v"(xo + lane * 2), "s"(sx));
    load4_sc1(ep, FL_EPOCH, sF);
  }
  asm volatile("s_barrier" ::: "memory");
  const bool qkv = r >= 24;
  const int item = xcd * 8 + r - 24, t9 = item / 7, g = item - 7 * t9;
  const int tok0 = qkv ? t9 * 240 - 16 : (r / 3) * 256, col0 = qkv ? grp_col0(g) : (xcd * 3 + r % 3) * 192;
  {
    u64 pc; unsigned na, nb;
    asm volatile("s_getpc_b64 %0\n.Lpsk_begin:\n\ts_mov_b32 %1, .Lpsk_end_a-.Lpsk_begin\n\ts_mov_b32 %2, .Lpsk_end_b-.Lpsk_begin" : "=s"(pc), "=s"(na), "=s"(nb));
    const int w3 = wave & 3, q8 = tok0 >> 8;
    unsigned vo;
    if (qkv) vo = (t9 * 64 + lane) * (3 * D * 2) + col0 * 2 + (wave < 4 ? 2304 : 1152) - 94 + 94 * w3;
    else { const unsigned id = (w3 - 1) * 64 + lane; vo = pick<unsigned>(id < 72, (q8 * 72 + id) * (F * 2), pick<unsigned>(id < 108, (q8 * 36 + id - 72) * (F * 2) + 128, 0x80000000u)) + col0 * 2; }
    if (wave == 4) vo = ((r & 15) * 64 + lane) * 128;
    const unsigned nby = wave == 4 ? (na > nb ? na : nb) : w3 == 0 || item == 63 ? 0u : qkv ? D * 3 * D * 2 : D * F * 2;
    const char* base = wave == 4 ? (const char*)pc : qkv ? A.wqkv : wave < 4 ? A.wg : A.wu;
    load4(tc[0], vo, mk_rsrc(base, nby));
  }
  wait_vm<1>();
  pin(v); pin(v9); pin(ep);
  STAMP(1);
  const unsigned tag = rfl(ep) * 1664525u + 1013904223u;
  {
    float f[9];
    sfor<4>([&](auto E) { f[2 * E] = bflo(v[(int)E]); f[2 * E + 1] = bfhi(v[(int)E]); });
    f[8] = bflo(v9);
    const float mean = wave_sum(((f[0] + f[1]) + (f[2] + f[3])) + ((f[4] + f[5]) + (f[6] + f[7])) + f[8]) * (1.0f / D);
    float q = 0.f;
    sfor<9>([&](auto E) { const float d = f[E] - mean; q += d * d; });
    const float var = wave_sum(q) * (1.0f / D);
    const float mb = rbf(mean), rs = LOG2E * __builtin_amdgcn_rcpf(rbf(__builtin_amdgcn_sqrtf(rbf(rbf(var) + 1e-5f))));
    sfor<4>([&](auto E) { v[(int)E] = cvtpk((f[2 * E] - mb) * rs, (f[2 * E + 1] - mb) * rs); });
    *(LDS u32x4*)(smem + LDS_X + lane * 128 + wave * 16) = v;
    *(LDS unsigned short*)(smem + LDS_X + 8192 + (lane >> 3) * 128 + wave * 16 + (lane & 7) * 2) = (unsigned short)cvtpk((f[8] - mb) * rs, 0.f);
  }
  lds_barrier();
  STAMP(2);
  if (wave == 0) {
    u32x4 t[9];
    sfor<9>([&](auto I) { t[I] = *(LDS u32x4*)(smem + LDS_X + 1024 * I + lane * 16); });
    const unsigned o = (unsigned)((bid >> 1) * 18432 + (bid & 1) * 128 + (lane >> 3) * 256 + (lane & 7) * 16);
    const rsrc_t sxn = mk_rsrc(ws + WS_XN, XN_B);
    sfor<9>([&](auto I) { store16_sc1<0>(t[I], sxn, o, 2048 * I); });
    publish(tag, FL_X + FS * bid, sF);
    STAMP(3);
  }
  asm volatile("s_barrier" ::: "memory");
  if (!qkv) body<false>(A, smem, tag, tok0, col0, 0, r % 3, false, wave, lane, tc[0], tstart);
  else body<true>(A, smem, tag, tok0, col0, g, g, item == 63, wave, lane, tc[0], tstart);
}

}
extern "C" void kernel_launch(void* const* d_in, const int* in_sizes, int n_in, void* d_out, int out_size, void* d_ws, size_t ws_size,
                              hipStream_t stream) {
  psk::Args A;
  A.x = (const char*)d_in[0]; A.wqkv = (const char*)d_in[1]; A.wo = (const char*)d_in[2]; A.wg = (const char*)d_in[4];
  A.wu = (const char*)d_in[5]; A.wd = (const char*)d_in[6]; A.ws = (char*)d_ws; A.out = (char*)d_out;
  psk::fused<<<256, 512, 0, stream>>>(A);
}
```

```cpp
#define PADI 29
#define PADT 6000
#define PADX 5
#include <hip/hip_runtime.h>
#include <utility>
#pragma clang diagnostic ignored "-Wint-to-pointer-cast"

typedef float    f32x4  __attribute__((ext_vector_type(4)));
typedef float    f32x2  __attribute__((ext_vector_type(2)));
typedef __bf16   bf16x8 __attribute__((ext_vector_type(8)));
typedef __bf16   bf16x4 __attribute__((ext_vector_type(4)));
typedef __bf16   bf16x2 __attribute__((ext_vector_type(2)));
typedef unsigned u32x4  __attribute__((ext_vector_type(4)));
typedef unsigned u32x2  __attribute__((ext_vector_type(2)));
typedef unsigned long long u64;
typedef __amdgpu_buffer_rsrc_t rsrc_t;
#define LDS __attribute__((address_space(3)))
#define DI __device__ __forceinline__
#define GAP() __builtin_amdgcn_sched_barrier(0)
#define CV(X) (decltype(X)::value)

#define XT 0
#define NPROV 6
#define LT 8
#define PB 52
#define SM 67
namespace psk {

constexpr int S = 2048, D = 576, F = 4608;
constexpr int KSTOT = 165;
constexpr unsigned HBUF_B = 128u * KSTOT * 1024u;
constexpr unsigned SLAB_B = 32u * 8u * 8u * 8192u;
constexpr unsigned XN_B = S * D * 2u;
constexpr unsigned FS = 128;
constexpr unsigned FL_EPOCH = 0, FL_X = FS, FL_H = FL_X + 256 * FS, FL_O = FL_H + 64 * 24 * 4 * FS, FL_S = FL_O + 128 * 8 * FS, FL_B = FL_S + 64 * 8 * 4 * FS;
constexpr size_t WS_H = 0, WS_SLAB = WS_H + HBUF_B, WS_XN = WS_SLAB + SLAB_B, WS_FL = WS_XN + XN_B, WS_DBG = WS_FL + FL_B;
constexpr int SC1 = 16;
constexpr unsigned SPIN_CAP = 1u << 18;

template <int N> using ic = std::integral_constant<int, N>;
template <class Fn, int... Is> DI void sfor_impl(Fn&& f, std::integer_sequence<int, Is...>) { (f(ic<Is>{}), ...); }
template <int N, class Fn> DI void sfor(Fn&& f) { sfor_impl(f, std::make_integer_sequence<int, N>{}); }

DI unsigned rfl(unsigned x) { return __builtin_amdgcn_readfirstlane(x); }
DI rsrc_t mk_rsrc(const void* p, unsigned bytes) { return __builtin_amdgcn_make_buffer_rsrc((void*)p, (short)0, (int)bytes, 0x00020000); }
DI unsigned cvtpk(float lo, float hi) { f32x2 v = {lo, hi}; return __builtin_bit_cast(unsigned, __builtin_convertvector(v, bf16x2)); }
DI float rbf(float x) { return __builtin_bit_cast(float, cvtpk(x, x) << 16); }
DI float bflo(unsigned v) { return __builtin_bit_cast(float, v << 16); }
DI float bfhi(unsigned v) { return __builtin_bit_cast(float, v & 0xffff0000u); }
DI bf16x4 trread(unsigned addr, int off) { return __builtin_amdgcn_ds_read_tr16_b64_v4bf16((LDS bf16x4*)(addr + off)); }
DI bf16x8 cat8(bf16x4 a, bf16x4 b) { return __builtin_shufflevector(a, b, 0, 1, 2, 3, 4, 5, 6, 7); }
DI bf16x8 as_bf(u32x4 v) { return __builtin_bit_cast(bf16x8, v); }
DI f32x4 mfma(bf16x8 a, bf16x8 b, f32x4 c) { return __builtin_amdgcn_mfma_f32_16x16x32_bf16(a, b, c, 0, 0, 0); }
DI void mfma_acc(f32x4& c, bf16x8 a, bf16x8 b) { asm volatile("v_mfma_f32_16x16x32_bf16 %0, %1, %2, %0" : "+v"(c) : "v"(a), "v"(b)); }
DI void mfma_new(f32x4& c, bf16x8 a, bf16x8 b) { asm volatile("v_mfma_f32_16x16x32_bf16 %0, %1, %2, 0" : "=v"(c) : "v"(a), "v"(b)); }
DI void mfma_drain() { asm volatile("s_nop 7\n\ts_nop 1"); GAP(); }
template <class T> DI void pin(T& x) { asm volatile("" : "+v"(x)); }
DI unsigned lane_now() { unsigned l; asm volatile("v_mbcnt_lo_u32_b32 %0, -1, 0\n\tv_mbcnt_hi_u32_b32 %0, -1, %0" : "=v"(l)); return l; }
DI unsigned spin(unsigned x) { asm volatile("" : "+s"(x)); return x; }
template <int N> DI void wait_vm() { if constexpr (N < 63) asm volatile("s_waitcnt vmcnt(%0)" ::"i"(N) : "memory"); }
template <int N> DI void wait_barrier() { asm volatile("s_waitcnt vmcnt(%0) lgkmcnt(0)\n\ts_barrier" ::"i"(N) : "memory"); }
DI void lds_barrier() { asm volatile("s_waitcnt lgkmcnt(0)\n\ts_barrier" ::: "memory"); }
DI float v_mul(float a, float b) { float r; asm volatile("v_mul_f32 %0, %1, %2" : "=v"(r) : "v"(a), "v"(b)); return r; }
DI float v_expn(float a) { float r; asm volatile("v_exp_f32_e64 %0, -%1" : "=v"(r) : "v"(a)); return r; }
DI float v_fmak(float a, float k) { float r; asm volatile("v_fma_f32 %0, %1, %2, %2" : "=v"(r) : "v"(a), "s"(k)); return r; }
template <unsigned K> DI unsigned v_addk(unsigned a) { unsigned r; asm volatile("v_add_u32 %0, %2, %1" : "=v"(r) : "v"(a), "i"(K)); return r; }
DI float v_rcp(float a) { float r; asm volatile("v_rcp_f32 %0, %1" : "=v"(r) : "v"(a)); return r; }
DI unsigned v_cvtpk(float a, float b) { unsigned r; asm volatile("v_cvt_pk_bf16_f32 %0, %1, %2" : "=v"(r) : "v"(a), "v"(b)); return r; }

DI void padto(u64 t0, unsigned ticks) {
  unsigned a, b; u64 t;
  GAP();
  asm volatile("s_memrealtime %0\n\ts_waitcnt lgkmcnt(0)" : "=s"(t)::"memory");
  while ((unsigned)(t - t0) < ticks) { asm volatile("s_sleep 1\n\ts_memrealtime %0\n\ts_waitcnt lgkmcnt(0)" : "=s"(t)::"memory"); }
  (void)a; (void)b; GAP();
}
#define STAMP(i) do { if ((i) == PADI && (PADX < 0 || (blockIdx.x & 7) == PADX)) padto(tstart, PADT); } while (0)

template <int LOFF, unsigned GOFF> DI void dma16(unsigned m0base, unsigned voff, rsrc_t srd) {
  unsigned t;
  asm volatile("s_add_u32 m0, %1, %2\n\ts_mov_b32 %0, %3\n\tbuffer_load_dwordx4 %4, %5, %0 offen lds"
               : "=&s"(t) : "s"(m0base), "i"(LOFF), "i"(GOFF), "v"(voff), "s"(srd) : "scc", "memory");
}
template <int LOFF, int GOFF> DI unsigned dma_w2a(unsigned m0base, unsigned voff, unsigned sbase, rsrc_t srd) {
  unsigned so;
  asm volatile("s_add_u32 m0, %1, %2\n\ts_add_u32 %0, %5, %6\n\tbuffer_load_dwordx4 %3, %4, %0 offen lds"
               : "=&s"(so) : "s"(m0base), "i"(LOFF), "v"(voff), "s"(srd), "s"(sbase), "i"(GOFF) : "scc", "memory");
  return so;
}
DI void dma_w2b(unsigned voff, unsigned so, rsrc_t srd) { asm volatile("buffer_load_dwordx4 %0, %1, %2 offen offset:1024 lds" ::"v"(voff), "s"(srd), "s"(so) : "memory"); }
DI void dma_w2c(unsigned voff, unsigned so, rsrc_t srd) {
  asm volatile("s_mov_b64 exec, 0xffff\n\tbuffer_load_dwordx4 %0, %1, %2 offen offset:2048 lds\n\ts_mov_b64 exec, -1" ::"v"(voff), "s"(srd), "s"(so) : "memory");
}
template <int GOFF> DI void load16_sc1(u32x4& d, unsigned voff, unsigned sbase, rsrc_t srd) {
  unsigned t;
  asm volatile("s_add_u32 %1, %3, %4\n\tbuffer_load_dwordx4 %0, %2, %5, %1 offen sc1" : "=v"(d), "=&s"(t) : "v"(voff), "s"(sbase), "i"(GOFF), "s"(srd) : "scc");
}
template <int OFF> DI void store16_sc1(u32x4 d, rsrc_t srd, unsigned voff, unsigned soff) {
  asm volatile("buffer_store_dwordx4 %0, %1, %2, %3 offen offset:%4 sc1\n\ts_nop 1" ::"v"(d), "v"(voff), "s"(srd), "s"(soff), "i"(OFF) : "memory");
}
DI void store16(u32x4 d, rsrc_t srd, unsigned voff, unsigned soff) {
  asm volatile("buffer_store_dwordx4 %0, %1, %2, %3 offen sc1\n\ts_nop 1" ::"v"(d), "v"(voff), "s"(srd), "s"(soff) : "memory");
}
DI void load16(u32x4& d, unsigned voff, rsrc_t srd) { asm volatile("buffer_load_dwordx4 %0, %1, %2, 0 offen" : "=v"(d) : "v"(voff), "s"(srd)); }
DI void load4(unsigned& d, unsigned voff, rsrc_t srd) { asm volatile("buffer_load_dword %0, %1, %2, 0 offen" : "=v"(d) : "v"(voff), "s"(srd)); }
template <unsigned SOFF> DI void load4s(unsigned& d, unsigned voff, rsrc_t srd) {
  unsigned t;
  asm volatile("s_mov_b32 %1, %4\n\tbuffer_load_dword %0, %2, %3, %1 offen" : "=v"(d), "=&s"(t) : "v"(voff), "s"(srd), "i"(SOFF));
}
DI void load4_sc1(unsigned& d, unsigned voff, rsrc_t srd) { asm volatile("buffer_load_dword %0, %1, %2, 0 offen sc1" : "=v"(d) : "v"(voff), "s"(srd)); }
template <int N, int SLEEP = 2> DI void flags_wait(unsigned& f, unsigned voff, unsigned tag, u64 mask, rsrc_t srd) {
  unsigned cnt;
  asm volatile("s_waitcnt vmcnt(%6)\n\ts_mov_b32 %1, %7\n"
               "1:\n\tv_cmp_ne_u32 vcc, %3, %0\n\ts_nop 1\n\ts_and_b64 vcc, vcc, %4\n\ts_cbranch_scc0 2f\n\t"
               "s_sleep %8\n\tbuffer_load_dword %0, %2, %5, 0 offen sc1\n\ts_waitcnt vmcnt(0)\n\ts_sub_u32 %1, %1, 1\n\ts_cbranch_scc0 1b\n"
               "2:"
               : "+v"(f), "=&s"(cnt) : "v"(voff), "s"(tag), "s"(mask), "s"(srd), "i"(N), "i"(SPIN_CAP), "i"(SLEEP) : "vcc", "scc", "memory");
}
DI void publish(unsigned tag, unsigned soff, rsrc_t srd) {
  unsigned t;
  asm volatile("v_mov_b32 %0, %1\n\ts_waitcnt vmcnt(0)\n\ts_mov_b64 exec, 1\n\tbuffer_store_dword %0, off, %3, %2 sc1\n\ts_mov_b64 exec, -1"
               : "=&v"(t) : "s"(tag), "s"(soff), "s"(srd) : "memory");
}

DI void gather7(u32x4 (&p)[7], u32x4 (&q)[7], unsigned& f, unsigned fo, unsigned v0, unsigned v1, unsigned tag, unsigned src0, unsigned s0, rsrc_t sp, rsrc_t sq, rsrc_t sf) {
  unsigned pend, rdy, t, cnt;
  asm volatile("s_mov_b32 %15, 0x7f\n\ts_mov_b32 %18, %28\n"
               "1:\n\tv_cmp_eq_u32 vcc, %22, %14\n\ts_nop 1\n\ts_and_b32 %16, vcc_lo, %15\n\ts_andn2_b32 %15, %15, %16\n\t"
               "s_bitcmp1_b32 %16, 0\n\ts_cbranch_scc0 3f\n\ts_sub_u32 %17, %23, 0\n\ts_and_b32 %17, %17, 7\n\ts_lshl_b32 %17, %17, 16\n\ts_add_u32 %17, %17, %24\n\t"
               "buffer_load_dwordx4 %0, %20, %25, %17 offen offset:0 sc1\n\tbuffer_load_dwordx4 %7, %21, %26, %17 offen offset:3072 sc1\n3:\n\t"
               "s_bitcmp1_b32 %16, 1\n\ts_cbranch_scc0 3f\n\ts_sub_u32 %17, %23, 1\n\ts_and_b32 %17, %17, 7\n\ts_lshl_b32 %17, %17, 16\n\ts_add_u32 %17, %17, %24\n\t"
               "buffer_load_dwordx4 %1, %20, %25, %17 offen offset:1024 sc1\n\tbuffer_load_dwordx4 %8, %21, %26, %17 offen offset:3072 sc1\n3:\n\t"
               "s_bitcmp1_b32 %16, 2\n\ts_cbranch_scc0 3f\n\ts_sub_u32 %17, %23, 2\n\ts_and_b32 %17, %17, 7\n\ts_lshl_b32 %17, %17, 16\n\ts_add_u32 %17, %17, %24\n\t"
               "buffer_load_dwordx4 %2, %20, %25, %17 offen offset:2048 sc1\n\tbuffer_load_dwordx4 %9, %21, %26, %17 offen offset:3072 sc1\n3:\n\t"
               "s_bitcmp1_b32 %16, 3\n\ts_cbranch_scc0 3f\n\ts_sub_u32 %17, %23, 3\n\ts_and_b32 %17, %17, 7\n\ts_lshl_b32 %17, %17, 16\n\ts_add_u32 %17, %17, %24\n\t"
               "buffer_load_dwordx4 %3, %20, %25, %17 offen offset:3072 sc1\n\tbuffer_load_dwordx4 %10, %21, %26, %17 offen offset:3072 sc1\n3:\n\t"
               "s_bitcmp1_b32 %16, 4\n\ts_cbranch_scc0 3f\n\ts_sub_u32 %17, %23, 4\n\ts_and_b32 %17, %17, 7\n\ts_lshl_b32 %17, %17, 16\n\ts_add_u32 %17, %17, %24\n\t"
               "buffer_load_dwordx4 %4, %21, %25, %17 offen offset:0 sc1\n\tbuffer_load_dwordx4 %11, %21, %26, %17 offen offset:3072 sc1\n3:\n\t"
               "s_bitcmp1_b32 %16, 5\n\ts_cbranch_scc0 3f\n\ts_sub_u32 %17, %23, 5\n\ts_and_b32 %17, %17, 7\n\ts_lshl_b32 %17, %17, 16\n\ts_add_u32 %17, %17, %24\n\t"
               "buffer_load_dwordx4 %5, %21, %25, %17 offen offset:1024 sc1\n\tbuffer_load_dwordx4 %12, %21, %26, %17 offen offset:3072 sc1\n3:\n\t"
               "s_bitcmp1_b32 %16, 6\n\ts_cbranch_scc0 3f\n\ts_sub_u32 %17, %23, 6\n\ts_and_b32 %17, %17, 7\n\ts_lshl_b32 %17, %17, 16\n\ts_add_u32 %17, %17, %24\n\t"
               "buffer_load_dwordx4 %6, %21, %25, %17 offen offset:2048 sc1\n\tbuffer_load_dwordx4 %13, %21, %26, %17 offen offset:3072 sc1\n3:\n\t"
               "s_cmp_eq_u32 %15, 0\n\ts_cbranch_scc1 2f\n\ts_sleep 1\n\tbuffer_load_dword %14, %19, %27, 0 offen sc1\n\ts_waitcnt vmcnt(0)\n\t"
               "s_sub_u32 %18, %18, 1\n\ts_cbranch_scc0 1b\n2:\n\ts_waitcnt vmcnt(0)"
               : "=&v"(p[0]), "=&v"(p[1]), "=&v"(p[2]), "=&v"(p[3]), "=&v"(p[4]), "=&v"(p[5]), "=&v"(p[6]), "=&v"(q[0]), "=&v"(q[1]), "=&v"(q[2]), "=&v"(q[3]), "=&v"(q[4]), "=&v"(q[5]), "=&v"(q[6]), "+v"(f), "=&s"(pend), "=&s"(rdy), "=&s"(t), "=&s"(cnt)
               : "v"(fo), "v"(v0), "v"(v1), "s"(tag), "s"(src0), "s"(s0), "s"(sp), "s"(sq), "s"(sf), "i"(SPIN_CAP) : "vcc", "scc", "memory");
}
DI void publish2(unsigned tag, unsigned s0, rsrc_t r0, unsigned s1, rsrc_t r1) {
  unsigned t;
  asm volatile("v_mov_b32 %0, %1\n\ts_waitcnt vmcnt(0)\n\ts_mov_b64 exec, 1\n\tbuffer_store_dword %0, off, %3, %2 sc1\n\tbuffer_store_dword %0, off, %5, %4 sc1\n\ts_mov_b64 exec, -1"
               : "=&v"(t) : "s"(tag), "s"(s0), "s"(r0), "s"(s1), "s"(r1) : "memory");
}
DI void load8_if(u32x2& d, unsigned voff, unsigned soff, rsrc_t srd, unsigned off) {
  asm volatile("s_cmp_lg_u32 %4, 0\n\ts_cbranch_scc1 1f\n\tbuffer_load_dwordx2 %0, %1, %2, %3 offen\n1:" : "=v"(d) : "v"(voff), "s"(srd), "s"(soff), "s"(off) : "scc");
}

template <class T> DI T pick(bool c, T a, T b) { return c ? a : b; }
DI unsigned vand(unsigned a, unsigned b) { return a & b; }
DI float fma4(float a, float b, float c, float d, const float (&k)[4]) {
  float r = a * k[0]; pin(r); r = __builtin_fmaf(b, k[1], r); r = __builtin_fmaf(c, k[2], r); return __builtin_fmaf(d, k[3], r);
}
DI float vmax(float a, float b) { float r; asm("v_max_f32 %0, %1, %2" : "=v"(r) : "v"(a), "v"(b)); return r; }
template <bool MAX> DI float x16(float v) {
  const unsigned u = __builtin_bit_cast(unsigned, v);
  auto a = __builtin_amdgcn_permlane16_swap(u, u, false, false);
  const float p = __builtin_bit_cast(float, (unsigned)a[0]), q = __builtin_bit_cast(float, (unsigned)a[1]);
  return MAX ? vmax(p, q) : p + q;
}
template <bool MAX> DI float x32(float v) {
  const unsigned u = __builtin_bit_cast(unsigned, v);
  auto a = __builtin_amdgcn_permlane32_swap(u, u, false, false);
  const float p = __builtin_bit_cast(float, (unsigned)a[0]), q = __builtin_bit_cast(float, (unsigned)a[1]);
  return MAX ? vmax(p, q) : p + q;
}
template <int W> DI void swp(float& a, float& b) {
  const unsigned x = __builtin_bit_cast(unsigned, a), y = __builtin_bit_cast(unsigned, b);
  if constexpr (W == 16) { auto r = __builtin_amdgcn_permlane16_swap(x, y, false, false); a = __builtin_bit_cast(float, (unsigned)r[0]); b = __builtin_bit_cast(float, (unsigned)r[1]); }
  else { auto r = __builtin_amdgcn_permlane32_swap(x, y, false, false); a = __builtin_bit_cast(float, (unsigned)r[0]); b = __builtin_bit_cast(float, (unsigned)r[1]); }
}
template <bool MAX> DI void xq10(float (&m)[10]) {
  auto op = [](float p, float q) { return MAX ? vmax(p, q) : p + q; };
  float a[6], c[3], d[6];
  sfor<5>([&](auto I) { swp<16>(m[2 * I], m[2 * I + 1]); }); GAP();
  sfor<5>([&](auto I) { a[I] = op(m[2 * I], m[2 * I + 1]); pin(a[I]); }); a[5] = a[4]; pin(a[5]); GAP();
  sfor<3>([&](auto I) { swp<32>(a[2 * I], a[2 * I + 1]); }); GAP();
  sfor<3>([&](auto I) { c[I] = op(a[2 * I], a[2 * I + 1]); if constexpr (!MAX) c[I] = __builtin_amdgcn_rcpf(c[I]); pin(c[I]); }); GAP();
  sfor<2>([&](auto I) { d[2 * I] = c[I]; d[2 * I + 1] = c[I]; pin(d[2 * I + 1]); }); d[4] = c[2]; GAP();
  sfor<2>([&](auto I) { swp<32>(d[2 * I], d[2 * I + 1]); }); GAP();
  sfor<5>([&](auto I) { m[2 * I] = d[I]; m[2 * I + 1] = d[I]; pin(m[2 * I + 1]); }); GAP();
  sfor<5>([&](auto I) { swp<16>(m[2 * I], m[2 * I + 1]); }); GAP();
}
template <int CTRL> DI float dpp_add(float v) { return v + __builtin_bit_cast(float, __builtin_amdgcn_update_dpp(0, __builtin_bit_cast(int, v), CTRL, 0xf, 0xf, true)); }
DI float wave_sum(float v) { return x32<false>(x16<false>(dpp_add<0x140>(dpp_add<0x141>(dpp_add<0x4e>(dpp_add<0xb1>(v)))))); }
__host__ __device__ constexpr int grp_col0(int g) { return 8 * (int)((0x3C362D21160B00ull >> (8 * g)) & 0xff); }

constexpr int LDS_V = 2 * 24576, LDS_K = 4 * 24576;
constexpr int LDS_X = 6 * 24576, LDS_B = LDS_X + 16384;
constexpr int SLOT2 = 64 * 288, NIT2 = 21 * 9, DA = 4;
constexpr float LOG2E = 1.4426950408889634f;
constexpr int cmin(int a, int b) { return a < b ? a : b; }

enum { NONE, W1, XF, TOUCH, W2, H2, SAMPLE,   CHECK, PUB, XRES };
struct Op { int kind, a, b; };
template <bool QKV> struct Plan {
  static constexpr int NCG = QKV ? 6 : 8, KS = QKV ? 3 : 9, NST = 54 / KS, SPU = 18 / KS, NSLOT = QKV ? 4 : 2;
  static constexpr int SB = KS * 8192, IPS = KS * NCG, NIT = NST * IPS, BAR = IPS - (DA - 1), NPRO = NPROV, NPRE2 = 24;
  static constexpr bool kmajor(int n) { return QKV ? ((n / IPS) % 6 != 5 && (n < 6 * IPS || (n / IPS) % 6 != 0)) : n < 96; }
  static constexpr int jof(int n) { return kmajor(n) ? n % NCG : (!QKV && n < 144) ? (n - 96) / 6 : (n % IPS) / KS; }
  static constexpr int sof(int n) { return kmajor(n) ? (n % (18 * NCG)) / NCG : (!QKV && n < 144) ? 12 + n % 6 : KS * ((n / IPS) % SPU) + n % KS; }
  static constexpr bool rebase(int m) {
    if (m >= NIT) return m - NIT == 50 || m - NIT == 51;
    if (QKV) return m >= IPS && (m / IPS) % 2 == 0 && (kmajor(m) ? m % IPS < 3 : (m % IPS) % 3 == 0 && m % IPS < 9);
    return m < 144 ? (m >= 72 && m < 80) : m % 9 == 0;
  }
  static constexpr int TM = 174 - 8 * LT;
  static constexpr int st2(int m) { return m < 54 ? m / 9 : m < TM ? 6 + (m - 54) / 8 : m < 174 ? 21 - LT + (m - TM) % LT : 6 + m - 174; }
  static constexpr int ti2(int m) { return m < 54 ? m % 9 : m < 174 ? ((m < TM ? (m - 54) % 8 : (m - TM) / LT) + 8) % 9 : 7; }
  static constexpr bool first2(int m) { return m < 54 ? m % 9 == 0 : m < TM ? (m - 54) % 8 == 0 : m < TM + LT; }
  static constexpr int lds2(int k) { return (k < 12 ? k : k < 18 ? k - 12 : k - 6) * (SLOT2 / 2); }
  static constexpr int bar2(int m) { return m == 67 ? 7 : m == 99 ? 10 : -1; }
  static constexpr int estart(int su) { return su == 0 ? 108 : 144 * su + 92; }
  static constexpr int eidx(int su, int n, int t) {
    if (n < estart(su) || n > estart(su) + 110 || (t == 1 && rebase(n + 4))) return -1;
    int c = 0;
    for (int i = estart(su); i < n; ++i) c += rebase(i + 4) ? 1 : 2;
    return c + t < 176 ? c + t : -1;
  }
  static constexpr int eat(int su, int e) { for (int n = estart(su);; ++n) if (eidx(su, n, 0) == e || eidx(su, n, 1) == e) return n; }
  static constexpr Op pre2(int i) { return i < 12 ? Op{W2, i / 3, i % 3} : Op{H2, (i - 12) / 2, i & 1}; }
  static constexpr Op at1(int n) {
    const int st = n / IPS, it = n % IPS;
    if (QKV) {
      if (n % 2 == 0 && NPRO + n / 2 < 36) return Op{XF, NPRO + n / 2, 0};
      if (st > 0 && it < 6 && it % 2 == 1 && st + 3 < NST) return Op{W1, st + 3, it / 2};
      if (st == 6 && it == 9) return Op{TOUCH, 0, 0};
      if (st == 14 && it == 6) return Op{SAMPLE, 0, 0};
      if (st == 15 && it == 12) return Op{CHECK, 0, 0};
      if (n > 15 * IPS + 12 && n % 3 == 1 && (n - 15 * IPS - 13) / 3 < 12) return pre2(12 + (n - 15 * IPS - 13) / 3);
      return Op{NONE, 0, 0};
    }
    if (n % 3 == 0 && NPRO + n / 3 < 36) return Op{XF, NPRO + n / 3, 0};
    if (st >= 1 && st <= 4) { const int i0 = st == 3 ? 10 : 1; if (it >= i0 && (it - i0) % 3 == 0 && (it - i0) / 3 < 9) return Op{W1, st + 1, (it - i0) / 3}; }
    if (n == 224) return Op{PUB, 0, 0};
    if (st >= 1 && st <= 3 && it == (st == 3 ? 37 : 31)) return Op{TOUCH, st + 2, 0};
    if (st >= 1 && st <= 3 && it == (st == 3 ? 39 : 33)) return Op{TOUCH, st - 1, 1};
    if (n == 330) return Op{SAMPLE, 0, 0};
    if (n == 352) return Op{PUB, 1, 0};
    if (n == 353) return Op{CHECK, 0, 0};
    if (n == 406) return Op{SAMPLE, 1, 0};
    if (n == 431) return Op{CHECK, 1, 0};
    if (n >= 358 && n % 5 < 4 && n % 5 != 1 && n % 5 != 2) { const int e = (n - 358) / 5 * 2 + ((n - 358) % 5 ? 1 : 0); if (e < 24) return pre2(e); }
    return Op{NONE, 0, 0};
  }
  static constexpr Op at2(int m) {
    if (QKV) {
      if (m == 12) return Op{PUB, 3, 0};
      if (m == 13) return Op{CHECK, 1, 0};
      if (m >= 14 && m < 56 && m % 7 != 2 && m % 7 != 4 && m % 7 != 6) {
        const int e = (m - 14) / 7 * 2 + (m % 7 > 1); return (m % 7 == 0 || m % 7 == 3) ? Op{H2, 6 + e / 2, e & 1} : Op{W2, 4 + e / 3, e % 3};
      }
    } else if (m < 42 && m % 7 != 2 && m % 7 != 4 && m % 7 != 6) {
      const int e = m / 7 * 2 + (m % 7 > 1); return (m % 7 == 0 || m % 7 == 3) ? Op{H2, 6 + e / 2, e & 1} : Op{W2, 4 + e / 3, e % 3};
    }
    if (!QKV && m == PB) return Op{PUB, 2, 0};
    if (!QKV && m == PB + 1) return Op{TOUCH, 0, 2};
    if (m == SM) return Op{SAMPLE, 2, 0};
    if (m >= 68 && m < 86 && m % 2 == 0) return Op{W2, 8 + (m - 68) / 6, ((m - 68) / 2) % 3};
    if (m == SM + 20) return Op{CHECK, 2, 0};
    if (m > SM + 20 && m <= SM + 38) return Op{H2, 12 + (m - SM - 21) / 2, (m - SM - 21) & 1};
    if (m >= 140 && m < 148 && m % 2 == 0) return Op{XRES, (m - 140) / 2, 0};
    return Op{NONE, 0, 0};
  }
  static constexpr int wait1(int n0, int kind, int amax) {
    int cnt = 0, need = -1;
    for (int i = -NPRO - 1; i < n0; ++i) {
      const Op o = i < -1 ? Op{XF, i + NPRO + 1, 0} : i < 0 ? Op{TOUCH, 9, 0} : at1(i);
      if (o.kind == PUB) need = -1;
      if (o.kind == NONE || o.kind > SAMPLE) continue;
      ++cnt;
      if (o.kind == kind && o.a <= amax) need = cnt;
    }
    return need < 0 || cnt - need > 62 ? 63 : cnt - need;
  }
  static constexpr int wait2(int m0, int kind, int amax) {
    int cnt = 0, need = -1;
    for (int i = -NPRE2; i < m0; ++i) {
      const Op o = i < 0 ? pre2(i + NPRE2) : at2(i);
      if (o.kind == PUB) need = -1;
      if (o.kind == NONE || o.kind > SAMPLE) continue;
      ++cnt;
      if (o.kind == kind && o.a <= amax) need = cnt;
    }
    return need < 0 || cnt - need > 62 ? 63 : cnt - need;
  }
};
using PF = Plan<false>;
static_assert(PF::eat(0, 175) < 224 - 16 && PF::eat(1, 175) < 352 - 16 && PF::eat(2, 175) < PF::NIT + PB - 8, "a unit is published after its last store");
static_assert(PF::eat(0, 44) >= 96 + 26 && PF::eat(0, 88) >= 96 + 38 && PF::eat(0, 132) >= 96 + 50, "unit 0: accumulators complete before they are read");
static_assert(PF::eat(0, 39) < 144 && PF::eat(0, 83) < 162 && PF::eat(0, 127) < 180 && PF::eat(0, 171) < 198, "unit 0: accumulators read before they restart");
static_assert(PF::eat(1, 44) >= 216 + 38 && PF::eat(1, 88) >= 216 + 56 && PF::eat(1, 132) >= 216 + 74 && PF::eat(1, 171) < 288 + 54, "unit 1");
static_assert(PF::eat(0, 153) > 144 + 30 && PF::eat(0, 153) < 213 && PF::eat(1, 65) > 216 + 40 && PF::eat(1, 65) < 285, "early weight waits follow the last request");
static_assert(PF::eat(2, 44) >= 360 + 38 && PF::eat(2, 88) >= 360 + 56 && PF::eat(2, 132) >= 360 + 74, "unit 2");

struct Args { const char* x; const char* wqkv; const char* wo; const char* wg; const char* wu; const char* wd; char* ws; char* out; };

template <bool QKV>
DI void body(const Args& A, unsigned smem, unsigned tag, int tok0, int col0, int grp, int share, bool spare, int wave, int lane, unsigned& tch0, u64 tstart) {
  using P = Plan<QKV>;
  constexpr int NCG = P::NCG, KS = P::KS, NSLOT = P::NSLOT, IPS = P::IPS, NIT = P::NIT, SPU = P::SPU, SB = P::SB;
  constexpr int PL1 = QKV ? 3 : 4;
  constexpr unsigned RS = QKV ? 3u * D * 2u : F * 2u, WB = QKV ? D * 3 * D * 2 : D * F * 2;
  char* ws = A.ws;
  const int bid = blockIdx.x, isU = wave >> 2, w3 = wave & 3;
  const rsrc_t sF = mk_rsrc(ws + WS_FL, FL_B), sxn = mk_rsrc(ws + WS_XN, XN_B);
  unsigned wv;
  {
    const int cgl = ((lane & 7) >> 1) ^ ((lane >> 4) & 3);
    wv = (unsigned)((16 * (w3 & 1) + 4 * (w3 >> 1) + 8 * (lane >> 5) + ((lane >> 3) & 3)) * RS + col0 * 2 + (QKV && cgl == 3 ? 2 : cgl) * 32 + (lane & 1) * 16);
  }
  const char* wA = QKV ? A.wqkv : A.wg;
  const char* wB = QKV ? A.wqkv + 96 : A.wu;
  const rsrc_t sw = mk_rsrc(isU ? wB : wA, QKV && isU ? WB - 96 : WB);
  const unsigned m0b = smem + isU * (SB / 2) + w3 * 1024;
  auto w1 = [&](auto ST, auto B, unsigned m0, rsrc_t srd) {
    constexpr int a = CV(ST), b = CV(B), su = a / SPU;
    constexpr unsigned sucol = QKV ? (su == 0 ? 1152u : su == 1 ? 2304u : 0u) : su * 128u;
    dma16<(a % NSLOT) * SB + b * 4096, 32 * (KS * (a % SPU) + b) * RS + sucol>(m0, wv, srd);
  };
  if (isU && !spare) {
    const rsrc_t sa = mk_rsrc(wA, WB);
    sfor<NSLOT>([&](auto ST) { sfor<KS>([&](auto B) { w1(ST, B, m0b - SB / 2, sa); w1(ST, B, m0b, sw); }); });
  }
  const int t16 = (tok0 >> 4) + 2 * wave;
  const int b2 = bid & 7, tile = bid >> 3, rt2 = QKV ? tile - 24 : tok0 >> 8, nt2 = QKV ? 3 : tile - 3 * rt2, u16 = rt2 * 16 + 2 * wave;
  const rsrc_t sh = mk_rsrc(ws + WS_H, HBUF_B), sWd = mk_rsrc(A.wd, F * D * 2), sS = mk_rsrc(ws + WS_SLAB, SLAB_B);
  const unsigned m0c = spin(smem + wave * 2304), ws2 = spin(576 * b2 * 1152);
  const unsigned hb[2] = {spin((u16 * KSTOT + 18 * b2) * 1024), spin(((u16 + 1) * KSTOT + 18 * b2) * 1024)};
  const unsigned ho[2] = {spin(hb[0] - 15 * 1024 * b2), spin(hb[1] - 15 * 1024 * b2)};
  const unsigned xs[2] = {spin(t16 * 18432), spin(t16 * 18432 + 18432)}, xz[2] = {spin(t16 >= 0 && t16 < 128 ? XN_B : 0u), spin(t16 + 1 < 128 ? XN_B : 0u)};
  const unsigned oso = spin(u16 * 16 * 1152 + nt2 * 288), hst = spin((t16 * KSTOT + (col0 >> 5)) * 1024);
  const unsigned guest = spin(wave ^ b2), sl = spin(((tile * 8 + b2) * 8 + wave) * 8192);
  const unsigned ow2 = spin(grp_col0(b2) * 1152), onh = spin(b2 < 7 ? HBUF_B : 0u);
  const unsigned onw[2] = {spin(b2 < 7 ? D * D * 2 : 0), spin(b2 < 7 && wave < 4 ? D * D * 2 : 0)};
  const unsigned om = spin(b2 < 7 ? 0x3444 : 0x444);
  const unsigned fq[2] = {spin(FL_O + (t16 * 8 + grp) * FS), spin(FL_O + (t16 * 8 + 8 + grp) * FS)};
  const unsigned fn[2] = {spin(wave > 0 && t16 < 128 ? FL_B : 0u), spin(t16 + 1 < 128 ? FL_B : 0u)};
  const unsigned l16 = lane * 16, tab = smem + LDS_X + wave * 2048 + l16;
  const bool wdo = (bid >> 3) * 8 + wave >= 162;
  unsigned ab[NCG], xto;
  {
    const unsigned l = lane;
    u32x4 t0, t1;
    sfor<3>([&](auto I) { const unsigned n = 64 * I + l, r = n / 18, c = n - 18 * r;
      t0[(int)I] = (8 * wave + r) * 1152 + nt2 * 288 + pick<unsigned>(c < 16, (c + 2 * b2 + 2) & 15, c) * 16 - 1024 * I; });
    const unsigned fa = FL_H + (((rt2 * 8 + wave) * 24 + 3 * b2) * 4 + l) * FS, fb = FL_O + ((u16 + (l & 1)) * 8 + b2) * FS;
    t0[3] = pick<unsigned>(l < 12, fa, fb);
    t1[0] = smem + (l >> 2) * 288 + (l & 3) * 8;
    t1[1] = (l & 15) * 1152 + (l >> 4) * 8;
    if constexpr (QKV) { t1[2] = (((tok0 + 16) / 240) * 64 + l) * RS + col0 * 2 + (!isU && w3 < 3 ? 94 * w3 : 0x80000000u); t1[3] = 0; }
    else {
      t1[2] = pick<unsigned>(l < 9, ((tok0 >> 8) * 36 + w3 * 9 + l) * RS + col0 * 2, 0x80000000u);
      t1[3] = pick<unsigned>(l < 11, (wdo ? ow2 - 162 * 4096 : ws2) + ((bid >> 3) * 8 + wave) * 4096 + l * 128, 0x80000000u);
    }
    *(LDS u32x4*)tab = t0; *(LDS u32x4*)(tab + 1024) = t1;
    const int g = lane >> 4, qq = (lane >> 2) & 3, p = lane & 3, c = (2 * g + (qq >> 1)) & 3;
    if constexpr (QKV) sfor<3>([&](auto C) { ab[C] = smem + (4 * g + qq) * 128 + ((CV(C) ^ c) * 32) + 8 * p; pin(ab[C]); });
    else sfor<8>([&](auto J) { ab[J] = smem + (4 * g + qq) * 128 + (((CV(J) >> 1) ^ c) * 32) + 8 * p + (CV(J) & 1) * (SB / 2); pin(ab[J]); });
    const unsigned ln = share * 512 + wave * 64 + l, tg16 = ln / 96;
    xto = QKV ? 0x80000000u : ((tok0 >> 4) + tg16) * 18432 + 3072 + (ln - tg16 * 96) * 128; pin(xto);
  }
  if (wave == 0) {
    if (spare) {
      sfor<4>([&](auto I) { unsigned f; const unsigned fo = FL_X + FS * (64 * I + lane); load4_sc1(f, fo, sF); flags_wait<0>(f, fo, tag, ~0ull, sF); });
      publish(tag, FL_EPOCH, sF);
    } else {
      const int fi = (tok0 >> 3) + (lane & 31);
      const unsigned fo = FL_X + FS * (fi < 0 ? 0 : fi > 255 ? 255 : fi);
      unsigned f; load4_sc1(f, fo, sF); flags_wait<0, 1>(f, fo, tag, ~0ull, sF);
    }
  }
  asm volatile("s_barrier" ::: "memory");
  STAMP(4);
  u32x4 xf[18][2], hf[21][2], hp[2], gp, t0;
  unsigned fl, sw2, cur, ooff, tch[3], tk[3][2];
  u32x2 xres[4];
  unsigned cb[3];
  auto ldtab = [&](int i) { return *(volatile LDS unsigned*)(tab + 1024 + 4 * i); };
  auto vmem = [&](auto K, auto AA, auto BB, auto NN) {
    constexpr int kind = CV(K), a = CV(AA), b = CV(BB);
    if constexpr (kind == W1) w1(AA, BB, m0b, sw);
    else if constexpr (kind == XF) {
      load16_sc1<(a / 2) * 1024>(xf[a / 2][a & 1], l16, xs[a & 1], QKV ? mk_rsrc(ws + WS_XN, xz[a & 1]) : sxn);
    } else if constexpr (kind == TOUCH) {
      if constexpr (b == 2) load4_sc1(tch[0], (l16 & 0xf0) * 8 + (l16 & 0x100) * (KSTOT * 4) + (l16 >> 9) * 0x80000000u + hst + 4096, sh);
      else if constexpr (QKV) load4(tch[0], tch[0], mk_rsrc(wA, WB));
      else if constexpr (b == 0) load4s<288 * (a % 2) * RS + (a / 2) * 128>(tch[0], tch[0], sw);
      else load4s<a * 11 * 128>(tch[1], tch[1], mk_rsrc(wdo ? A.wo : A.wd, wdo ? D * D * 2 : F * D * 2));
    } else if constexpr (kind == XRES) load8_if(xres[a], ooff, oso + (a & 1) * 16 * 1152 + (a < 2 ? 32 * b2 : 256), mk_rsrc(A.x, S * D * 2), a < 2 ? 0u : guest);
    else if constexpr (kind == W2) {
      constexpr bool isO = a == 6 || a == 7;
      constexpr int e = a < 6 ? a : a - 2, row = isO ? 64 * (a - 6) : 192 * (e % 3) + 64 * (e / 3);
      const rsrc_t r = isO ? mk_rsrc(A.wo, onw[a & 1]) : sWd;
      if constexpr (b == 0) sw2 = dma_w2a<(a % 8) * SLOT2, row * 1152>(m0c, t0[0], isO ? ow2 : ws2, r);
      else if constexpr (b == 1) dma_w2b(t0[1], sw2, r);
      else dma_w2c(t0[2], sw2, r);
    } else if constexpr (kind == H2) {
      if constexpr (a >= 18) load16_sc1<(126 + a) * 1024>(hf[a][b], l16, ho[b], mk_rsrc(ws + WS_H, onh));
      else load16_sc1<(6 * ((a % 6) / 2) + 2 * (a / 6) + (a & 1)) * 1024>(hf[a][b], l16, hb[b], sh);
    } else if constexpr (kind == SAMPLE) load4_sc1(fl, t0[3], sF);
    else if constexpr (kind == CHECK) flags_wait<CV(NN), QKV ? 8 : 2>(fl, t0[3], tag, a == 0 ? 0x111ull : a == 1 ? 0x222ull : (u64)om, sF);
    else if constexpr (kind == PUB && QKV) publish2(tag, fq[0], mk_rsrc(ws + WS_FL, fn[0]), fq[1], mk_rsrc(ws + WS_FL, fn[1]));
    else if constexpr (kind == PUB) publish(tag, (unsigned)(FL_H + ((((tok0 >> 5) + wave) * 24 + col0 / 192) * 4 + a) * FS), sF);
  };
  f32x4 acc[NCG][2], acc2[9][2];
  bf16x4 al[DA], ah[DA];
  auto aread = [&](auto N, auto T) {
    constexpr int n = CV(N);
    bf16x4 r;
    if constexpr (n >= NIT) { constexpr int o = P::lds2(P::st2(n - NIT)); r = trread(cb[o / (3 * SLOT2)], o % (3 * SLOT2) + P::ti2(n - NIT) * 32 + CV(T) * 16 * 288); }
    else if constexpr (QKV) r = trread(ab[P::jof(n) % 3], ((n / IPS) & 1) * SB + (P::jof(n) / 3) * (SB / 2) + (P::sof(n) % KS) * 4096 + CV(T) * 2048);
    else r = trread(n < 144 ? ab[P::jof(n)] : cur, (P::sof(n) % KS) * 4096 + CV(T) * 2048);
    if constexpr (CV(T) == 0) al[n % DA] = r; else ah[n % DA] = r;
  };
  auto rebase = [&](auto M) {
    constexpr int m = CV(M);
    if constexpr (m >= NIT) cb[m - NIT - 49] = v_addk<(m - NIT - 49) * 3u * SLOT2>(cb[0]);
    else if constexpr (QKV) ab[P::jof(m)] = v_addk<(m / IPS) % 4 == 0 ? (unsigned)(-2 * SB) : 2u * SB>(ab[P::jof(m)]);
    else if constexpr (m < 144) ab[P::jof(m)] = v_addk<SB>(ab[P::jof(m)]);
    else cur = v_addk<(unsigned)((((m / IPS) & 1) - 1) * SB + (P::jof(m) & 1) * (SB / 2))>(ab[P::jof(m) & ~1]);
  };
  float ee, eg, eh[2];
  const float k2 = __builtin_bit_cast(float, spin(__builtin_bit_cast(unsigned, LOG2E * LOG2E)));
  auto epi = [&](auto SU, auto E) {
    constexpr int e = CV(E), w = e / 11, ph = e % 11, p = w >> 2, th = (w >> 1) & 1, r = 2 * (w & 1) + (ph >= 4 && ph != 5);
    if constexpr (ph == 0 || ph == 4) { if constexpr (ph == 4) eh[0] = ee; ee = v_expn(acc[2 * p][th][r]); }
    if constexpr (ph == 1 || ph == 6) eg = v_mul(acc[2 * p][th][r], acc[2 * p + 1][th][r]);
    if constexpr (ph == 2 || ph == 7) ee = v_fmak(ee, k2);
    if constexpr (ph == 3 || ph == 8) ee = v_rcp(ee);
    if constexpr (ph == 5) eh[0] = v_mul(eg, eh[0]);
    if constexpr (ph == 9) eh[1] = v_mul(eg, ee);
    if constexpr (ph == 10) {
      hp[th][2 * (p & 1) + (w & 1)] = v_cvtpk(eh[0], eh[1]);
      if constexpr ((p & 1) && (w & 1)) store16_sc1<(p >> 1) * 1024>(hp[th], sh, l16, hst + (th * KSTOT + 2 * CV(SU)) * 1024);
    }
  };
  auto filler = [&](auto N, auto T) {
    constexpr int n = CV(N), t = CV(T);
    if constexpr (t == 1 && P::rebase(n + 4)) rebase(ic<n + 4>{});
    else if constexpr (!QKV) sfor<3>([&](auto SU) { constexpr int e = P::eidx(CV(SU), n, t); if constexpr (e >= 0) epi(SU, ic<e>{}); });
    GAP();
  };
  u32x2 pk[6][2];
  const unsigned stg = smem + wave * 6144 + lane * 8;
  auto epiq = [&](auto SU, auto HB, auto J, auto T) {
    constexpr int j = CV(J), t = CV(T);
    if constexpr (j >= 2 && j < 8) {
      constexpr int e = j - 2, cg = 3 * CV(HB) + (e >> 1), th = e & 1;
      pk[cg][th][t] = v_cvtpk(acc[cg][th][2 * t], acc[cg][th][2 * t + 1]);
      if constexpr (CV(SU) == 0 && t == 1) *(LDS u32x2*)(stg + LDS_K + th * 3072 + 512 * cg) = pk[cg][th];
      GAP();
    }
  };
  if (!QKV || !spare) {
    sfor<P::NPRO>([&](auto I) { vmem(ic<XF>{}, I, ic<0>{}, ic<0>{}); });
    GAP(); wait_barrier<P::NPRO - 2>(); GAP();
    load4_sc1(tch[2], XT ? xto : 0x80000000u, sxn);
    pin(tch0);
    STAMP(5);
    __builtin_amdgcn_s_setprio(PL1 - 1);
    sfor<DA - 1>([&](auto N) { aread(N, ic<0>{}); aread(N, ic<1>{}); });
    GAP();
    sfor<NIT>([&](auto N) {
      constexpr int n = CV(N), st = n / IPS, it = n % IPS, j = P::jof(n), sg = P::sof(n), su = st / SPU, c = st % SPU, m = n + DA - 1;
      constexpr bool more = m < NIT || !QKV;
      if constexpr (QKV && n == NIT - (DA - 1)) { lds_barrier(); GAP(); }
      constexpr bool early = !QKV && (st == 2 || st == 3);
      if constexpr (early && n == P::eat(st - 2, st == 2 ? 153 : 65) - 1) { wait_vm<P::wait1(n, W1, st + 1)>(); GAP(); }
      if constexpr (it == P::BAR && st < P::NST - 1) { wait_barrier<early ? 63 : P::wait1(n, W1, st + 1)>(); GAP(); }
      if constexpr (!QKV && n == NIT - (DA - 1)) { wait_barrier<cmin(P::wait1(n, W2, 3), P::wait1(n, H2, 0))>(); GAP(); }
      { constexpr int pp = (it + DA - 1) % IPS; if constexpr (pp % (IPS / PL1) == 0) { __builtin_amdgcn_s_setprio(PL1 - 1 - pp / (IPS / PL1)); GAP(); } }
      if constexpr (su == 0 && j == 0 && n > 0) { wait_vm<P::wait1(n, XF, 2 * sg + 1)>(); GAP(); }
      const bf16x8 a = cat8(al[n % DA], ah[n % DA]);
      constexpr bool swp = QKV && su == 1;
      sfor<2>([&](auto T) {
        if constexpr (more) { aread(ic<m>{}, T); GAP(); }
        const bf16x8 b = as_bf(xf[sg][T]);
        if constexpr (sg == 0) mfma_new(acc[j][T], swp ? b : a, swp ? a : b); else mfma_acc(acc[j][T], swp ? b : a, swp ? a : b);
        GAP();
        filler(N, T);
        if constexpr (QKV && c == 5 && it >= IPS / 2) epiq(ic<su>{}, ic<0>{}, ic<it - IPS / 2>{}, T);
        if constexpr (QKV && c == 0 && it < IPS / 2 && su > 0) epiq(ic<su - 1>{}, ic<1>{}, ic<it>{}, T);
      });
      if constexpr (QKV) {
        if constexpr (st == 16 && it < 12) *(LDS u32x2*)(stg + LDS_V + (it % 2) * 3072 + 512 * (it / 2)) = pk[it / 2][it % 2];
        if constexpr (n == 6 * IPS) tch[0] = ldtab(2);
        if constexpr (n == 11 * IPS) pin(tch[0]);
        if constexpr (n == 14 * IPS) t0 = *(volatile LDS u32x4*)tab;
      } else {
        if constexpr (st >= 1 && st <= 3 && it == 24) { tch[0] = ldtab(2); tch[1] = ldtab(3); }
        if constexpr (st >= 1 && st <= 3 && it == 41) { tk[st - 1][0] = tch[0]; tk[st - 1][1] = tch[1]; }
        if constexpr (n == 200 || n == 230 || n == 354) { constexpr int q = n == 200 ? 0 : n == 230 ? 1 : 2; pin(tk[q][0]); pin(tk[q][1]); }
        if constexpr (n == 320) t0 = *(volatile LDS u32x4*)tab;
        if constexpr (n == NIT - 10) cb[0] = ldtab(0);
      }
      if constexpr (n == 60) pin(tch[2]);
      constexpr Op o = P::at1(n);
      if constexpr (o.kind != NONE) vmem(ic<o.kind>{}, ic<o.a>{}, ic<o.b>{}, ic<P::wait1(n, SAMPLE, 9)>{});
      GAP();
      if constexpr (n % (NIT / 9) == NIT / 9 - 1) STAMP(6 + n / (NIT / 9));
    });
    if constexpr (QKV) {
      mfma_drain();
      sfor<18>([&](auto J) { epiq(ic<2>{}, ic<1>{}, ic<CV(J) / 2>{}, ic<CV(J) % 2>{}); });
      const int i16 = lane & 15, q4 = lane >> 4, odd = lane & 1;
      const int lo = grp == 6 ? 6 : grp < 4 ? 2 * grp : 0, hlo = grp == 6 ? 1 : 0, hhi = grp < 4 || grp == 6 ? 5 : 4;
      const bool cur0 = 4 * q4 + odd <= i16, cur1 = 4 * q4 + 2 + odd <= i16;
      const unsigned s0 = odd ? 0x01000c0cu : 0x0c0c0100u, s1 = odd ? 0x03020c0cu : 0x0c0c0302u;
      const unsigned sel[4] = {cur0 ? 0x0c0c0c0cu : s0, cur1 ? 0x0c0c0c0cu : s1, cur0 ? s0 : 0x0c0c0c0cu, cur1 ? s1 : 0x0c0c0c0cu};
      constexpr float SC = 0.23570226039551584f / LOG2E;
      auto ld2 = [&](unsigned a0, unsigned a1) { const u32x2 u = *(volatile LDS u32x2*)a0, v = *(volatile LDS u32x2*)a1; return as_bf(u32x4{u[0], u[1], v[0], v[1]}); };
      const unsigned prv0 = wave == 0 ? stg : stg - 3072;
      const float cp = t16 < 0 ? -1.0e30f : 0.f;
      unsigned ge[7], lt[7];
      sfor<7>([&](auto I) { lt[I] = (unsigned)((4 * q4 - lo - 2 * (CV(I) - 1)) >> 31); ge[I] = ~lt[I]; pin(lt[I]); pin(ge[I]); });
      float ca[4], cb[4];
      sfor<4>([&](auto I) { const bool par = odd == (CV(I) & 1); ca[I] = par && cur0 == (CV(I) < 2) ? SC : 0.f; cb[I] = par && cur1 == (CV(I) < 2) ? SC : 0.f; pin(ca[I]); pin(cb[I]); });
      f32x4 sp[10], sc[10];
      float l0[10], l1[10];
      bf16x8 pb[10], kb[5][3], vb[2][6];
      sfor<5>([&](auto H) {
        kb[H][0] = ld2(prv0 + LDS_K + 512 * H, prv0 + LDS_K + 512 * H + 512); kb[H][1] = ld2(stg + LDS_K + 512 * H, stg + LDS_K + 512 * H + 512);
        kb[H][2] = ld2(stg + LDS_K + 3072 + 512 * H, stg + LDS_K + 3072 + 512 * H + 512);
      });
      GAP();
      auto pf = [&](auto I) { constexpr Op o = P::pre2(CV(I)); GAP(); vmem(ic<o.kind>{}, ic<o.a>{}, ic<o.b>{}, ic<0>{}); GAP(); };
      pf(ic<0>{});
      sfor<5>([&](auto HH) {
        constexpr int hd = CV(HH);
        sfor<2>([&](auto TH) {
          const bf16x8 qb = as_bf(u32x4{vand(pk[hd][TH][0], ge[hd + 1]), vand(pk[hd][TH][1], ge[hd]), vand(pk[hd + 1][TH][0], lt[hd + 2]), vand(pk[hd + 1][TH][1], lt[hd + 1])});
          sp[2 * hd + TH] = CV(TH) ? mfma(kb[hd][1], qb, f32x4{cp, cp, cp, cp}) : mfma(kb[hd][0], qb, f32x4{});
          sc[2 * hd + TH] = mfma(kb[hd][1 + TH], qb, f32x4{});
        });
        pf(ic<1 + hd>{});
      });
      GAP();
      sfor<6>([&](auto J) { vb[0][J] = ld2(prv0 + LDS_V + 512 * J, stg + LDS_V + 512 * J); vb[1][J] = ld2(stg + LDS_V + 512 * J, stg + LDS_V + 3072 + 512 * J); });
      GAP();
      lds_barrier();
      float mx[10];
      sfor<10>([&](auto E) {
        l0[E] = fma4(sc[E][0], sc[E][1], sp[E][0], sp[E][1], ca);
        l1[E] = fma4(sc[E][2], sc[E][3], sp[E][2], sp[E][3], cb);
        mx[E] = vmax(l0[E], l1[E]); pin(mx[E]); pin(l0[E]); pin(l1[E]);
        if constexpr (CV(E) % 5 == 0) pf(ic<6 + CV(E) / 5>{});
      });
      GAP();
      xq10<true>(mx);
      pf(ic<8>{});
      sfor<10>([&](auto E) { l0[E] = __builtin_amdgcn_exp2f(l0[E] - mx[E]); l1[E] = __builtin_amdgcn_exp2f(l1[E] - mx[E]); mx[E] = l0[E] + l1[E]; }); GAP();
      pf(ic<9>{});
      xq10<false>(mx);
      pf(ic<10>{});
      sfor<10>([&](auto E) {
        constexpr int hd = CV(E) >> 1;
        const float inv = mx[E] * ((hd >= hlo && hd < hhi) ? 1.f / LOG2E : 0.f);
        const unsigned pp = cvtpk(l0[E] * inv, l1[E] * inv);
        pb[E] = as_bf(u32x4{__builtin_amdgcn_perm(pp, pp, sel[0]), __builtin_amdgcn_perm(pp, pp, sel[1]),
                            __builtin_amdgcn_perm(pp, pp, sel[2]), __builtin_amdgcn_perm(pp, pp, sel[3])});
      });
      pf(ic<11>{});
      sfor<10>([&](auto E) { sp[E] = mfma(vb[CV(E) & 1][CV(E) >> 1], pb[E], f32x4{}); sc[E] = mfma(vb[CV(E) & 1][(CV(E) >> 1) + 1], pb[E], f32x4{}); });
      GAP();
      wait_vm<0>();
      sfor<6>([&](auto I) { pin(hf[I][0]); pin(hf[I][1]); });
      sfor<2>([&](auto TH) {
        unsigned ow[12];
        sfor<12>([&](auto E) {
          constexpr int j = CV(E) >> 1, e = CV(E) & 1;
          unsigned a = 0u, b = 0u;
          if constexpr (j < 5) a = cvtpk(sp[2 * j + TH][2 * e], sp[2 * j + TH][2 * e + 1]);
          if constexpr (j > 0) b = cvtpk(sc[2 * j - 2 + TH][2 * e], sc[2 * j - 2 + TH][2 * e + 1]);
          ow[E] = 4 * q4 + 2 * e >= lo + 2 * j ? a : b;
        });
        const rsrc_t so = mk_rsrc(ws + WS_H, fn[TH] ? HBUF_B : 0u);
        sfor<3>([&](auto M) {
          store16_sc1<1024 * CV(M)>(u32x4{ow[4 * M], ow[4 * M + 1], ow[4 * M + 2], ow[4 * M + 3]}, so, l16, ((t16 + TH) * KSTOT + 144 + 3 * grp) * 1024);
        });
      });
      STAMP(16);
    }
  } else {
    wait_vm<0>(); pin(tch0);
    t0 = *(volatile LDS u32x4*)tab;
    vmem(ic<SAMPLE>{}, ic<0>{}, ic<0>{}, ic<0>{}); vmem(ic<CHECK>{}, ic<0>{}, ic<0>{}, ic<0>{});
    sfor<24>([&](auto I) { constexpr Op o = P::pre2(CV(I)); vmem(ic<o.kind>{}, ic<o.a>{}, ic<o.b>{}, ic<0>{}); });
    wait_vm<0>();
    sfor<6>([&](auto I) { pin(hf[I][0]); pin(hf[I][1]); });
  }
  if constexpr (QKV) {
    cb[0] = ldtab(0);
    vmem(ic<SAMPLE>{}, ic<1>{}, ic<0>{}, ic<0>{});
    GAP(); lds_barrier(); GAP();
    sfor<DA - 1>([&](auto N) { aread(ic<NIT + CV(N)>{}, ic<0>{}); aread(ic<NIT + CV(N)>{}, ic<1>{}); });
    __builtin_amdgcn_s_setprio(3);
    GAP();
    STAMP(17);
  }
  sfor<NIT2>([&](auto M) {
    constexpr int m = CV(M), n = NIT + m, st = P::st2(m), i = P::ti2(m);
    if constexpr (P::bar2(m) >= 0) { wait_barrier<P::wait2(m, W2, P::bar2(m))>(); GAP(); }
    {
      constexpr int B2 = 99, L1 = (B2 - 67 + 3) / 4, L2 = (189 - B2 + 3) / 4;
      constexpr int pr = m < 67 ? ((m + 3) % 18 == 0 ? 3 - (m + 3) / 18 : -1) : m < B2 ? ((m - 67) % L1 == 0 ? 3 - (m - 67) / L1 : -1) : ((m - B2) % L2 == 0 ? 3 - (m - B2) / L2 : -1);
      if constexpr (pr >= 0) { __builtin_amdgcn_s_setprio(pr); GAP(); }
    }
    if constexpr (P::first2(m) && st > 0) { wait_vm<P::wait2(m, H2, st)>(); GAP(); }
    const bf16x8 a = cat8(al[n % DA], ah[n % DA]);
    sfor<2>([&](auto T) {
      if constexpr (m + DA - 1 < NIT2) { aread(ic<n + DA - 1>{}, T); GAP(); }
      if constexpr (st == 0) mfma_new(acc2[i][T], a, as_bf(hf[st][T])); else mfma_acc(acc2[i][T], a, as_bf(hf[st][T]));
      GAP();
      filler(ic<n>{}, T);
      constexpr int C0 = P::TM + LT + 1;
      if constexpr (m >= C0 && m < 177 && (m - C0) % LT < 2) {
        constexpr int k = (m - C0) / LT, tl = (k + 8) % 9, th = (m - C0) % LT;
        gp[2 * th + T] = v_cvtpk(acc2[tl][th][2 * T], acc2[tl][th][2 * T + 1]);
        if constexpr (th == 1 && CV(T) == 1) store16_sc1<0>(gp, sS, l16, sl + ((k + 7) % 8) * 1024);
        GAP();
      }
    });
    if constexpr (m == 134) { ooff = ldtab(1); if constexpr (!QKV) pin(tch[0]); }
    constexpr Op o = P::at2(m);
    if constexpr (o.kind != NONE) { vmem(ic<o.kind>{}, ic<o.a>{}, ic<o.b>{}, ic<P::wait2(m, SAMPLE, 9)>{}); GAP(); }
    if constexpr (m % 27 == 26) STAMP(18 + m / 27);
  });
  unsigned fo = FL_S + (tile * 8 + wave) * 8 * FS + ((16 * (b2 + 7) - l16) & 0x70) * (FS / 16), f;
  load4_sc1(f, fo, sF);
  publish(tag, (unsigned)(FL_S + ((tile * 8 + wave) * 8 + b2) * FS), sF);
  STAMP(26);
  mfma_drain();
  sfor<4>([&](auto I) { pin(acc2[7 + CV(I) / 2][CV(I) & 1]); pin(xres[I]); });
  lane = lane_now();
  const unsigned one = (lane >> 4) == ((lane >> 2) & 3) ? (lane & 1 ? 0x3f800000u : 0x3f80u) : 0u, e0 = lane & 2 ? 0u : one, e1 = lane & 2 ? one : 0u;
  const bf16x8 elo = as_bf(u32x4{e0, e1, 0u, 0u}), ehi = as_bf(u32x4{0u, 0u, e0, e1});
  const bf16x8 xb = as_bf(u32x4{xres[0][0], xres[0][1], xres[1][0], xres[1][1]});
  f32x4 o0 = mfma(elo, xb, acc2[7][0]), o1 = mfma(ehi, xb, acc2[7][1]);
  const unsigned xr = smem + LDS_X + wave * 2048;
  unsigned tw = xr + (lane & 15) * 48 + (lane >> 4) * 8, trd = xr + (lane >> 1) * 48 + (lane & 1) * 16, tst = (lane >> 1) * 1152 + (lane & 1) * 16;
  pin(tw); pin(trd); pin(tst); pin(o0); pin(o1);
  const unsigned s0 = spin((tile * 64 + wave) * 8192);
  u32x4 pv[7], pw[7];
  gather7(pv, pw, f, fo, l16, l16 + 4096, tag, b2 + 7, s0, sS, mk_rsrc(ws + WS_SLAB, guest ? 0u : SLAB_B), sF);
  STAMP(28);
  const rsrc_t sO = mk_rsrc(A.out, S * D * 2);
  auto finish = [&](const u32x4 (&p)[7], unsigned so) {
    sfor<7>([&](auto J) { o0 = mfma(elo, as_bf(p[J]), o0); o1 = mfma(ehi, as_bf(p[J]), o1); });
    *(LDS u32x2*)tw = u32x2{cvtpk(o0[0], o0[1]), cvtpk(o0[2], o0[3])}; *(LDS u32x2*)(tw + 768) = u32x2{cvtpk(o1[0], o1[1]), cvtpk(o1[2], o1[3])};
    asm volatile("" ::: "memory");
    store16(*(LDS u32x4*)trd, sO, tst, so);
  };
  finish(pv, oso + 32 * b2);
  if (!guest) {
    const bf16x8 x8 = as_bf(u32x4{xres[2][0], xres[2][1], xres[3][0], xres[3][1]});
    o0 = mfma(elo, x8, acc2[8][0]); o1 = mfma(ehi, x8, acc2[8][1]);
    finish(pw, oso + 256);
  }
  STAMP(29);
  if constexpr (QKV) asm volatile(".Lpsk_end_a:"); else asm volatile(".Lpsk_end_b:");
}

__global__ __launch_bounds__(512, 2) void fused(Args A) {
  __shared__ __attribute__((aligned(256))) char smem_[LDS_B];
  const unsigned smem = (unsigned)(size_t)(LDS char*)smem_;
  const int wave = rfl(threadIdx.x >> 6), lane = threadIdx.x & 63, bid = blockIdx.x, xcd = bid & 7, r = bid >> 3;
  char* ws = A.ws;
  u64 tstart; asm volatile("s_memrealtime %0\n\ts_waitcnt lgkmcnt(0)" : "=s"(tstart)::"memory");
  u32x4 v; unsigned v9, ep, tc[2];
  const rsrc_t sF = mk_rsrc(ws + WS_FL, FL_B);
  {
    const rsrc_t sx = mk_rsrc(A.x, S * D * 2);
    const unsigned xo = (unsigned)((bid * 8 + wave) * 1152);
    load16(v, xo + lane * 16, sx);
    asm volatile("buffer_load_ushort %0, %1, %2, 0 offen offset:1024" : "=v"(v9) : "v"(xo + lane * 2), "s"(sx));
    load4_sc1(ep, FL_EPOCH, sF);
  }
  asm volatile("s_barrier" ::: "memory");
  const bool qkv = r >= 24;
  const int item = xcd * 8 + r - 24, t9 = item / 7, g = item - 7 * t9;
  const int tok0 = qkv ? t9 * 240 - 16 : (r / 3) * 256, col0 = qkv ? grp_col0(g) : (xcd * 3 + r % 3) * 192;
  {
    u64 pc; unsigned na, nb;
    asm volatile("s_getpc_b64 %0\n.Lpsk_begin:\n\ts_mov_b32 %1, .Lpsk_end_a-.Lpsk_begin\n\ts_mov_b32 %2, .Lpsk_end_b-.Lpsk_begin" : "=s"(pc), "=s"(na), "=s"(nb));
    const int w3 = wave & 3, q8 = tok0 >> 8;
    unsigned vo;
    if (qkv) vo = (t9 * 64 + lane) * (3 * D * 2) + col0 * 2 + (wave < 4 ? 2304 : 1152) - 94 + 94 * w3;
    else { const unsigned id = (w3 - 1) * 64 + lane; vo = pick<unsigned>(id < 72, (q8 * 72 + id) * (F * 2), pick<unsigned>(id < 108, (q8 * 36 + id - 72) * (F * 2) + 128, 0x80000000u)) + col0 * 2; }
    if (wave == 4) vo = ((r & 15) * 64 + lane) * 128;
    const unsigned nby = wave == 4 ? (na > nb ? na : nb) : w3 == 0 || item == 63 ? 0u : qkv ? D * 3 * D * 2 : D * F * 2;
    const char* base = wave == 4 ? (const char*)pc : qkv ? A.wqkv : wave < 4 ? A.wg : A.wu;
    load4(tc[0], vo, mk_rsrc(base, nby));
  }
  wait_vm<1>();
  pin(v); pin(v9); pin(ep);
  STAMP(1);
  const unsigned tag = rfl(ep) * 1664525u + 1013904223u;
  {
    float f[9];
    sfor<4>([&](auto E) { f[2 * E] = bflo(v[(int)E]); f[2 * E + 1] = bfhi(v[(int)E]); });
    f[8] = bflo(v9);
    const float mean = wave_sum(((f[0] + f[1]) + (f[2] + f[3])) + ((f[4] + f[5]) + (f[6] + f[7])) + f[8]) * (1.0f / D);
    float q = 0.f;
    sfor<9>([&](auto E) { const float d = f[E] - mean; q += d * d; });
    const float var = wave_sum(q) * (1.0f / D);
    const float mb = rbf(mean), rs = LOG2E * __builtin_amdgcn_rcpf(rbf(__builtin_amdgcn_sqrtf(rbf(rbf(var) + 1e-5f))));
    sfor<4>([&](auto E) { v[(int)E] = cvtpk((f[2 * E] - mb) * rs, (f[2 * E + 1] - mb) * rs); });
    *(LDS u32x4*)(smem + LDS_X + lane * 128 + wave * 16) = v;
    *(LDS unsigned short*)(smem + LDS_X + 8192 + (lane >> 3) * 128 + wave * 16 + (lane & 7) * 2) = (unsigned short)cvtpk((f[8] - mb) * rs, 0.f);
  }
  lds_barrier();
  STAMP(2);
  if (wave == 0) {
    u32x4 t[9];
    sfor<9>([&](auto I) { t[I] = *(LDS u32x4*)(smem + LDS_X + 1024 * I + lane * 16); });
    const unsigned o = (unsigned)((bid >> 1) * 18432 + (bid & 1) * 128 + (lane >> 3) * 256 + (lane & 7) * 16);
    const rsrc_t sxn = mk_rsrc(ws + WS_XN, XN_B);
    sfor<9>([&](auto I) { store16_sc1<0>(t[I], sxn, o, 2048 * I); });
    publish(tag, FL_X + FS * bid, sF);
    STAMP(3);
  }
  asm volatile("s_barrier" ::: "memory");
  if (!qkv) body<false>(A, smem, tag, tok0, col0, 0, r % 3, false, wave, lane, tc[0], tstart);
  else body<true>(A, smem, tag, tok0, col0, g, g, item == 63, wave, lane, tc[0], tstart);
}

}
extern "C" void kernel_launch(void* const* d_in, const int* in_sizes, int n_in, void* d_out, int out_size, void* d_ws, size_t ws_size,
                              hipStream_t stream) {
  psk::Args A;
  A.x = (const char*)d_in[0]; A.wqkv = (const char*)d_in[1]; A.wo = (const char*)d_in[2]; A.wg = (const char*)d_in[4];
  A.wu = (const char*)d_in[5]; A.wd = (const char*)d_in[6]; A.ws = (char*)d_ws; A.out = (char*)d_out;
  psk::fused<<<256, 512, 0, stream>>>(A);
}
```

```cpp
#include <hip/hip_runtime.h>
#include <utility>
#pragma clang diagnostic ignored "-Wint-to-pointer-cast"

typedef float    f32x4  __attribute__((ext_vector_type(4)));
typedef float    f32x2  __attribute__((ext_vector_type(2)));
typedef __bf16   bf16x8 __attribute__((ext_vector_type(8)));
typedef __bf16   bf16x4 __attribute__((ext_vector_type(4)));
typedef __bf16   bf16x2 __attribute__((ext_vector_type(2)));
typedef unsigned u32x4  __attribute__((ext_vector_type(4)));
typedef unsigned u32x2  __attribute__((ext_vector_type(2)));
typedef unsigned long long u64;
typedef __amdgpu_buffer_rsrc_t rsrc_t;
#define LDS __attribute__((address_space(3)))
#define DI __device__ __forceinline__
#define GAP() __builtin_amdgcn_sched_barrier(0)
#define CV(X) (decltype(X)::value)

namespace psk {

constexpr int S = 2048, D = 576, F = 4608;
constexpr int KSTOT = 168;
constexpr unsigned HBUF_B = 128u * KSTOT * 1024u;
constexpr unsigned SLAB_W = 12288, SLAB_B = 32u * 8u * 8u * SLAB_W;
constexpr unsigned XN_B = S * D * 2u, HALO_B = 64u * 5120u;
constexpr unsigned FS = 128;
constexpr unsigned FL_EPOCH = 0, FL_X = FS, FL_H = FL_X + 256 * FS, FL_S = FL_H + 72 * 96 * FS, FL_B = FL_S + 32 * 64 * FS;
constexpr size_t WS_H = 0, WS_SLAB = WS_H + HBUF_B, WS_XN = WS_SLAB + SLAB_B, WS_HALO = WS_XN + XN_B, WS_FL = WS_HALO + HALO_B, WS_DBG = WS_FL + FL_B;
constexpr unsigned SPIN_CAP = 1u << 18;

template <int N> using ic = std::integral_constant<int, N>;
template <class Fn, int... Is> DI void sfor_impl(Fn&& f, std::integer_sequence<int, Is...>) { (f(ic<Is>{}), ...); }
template <int N, class Fn> DI void sfor(Fn&& f) { sfor_impl(f, std::make_integer_sequence<int, N>{}); }

DI unsigned rfl(unsigned x) { return __builtin_amdgcn_readfirstlane(x); }
DI rsrc_t mk_rsrc(const void* p, unsigned bytes) { return __builtin_amdgcn_make_buffer_rsrc((void*)p, (short)0, (int)bytes, 0x00020000); }
DI unsigned cvtpk(float lo, float hi) { f32x2 v = {lo, hi}; return __builtin_bit_cast(unsigned, __builtin_convertvector(v, bf16x2)); }
DI float rbf(float x) { return __builtin_bit_cast(float, cvtpk(x, x) << 16); }
DI float bflo(unsigned v) { return __builtin_bit_cast(float, v << 16); }
DI float bfhi(unsigned v) { return __builtin_bit_cast(float, v & 0xffff0000u); }
DI bf16x4 trread(unsigned addr, int off) { return __builtin_amdgcn_ds_read_tr16_b64_v4bf16((LDS bf16x4*)(addr + off)); }
DI bf16x8 cat8(bf16x4 a, bf16x4 b) { return __builtin_shufflevector(a, b, 0, 1, 2, 3, 4, 5, 6, 7); }
DI bf16x8 as_bf(u32x4 v) { return __builtin_bit_cast(bf16x8, v); }
DI f32x4 mfma(bf16x8 a, bf16x8 b, f32x4 c) { return __builtin_amdgcn_mfma_f32_16x16x32_bf16(a, b, c, 0, 0, 0); }
DI void mfma_acc(f32x4& c, bf16x8 a, bf16x8 b) { asm volatile("v_mfma_f32_16x16x32_bf16 %0, %1, %2, %0" : "+v"(c) : "v"(a), "v"(b)); }
DI void mfma_new(f32x4& c, bf16x8 a, bf16x8 b) { asm volatile("v_mfma_f32_16x16x32_bf16 %0, %1, %2, 0" : "=v"(c) : "v"(a), "v"(b)); }
DI void mfma_drain() { asm volatile("s_nop 7\n\ts_nop 1"); GAP(); }
template <class T> DI void pin(T& x) { asm volatile("" : "+v"(x)); }
template <class T> DI void keep(T x) { asm volatile("" ::"v"(x)); }
DI unsigned lane_now() { unsigned l; asm volatile("v_mbcnt_lo_u32_b32 %0, -1, 0\n\tv_mbcnt_hi_u32_b32 %0, -1, %0" : "=v"(l)); return l; }
DI unsigned spin(unsigned x) { asm volatile("" : "+s"(x)); return x; }
template <int N> DI void wait_vm() { if constexpr (N < 63) asm volatile("s_waitcnt vmcnt(%0)" ::"i"(N) : "memory"); }
template <int N> DI void wait_barrier() { asm volatile("s_waitcnt vmcnt(%0) lgkmcnt(0)\n\ts_barrier" ::"i"(N) : "memory"); }
DI void lds_barrier() { asm volatile("s_waitcnt lgkmcnt(0)\n\ts_barrier" ::: "memory"); }
DI float v_mul(float a, float b) { float r; asm volatile("v_mul_f32 %0, %1, %2" : "=v"(r) : "v"(a), "v"(b)); return r; }
DI float v_expn(float a) { float r; asm volatile("v_exp_f32_e64 %0, -%1" : "=v"(r) : "v"(a)); return r; }
DI float v_fmak(float a, float k) { float r; asm volatile("v_fma_f32 %0, %1, %2, %2" : "=v"(r) : "v"(a), "s"(k)); return r; }
DI unsigned v_xors(unsigned a, unsigned k) { unsigned r; asm volatile("v_xor_b32 %0, %2, %1" : "=v"(r) : "v"(a), "s"(k)); return r; }
template <unsigned K> DI unsigned v_addk(unsigned a) { unsigned r; asm volatile("v_add_u32 %0, %2, %1" : "=v"(r) : "v"(a), "i"(K)); return r; }
DI float v_rcp(float a) { float r; asm volatile("v_rcp_f32 %0, %1" : "=v"(r) : "v"(a)); return r; }
DI unsigned v_cvtpk(float a, float b) { unsigned r; asm volatile("v_cvt_pk_bf16_f32 %0, %1, %2" : "=v"(r) : "v"(a), "v"(b)); return r; }

#define STAMP(i) do {} while (0)

template <int LOFF, unsigned GOFF> DI void dma16(unsigned m0base, unsigned voff, rsrc_t srd) {
  unsigned t;
  asm volatile("s_add_u32 m0, %1, %2\n\ts_mov_b32 %0, %3\n\tbuffer_load_dwordx4 %4, %5, %0 offen lds"
               : "=&s"(t) : "s"(m0base), "i"(LOFF), "i"(GOFF), "v"(voff), "s"(srd) : "scc", "memory");
}
template <int GOFF> DI void load16_sc1(u32x4& d, unsigned voff, unsigned sbase, rsrc_t srd) {
  unsigned t;
  asm volatile("s_add_u32 %1, %3, %4\n\tbuffer_load_dwordx4 %0, %2, %5, %1 offen sc1" : "=v"(d), "=&s"(t) : "v"(voff), "s"(sbase), "i"(GOFF), "s"(srd) : "scc");
}
template <int OFF> DI void store16_sc1(u32x4 d, rsrc_t srd, unsigned voff, unsigned soff) {
  asm volatile("buffer_store_dwordx4 %0, %1, %2, %3 offen offset:%4 sc1\n\ts_nop 1" ::"v"(d), "v"(voff), "s"(srd), "s"(soff), "i"(OFF) : "memory");
}
DI void store16(u32x4 d, rsrc_t srd, unsigned voff, unsigned soff) {
  asm volatile("buffer_store_dwordx4 %0, %1, %2, %3 offen sc1\n\ts_nop 1" ::"v"(d), "v"(voff), "s"(srd), "s"(soff) : "memory");
}
DI void load16(u32x4& d, unsigned voff, rsrc_t srd) { asm volatile("buffer_load_dwordx4 %0, %1, %2, 0 offen" : "=v"(d) : "v"(voff), "s"(srd)); }
DI void load4(unsigned& d, unsigned voff, rsrc_t srd) { asm volatile("buffer_load_dword %0, %1, %2, 0 offen" : "=v"(d) : "v"(voff), "s"(srd)); }
template <unsigned SOFF> DI void load4s(unsigned& d, unsigned voff, rsrc_t srd) {
  unsigned t;
  asm volatile("s_mov_b32 %1, %4\n\tbuffer_load_dword %0, %2, %3, %1 offen" : "=v"(d), "=&s"(t) : "v"(voff), "s"(srd), "i"(SOFF));
}
DI void load4_sc1(unsigned& d, unsigned voff, rsrc_t srd) { asm volatile("buffer_load_dword %0, %1, %2, 0 offen sc1" : "=v"(d) : "v"(voff), "s"(srd)); }
template <int N, int SLEEP = 2> DI void flags_wait(unsigned& f, unsigned voff, unsigned tag, u64 mask, rsrc_t srd) {
  unsigned cnt;
  asm volatile("s_waitcnt vmcnt(%6)\n\ts_mov_b32 %1, %7\n"
               "1:\n\tv_cmp_ne_u32 vcc, %3, %0\n\ts_nop 1\n\ts_and_b64 vcc, vcc, %4\n\ts_cbranch_scc0 2f\n\t"
               "s_sleep %8\n\tbuffer_load_dword %0, %2, %5, 0 offen sc1\n\ts_waitcnt vmcnt(0)\n\ts_sub_u32 %1, %1, 1\n\ts_cbranch_scc0 1b\n"
               "2:"
               : "+v"(f), "=&s"(cnt) : "v"(voff), "s"(tag), "s"(mask), "s"(srd), "i"(N), "i"(SPIN_CAP), "i"(SLEEP) : "vcc", "scc", "memory");
}
DI void publish(unsigned tag, unsigned soff, rsrc_t srd) {
  unsigned t;
  asm volatile("v_mov_b32 %0, %1\n\ts_waitcnt vmcnt(0)\n\ts_mov_b64 exec, 1\n\tbuffer_store_dword %0, off, %3, %2 sc1\n\ts_mov_b64 exec, -1"
               : "=&v"(t) : "s"(tag), "s"(soff), "s"(srd) : "memory");
}

template <bool TWO> DI void gather7(u32x4 (&p)[7], u32x4 (&q)[8], unsigned& f, unsigned fo, unsigned v0, unsigned v1, unsigned tag, unsigned src0, unsigned s0, rsrc_t sp, rsrc_t sq, rsrc_t sf) {
  unsigned pend, rdy, t, cnt;
  if constexpr (TWO) {
#define X(R) "buffer_load_dwordx4 " R ", %20, %26, %17 offen sc1\n"
  asm volatile("s_mov_b32 %15, 0x7f\n\ts_mov_b32 %18, %28\n"
               "1:\n\tv_cmp_eq_u32 vcc, %22, %14\n\ts_nop 1\n\ts_and_b32 %16, vcc_lo, %15\n\ts_andn2_b32 %15, %15, %16\n\t"
               "s_bitcmp1_b32 %16, 0\n\ts_cbranch_scc0 3f\n\ts_sub_u32 %17, %23, 0\n\ts_and_b32 %17, %17, 7\n\ts_mul_i32 %17, %17, %29\n\ts_add_u32 %17, %17, %24\n\t"
               "buffer_load_dwordx4 %0, %20, %25, %17 offen offset:0 sc1\n\t" X("%7") "3:\n\t"
               "s_bitcmp1_b32 %16, 1\n\ts_cbranch_scc0 3f\n\ts_sub_u32 %17, %23, 1\n\ts_and_b32 %17, %17, 7\n\ts_mul_i32 %17, %17, %29\n\ts_add_u32 %17, %17, %24\n\t"
               "buffer_load_dwordx4 %1, %20, %25, %17 offen offset:1024 sc1\n\t" X("%8") "3:\n\t"
               "s_bitcmp1_b32 %16, 2\n\ts_cbranch_scc0 3f\n\ts_sub_u32 %17, %23, 2\n\ts_and_b32 %17, %17, 7\n\ts_mul_i32 %17, %17, %29\n\ts_add_u32 %17, %17, %24\n\t"
               "buffer_load_dwordx4 %2, %20, %25, %17 offen offset:2048 sc1\n\t" X("%9") "3:\n\t"
               "s_bitcmp1_b32 %16, 3\n\ts_cbranch_scc0 3f\n\ts_sub_u32 %17, %23, 3\n\ts_and_b32 %17, %17, 7\n\ts_mul_i32 %17, %17, %29\n\ts_add_u32 %17, %17, %24\n\t"
               "buffer_load_dwordx4 %3, %20, %25, %17 offen offset:3072 sc1\n\t" X("%10") "3:\n\t"
               "s_bitcmp1_b32 %16, 4\n\ts_cbranch_scc0 3f\n\ts_sub_u32 %17, %23, 4\n\ts_and_b32 %17, %17, 7\n\ts_mul_i32 %17, %17, %29\n\ts_add_u32 %17, %17, %24\n\t"
               "buffer_load_dwordx4 %4, %21, %25, %17 offen offset:0 sc1\n\t" X("%11") "3:\n\t"
               "s_bitcmp1_b32 %16, 5\n\ts_cbranch_scc0 3f\n\ts_sub_u32 %17, %23, 5\n\ts_and_b32 %17, %17, 7\n\ts_mul_i32 %17, %17, %29\n\ts_add_u32 %17, %17, %24\n\t"
               "buffer_load_dwordx4 %5, %21, %25, %17 offen offset:1024 sc1\n\t" X("%12") "3:\n\t"
               "s_bitcmp1_b32 %16, 6\n\ts_cbranch_scc0 3f\n\ts_sub_u32 %17, %23, 6\n\ts_and_b32 %17, %17, 7\n\ts_mul_i32 %17, %17, %29\n\ts_add_u32 %17, %17, %24\n\t"
               "buffer_load_dwordx4 %6, %21, %25, %17 offen offset:2048 sc1\n\t" X("%13") "3:\n\t"
               "s_cmp_eq_u32 %15, 0\n\ts_cbranch_scc1 2f\n\ts_sleep 1\n\t"
               "s_mov_b32 exec_lo, %15\n\ts_mov_b32 exec_hi, 0\n\tbuffer_load_dword %14, %19, %27, 0 offen sc1\n\ts_mov_b64 exec, -1\n\t"
               "s_waitcnt vmcnt(0)\n\ts_sub_u32 %18, %18, 1\n\ts_cbranch_scc0 1b\n2:\n\ts_waitcnt vmcnt(0)"
               : "=&v"(p[0]), "=&v"(p[1]), "=&v"(p[2]), "=&v"(p[3]), "=&v"(p[4]), "=&v"(p[5]), "=&v"(p[6]), "=&v"(q[0]), "=&v"(q[1]), "=&v"(q[2]), "=&v"(q[3]), "=&v"(q[4]), "=&v"(q[5]), "=&v"(q[6]), "+v"(f), "=&s"(pend), "=&s"(rdy), "=&s"(t), "=&s"(cnt)
               : "v"(fo), "v"(v0), "v"(v1), "s"(tag), "s"(src0), "s"(s0), "s"(sp), "s"(sq), "s"(sf), "i"(SPIN_CAP), "i"(8 * SLAB_W) : "vcc", "scc", "memory");
#undef X
  } else {
#define X(R) "\n"
  asm volatile("s_mov_b32 %15, 0x7f\n\ts_mov_b32 %18, %28\n"
               "1:\n\tv_cmp_eq_u32 vcc, %22, %14\n\ts_nop 1\n\ts_and_b32 %16, vcc_lo, %15\n\ts_andn2_b32 %15, %15, %16\n\t"
               "s_bitcmp1_b32 %16, 0\n\ts_cbranch_scc0 3f\n\ts_sub_u32 %17, %23, 0\n\ts_and_b32 %17, %17, 7\n\ts_mul_i32 %17, %17, %29\n\ts_add_u32 %17, %17, %24\n\t"
               "buffer_load_dwordx4 %0, %20, %25, %17 offen offset:0 sc1\n\t" X("%7") "3:\n\t"
               "s_bitcmp1_b32 %16, 1\n\ts_cbranch_scc0 3f\n\ts_sub_u32 %17, %23, 1\n\ts_and_b32 %17, %17, 7\n\ts_mul_i32 %17, %17, %29\n\ts_add_u32 %17, %17, %24\n\t"
               "buffer_load_dwordx4 %1, %20, %25, %17 offen offset:1024 sc1\n\t" X("%8") "3:\n\t"
               "s_bitcmp1_b32 %16, 2\n\ts_cbranch_scc0 3f\n\ts_sub_u32 %17, %23, 2\n\ts_and_b32 %17, %17, 7\n\ts_mul_i32 %17, %17, %29\n\ts_add_u32 %17, %17, %24\n\t"
               "buffer_load_dwordx4 %2, %20, %25, %17 offen offset:2048 sc1\n\t" X("%9") "3:\n\t"
               "s_bitcmp1_b32 %16, 3\n\ts_cbranch_scc0 3f\n\ts_sub_u32 %17, %23, 3\n\ts_and_b32 %17, %17, 7\n\ts_mul_i32 %17, %17, %29\n\ts_add_u32 %17, %17, %24\n\t"
               "buffer_load_dwordx4 %3, %20, %25, %17 offen offset:3072 sc1\n\t" X("%10") "3:\n\t"
               "s_bitcmp1_b32 %16, 4\n\ts_cbranch_scc0 3f\n\ts_sub_u32 %17, %23, 4\n\ts_and_b32 %17, %17, 7\n\ts_mul_i32 %17, %17, %29\n\ts_add_u32 %17, %17, %24\n\t"
               "buffer_load_dwordx4 %4, %21, %25, %17 offen offset:0 sc1\n\t" X("%11") "3:\n\t"
               "s_bitcmp1_b32 %16, 5\n\ts_cbranch_scc0 3f\n\ts_sub_u32 %17, %23, 5\n\ts_and_b32 %17, %17, 7\n\ts_mul_i32 %17, %17, %29\n\ts_add_u32 %17, %17, %24\n\t"
               "buffer_load_dwordx4 %5, %21, %25, %17 offen offset:1024 sc1\n\t" X("%12") "3:\n\t"
               "s_bitcmp1_b32 %16, 6\n\ts_cbranch_scc0 3f\n\ts_sub_u32 %17, %23, 6\n\ts_and_b32 %17, %17, 7\n\ts_mul_i32 %17, %17, %29\n\ts_add_u32 %17, %17, %24\n\t"
               "buffer_load_dwordx4 %6, %21, %25, %17 offen offset:2048 sc1\n\t" X("%13") "3:\n\t"
               "s_cmp_eq_u32 %15, 0\n\ts_cbranch_scc1 2f\n\ts_sleep 1\n\t"
               "s_mov_b32 exec_lo, %15\n\ts_mov_b32 exec_hi, 0\n\tbuffer_load_dword %14, %19, %27, 0 offen sc1\n\ts_mov_b64 exec, -1\n\t"
               "s_waitcnt vmcnt(0)\n\ts_sub_u32 %18, %18, 1\n\ts_cbranch_scc0 1b\n2:\n\ts_waitcnt vmcnt(0)"
               : "=&v"(p[0]), "=&v"(p[1]), "=&v"(p[2]), "=&v"(p[3]), "=&v"(p[4]), "=&v"(p[5]), "=&v"(p[6]), "=&v"(q[0]), "=&v"(q[1]), "=&v"(q[2]), "=&v"(q[3]), "=&v"(q[4]), "=&v"(q[5]), "=&v"(q[6]), "+v"(f), "=&s"(pend), "=&s"(rdy), "=&s"(t), "=&s"(cnt)
               : "v"(fo), "v"(v0), "v"(v1), "s"(tag), "s"(src0), "s"(s0), "s"(sp), "s"(sq), "s"(sf), "i"(SPIN_CAP), "i"(8 * SLAB_W) : "vcc", "scc", "memory");
#undef X
  }
}
template <int OFF, int W> DI void store8_sc1(u32x2 d, rsrc_t srd, unsigned voff, unsigned wave) {
  asm volatile("s_cmp_lg_u32 %4, %5\n\ts_cbranch_scc1 1f\n\tbuffer_store_dwordx2 %0, %1, %2, 0 offen offset:%3 sc1\n1:" ::"v"(d), "v"(voff), "s"(srd), "i"(OFF), "s"(wave), "i"(W) : "scc", "memory");
}
DI void load8x5_sc1(u32x2* d, rsrc_t srd, unsigned voff, unsigned wave) {
  asm volatile("s_cmp_lg_u32 %7, 0\n\ts_cbranch_scc1 1f\n\tbuffer_load_dwordx2 %0, %5, %6, 0 offen sc1\n\tbuffer_load_dwordx2 %1, %5, %6, 0 offen offset:512 sc1\n\t"
               "buffer_load_dwordx2 %2, %5, %6, 0 offen offset:1024 sc1\n\tbuffer_load_dwordx2 %3, %5, %6, 0 offen offset:1536 sc1\n\tbuffer_load_dwordx2 %4, %5, %6, 0 offen offset:2048 sc1\n1:"
               : "=&v"(d[0]), "=&v"(d[1]), "=&v"(d[2]), "=&v"(d[3]), "=&v"(d[4]) : "v"(voff), "s"(srd), "s"(wave) : "scc");
}
DI void publish_if(unsigned tag, unsigned soff, rsrc_t srd, unsigned off) {
  unsigned t;
  asm volatile("s_cmp_lg_u32 %4, 0\n\ts_cbranch_scc1 1f\n\tv_mov_b32 %0, %1\n\ts_waitcnt vmcnt(0)\n\ts_mov_b64 exec, 1\n\tbuffer_store_dword %0, off, %3, %2 sc1\n\ts_mov_b64 exec, -1\n1:"
               : "=&v"(t) : "s"(tag), "s"(soff), "s"(srd), "s"(off) : "scc", "memory");
}
DI void flag_only(unsigned tag, unsigned soff, rsrc_t srd) {
  unsigned t;
  asm volatile("v_mov_b32 %0, %1\n\ts_mov_b64 exec, 1\n\tbuffer_store_dword %0, off, %3, %2 sc1\n\ts_mov_b64 exec, -1" : "=&v"(t) : "s"(tag), "s"(soff), "s"(srd) : "memory");
}
DI void load8_if(u32x2& d, unsigned voff, unsigned soff, rsrc_t srd, unsigned off) {
  asm volatile("s_cmp_lg_u32 %4, 0\n\ts_cbranch_scc1 1f\n\tbuffer_load_dwordx2 %0, %1, %2, %3 offen\n1:" : "=v"(d) : "v"(voff), "s"(srd), "s"(soff), "s"(off) : "scc");
}

template <class T> DI T pick(bool c, T a, T b) { return c ? a : b; }
DI unsigned vand(unsigned a, unsigned b) { return a & b; }
DI float fma4(float a, float b, float c, float d, const float (&k)[4]) {
  float r = a * k[0]; pin(r); r = __builtin_fmaf(b, k[1], r); r = __builtin_fmaf(c, k[2], r); return __builtin_fmaf(d, k[3], r);
}
DI float vmax(float a, float b) { float r; asm("v_max_f32 %0, %1, %2" : "=v"(r) : "v"(a), "v"(b)); return r; }
template <bool MAX> DI float x16(float v) {
  const unsigned u = __builtin_bit_cast(unsigned, v);
  auto a = __builtin_amdgcn_permlane16_swap(u, u, false, false);
  const float p = __builtin_bit_cast(float, (unsigned)a[0]), q = __builtin_bit_cast(float, (unsigned)a[1]);
  return MAX ? vmax(p, q) : p + q;
}
template <bool MAX> DI float x32(float v) {
  const unsigned u = __builtin_bit_cast(unsigned, v);
  auto a = __builtin_amdgcn_permlane32_swap(u, u, false, false);
  const float p = __builtin_bit_cast(float, (unsigned)a[0]), q = __builtin_bit_cast(float, (unsigned)a[1]);
  return MAX ? vmax(p, q) : p + q;
}
template <int W> DI void swp(float& a, float& b) {
  const unsigned x = __builtin_bit_cast(unsigned, a), y = __builtin_bit_cast(unsigned, b);
  if constexpr (W == 16) { auto r = __builtin_amdgcn_permlane16_swap(x, y, false, false); a = __builtin_bit_cast(float, (unsigned)r[0]); b = __builtin_bit_cast(float, (unsigned)r[1]); }
  else { auto r = __builtin_amdgcn_permlane32_swap(x, y, false, false); a = __builtin_bit_cast(float, (unsigned)r[0]); b = __builtin_bit_cast(float, (unsigned)r[1]); }
}
template <bool MAX> DI void xq8(float (&m)[8], float k) {
  auto op = [](float p, float q) { return MAX ? vmax(p, q) : p + q; };
  float a[4], d[4];
  sfor<4>([&](auto I) { swp<16>(m[2 * I], m[2 * I + 1]); }); GAP();
  sfor<4>([&](auto I) { a[I] = op(m[2 * I], m[2 * I + 1]); pin(a[I]); }); GAP();
  sfor<2>([&](auto I) { swp<32>(a[2 * I], a[2 * I + 1]); }); GAP();
  sfor<2>([&](auto I) { float c = op(a[2 * I], a[2 * I + 1]); if constexpr (!MAX) c = __builtin_amdgcn_rcpf(c) * k; d[2 * I] = c; d[2 * I + 1] = c; pin(d[2 * I + 1]); }); GAP();
  sfor<2>([&](auto I) { swp<32>(d[2 * I], d[2 * I + 1]); }); GAP();
  sfor<4>([&](auto I) { m[2 * I] = d[I]; m[2 * I + 1] = d[I]; pin(m[2 * I + 1]); }); GAP();
  sfor<4>([&](auto I) { swp<16>(m[2 * I], m[2 * I + 1]); }); GAP();
}
template <int CTRL> DI float dpp_add(float v) { return v + __builtin_bit_cast(float, __builtin_amdgcn_update_dpp(0, __builtin_bit_cast(int, v), CTRL, 0xf, 0xf, true)); }
DI float wave_sum(float v) { return x32<false>(x16<false>(dpp_add<0x140>(dpp_add<0x141>(dpp_add<0x4e>(dpp_add<0xb1>(v)))))); }

constexpr int LDS_V = 2 * 24576 + 2560, LDS_K = 4 * 24576 + 2560;
constexpr int LDS_X = 6 * 24576, LDS_B = LDS_X + 16384;
constexpr int DA = 4, PB = 52, SM = 67;
constexpr float LOG2E = 1.4426950408889634f;
constexpr int cmin(int a, int b) { return a < b ? a : b; }

enum { NONE, W1, XF, TOUCH, W2, H2, SAMPLE,   CHECK, PUB, PUBH, XRES, HALO };
struct Op { int kind, a, b; };
template <bool QKV> struct Plan {
  static constexpr int NCG = QKV ? 5 : 8, KS = QKV ? 3 : 9, NST = 54 / KS, SPU = 18 / KS, NSLOT = QKV ? 4 : 2;
  static constexpr int SB = KS * 8192, IPS = KS * NCG, NIT = NST * IPS, BAR = IPS - (DA - 1), NPRO = 6;
  static constexpr bool kmajor(int n) { return QKV ? (n / IPS) % 6 != 5 : n < 96; }
  static constexpr int jof(int n) { return kmajor(n) ? n % NCG : (!QKV && n < 144) ? (n - 96) / 6 : (n % IPS) / KS; }
  static constexpr int sof(int n) { return kmajor(n) ? (n % (18 * NCG)) / NCG : (!QKV && n < 144) ? 12 + n % 6 : KS * ((n / IPS) % SPU) + n % KS; }
  static constexpr int NT = QKV ? 12 : 8, NIT2 = 21 * NT, A2 = QKV ? 9 : 6, M1 = A2 * NT, M2 = M1 + (15 - A2) * (NT - 1), M3 = M2 + 6 * (NT - 1);
  static constexpr int SB2 = NT * 1024, NSL2 = 147456 / SB2, WIN = QKV ? 4 : 8, O2 = QKV ? 0 : 12;
  static constexpr int gt(int k) { return QKV ? (k + 8) % 12 : k; }
  static constexpr int st2(int m) { return m < M1 ? m / NT : m < M2 ? A2 + (m - M1) / (NT - 1) : m < M3 ? 15 + (m - M2) % 6 : A2 + m - M3; }
  static constexpr int ti2(int m) { return m < M1 ? m % NT : m < M3 ? gt(m < M2 ? (m - M1) % (NT - 1) : (m - M2) / 6) : 7; }
  static constexpr bool first2(int m) { return m < M1 ? m % NT == 0 : m < M2 ? (m - M1) % (NT - 1) == 0 : m < M2 + 6; }
  static constexpr int win2(int m) { return (st2(m) % NSL2) / WIN; }
  static constexpr int pwin2(int m) { for (int i = m - 1; i >= 0; --i) if (ti2(i) == ti2(m)) return win2(i); return -1; }
  static constexpr int bar2(int m) {
    if (QKV) return m == 57 ? 11 : m == M1 - 3 ? 14 : m == M2 - 3 ? 20 : -1;
    return m == 66 ? 17 : m == M2 ? 20 : -1;
  }
  static constexpr int hstep(int a) { return a < O2 ? a : a - 3; }
  static constexpr bool rebase(int m) {
    if (m >= NIT) return m - NIT < NIT2 && win2(m - NIT) != pwin2(m - NIT) && !(QKV && m - NIT < NT);
    if (QKV) return m >= IPS && (m / IPS) % 2 == 0 && m % IPS < 3;
    return m < 144 ? (m >= 72 && m < 80) : m % 9 == 0;
  }
  static constexpr int estart(int su) { return su == 0 ? 108 : 144 * su + 92; }
  static constexpr int eidx(int su, int n, int t) {
    if (n < estart(su) || n > estart(su) + 110 || (t == 1 && rebase(n + 4))) return -1;
    int c = 0;
    for (int i = estart(su); i < n; ++i) c += rebase(i + 4) ? 1 : 2;
    return c + t < 176 ? c + t : -1;
  }
  static constexpr int eat(int su, int e) { for (int n = estart(su);; ++n) if (eidx(su, n, 0) == e || eidx(su, n, 1) == e) return n; }
  static constexpr Op w2op(int i) {
    if (!QKV) return Op{W2, i, 0};
    if (i < 5) return i == 2 ? Op{W2, 0, 1} : i == 4 ? Op{W2, 2, 1} : Op{W2, i < 2 ? i : 2, 0};
    return (i - 5) % 3 == 2 ? Op{W2, 3 + (i - 5) / 3 * 2, 1} : Op{W2, 3 + (i - 5) / 3 * 2 + (i - 5) % 3, 0};
  }
  static constexpr int NPRE2 = QKV ? 0 : 21;
  static constexpr Op pre2(int i) { return i < 9 ? w2op(i) : Op{H2, (i - 9) / 2, (i - 9) & 1}; }
  static constexpr Op at1(int n) {
    const int st = n / IPS, it = n % IPS;
    if (QKV) {
      if (n % 2 == 0 && NPRO + n / 2 < 36) return Op{XF, NPRO + n / 2, 0};
      if (st > 0 && st + 3 < NST) { const int i = it - (n - it) % 2 - (n < 60 ? 1 - 2 * ((n - it) % 2) : 1 - (n - it) % 2); if (i >= 0 && i < 6 && i % 2 == 0) return Op{W1, st + 3, i / 2}; }
      if (st == 13 && it == 11) return Op{PUBH, 0, 0};
      if (st == 15 && it == 8) return Op{SAMPLE, 0, 0};
      if (st == 16 && it == 13) return Op{CHECK, 3, 0};
      if (st == 17 && (it == 0 || it == 2)) return Op{HALO, it / 2, 0};
      return Op{NONE, 0, 0};
    }
    if (n % 3 == 0 && NPRO + n / 3 < 36) return Op{XF, NPRO + n / 3, 0};
    if (st >= 1 && st <= 4) { const int i0 = st == 3 ? 10 : 1; if (it >= i0 && (it - i0) % 3 == 0 && (it - i0) / 3 < 9) return Op{W1, st + 1, (it - i0) / 3}; }
    if (n == 224) return Op{PUB, 0, 0};
    if (st >= 1 && st <= 3 && it == (st == 3 ? 37 : 31)) return Op{TOUCH, st + 2, 0};
    if (st >= 1 && st <= 3 && it == (st == 3 ? 39 : 33)) return Op{TOUCH, st - 1, 1};
    if (n == 330) return Op{SAMPLE, 0, 0};
    if (n == 352) return Op{PUB, 1, 0};
    if (n == 353) return Op{CHECK, 0, 0};
    if (n == 407) return Op{SAMPLE, 1, 0};
    if (n == 431) return Op{CHECK, 1, 0};
    if (n >= 358 && n % 3 == 1 && (n - 358) / 3 < 21) return pre2((n - 358) / 3);
    return Op{NONE, 0, 0};
  }
  static constexpr Op at2(int m) {
    if (QKV) {
      if (m == 12) return Op{PUB, 3, 0};
      if (m >= 14 && m < 44 && m % 3 == 2) return w2op(8 + (m - 14) / 3);
      if (m == 43) return Op{W2, 11, 2};
      if (m == 47) return Op{SAMPLE, 1, 0};
      if (m >= 58 && m < 68 && m % 2 == 0) return m == 60 ? Op{W2, 12, 2} : w2op(18 + (m - 58) / 2);
      if (m == 69) return Op{CHECK, 1, 0};
      if (m >= 70 && m < 94 && m % 2 == 0) return Op{H2, 9 + (m - 70) / 4, ((m - 70) / 2) & 1};
      if (m >= 106 && m < 124 && m % 2 == 0) return w2op(23 + (m - 106) / 2);
      if (m == 129) return Op{SAMPLE, 2, 0};
      if (m == 149) return Op{CHECK, 2, 0};
      if (m >= 150 && m < 168 && m % 3 != 2) { const int e = (m - 150) - (m - 150) / 3; return Op{H2, 15 + e / 2, e & 1}; }
      if (m >= 190 && m < 198 && m % 2 == 0) return Op{XRES, (m - 190) / 2, 0};
      return Op{NONE, 0, 0};
    }
    if (m < 42 && m % 2 == 0) { const int e = m / 2; return e < 18 ? (e % 3 == 2 ? w2op(9 + e / 3) : Op{H2, 6 + e / 3, e % 3}) : w2op(e - 3); }
    if (m == 31) return Op{SAMPLE, 1, 0};
    if (m == PB + 2) return Op{CHECK, 4, 0};
    if (m > PB + 2 && m < PB + 14 && m % 2 == 1) return Op{H2, 12 + (m - PB - 3) / 4, ((m - PB - 3) / 2) & 1};
    if (m == PB) return Op{PUB, 2, 0};
    if (m == PB + 1) return Op{TOUCH, 0, 2};
    if (m == SM) return Op{SAMPLE, 2, 0};
    if (m >= 70 && m < 76 && m % 2 == 0) return w2op(18 + (m - 70) / 2);
    if (m == SM + 20) return Op{CHECK, 2, 0};
    if (m > SM + 20 && m <= SM + 32) return Op{H2, 15 + (m - SM - 21) / 2, (m - SM - 21) & 1};
    if (m == 122 || m == 124) return Op{XRES, (m - 122) / 2, 0};
    return Op{NONE, 0, 0};
  }
  static constexpr int wait1(int n0, int kind, int amax) {
    int cnt = 0, need = -1;
    for (int i = -NPRO; i < n0; ++i) {
      const Op o = i < 0 ? Op{XF, i + NPRO, 0} : at1(i);
      if (o.kind == PUB) need = -1;
      if (o.kind == NONE || o.kind > SAMPLE) continue;
      ++cnt;
      if (o.kind == kind && o.a <= amax) need = cnt;
    }
    return need < 0 || cnt - need > 62 ? 63 : cnt - need;
  }
  static constexpr int wait2(int m0, int kind, int amax) {
    int cnt = 0, need = -1;
    for (int i = -NPRE2; i < m0; ++i) {
      const Op o = i < 0 ? pre2(i + NPRE2) : at2(i);
      if (o.kind == PUB) need = -1;
      if (o.kind == NONE || o.kind > SAMPLE) continue;
      ++cnt;
      if (o.kind == kind && o.a <= amax) need = cnt;
    }
    return need < 0 || cnt - need > 62 ? 63 : cnt - need;
  }
};
using PF = Plan<false>;
static_assert(PF::eat(0, 175) < 224 - 16 && PF::eat(1, 175) < 352 - 16 && PF::eat(2, 175) < PF::NIT + PB - 8, "a unit is published after its last store");
static_assert(PF::eat(0, 44) >= 96 + 26 && PF::eat(0, 88) >= 96 + 38 && PF::eat(0, 132) >= 96 + 50, "unit 0: accumulators complete before they are read");
static_assert(PF::eat(0, 39) < 144 && PF::eat(0, 83) < 162 && PF::eat(0, 127) < 180 && PF::eat(0, 171) < 198, "unit 0: accumulators read before they restart");
static_assert(PF::eat(1, 44) >= 216 + 38 && PF::eat(1, 88) >= 216 + 56 && PF::eat(1, 132) >= 216 + 74 && PF::eat(1, 171) < 288 + 54, "unit 1");
static_assert(PF::eat(0, 153) > 144 + 30 && PF::eat(0, 153) < 213 && PF::eat(1, 65) > 216 + 40 && PF::eat(1, 65) < 285, "early weight waits follow the last request");
static_assert(PF::eat(2, 44) >= 360 + 38 && PF::eat(2, 88) >= 360 + 56 && PF::eat(2, 132) >= 360 + 74, "unit 2");

struct Args { const char* x; const char* wqkv; const char* wo; const char* wg; const char* wu; const char* wd; char* ws; char* out; };

DI void ln_load(u32x4& v, unsigned& v9, const char* x, int row, int lane) {
  const rsrc_t sx = mk_rsrc(x, S * D * 2);
  const unsigned xo = (unsigned)(row * 1152);
  load16(v, xo + lane * 16, sx);
  asm volatile("buffer_load_ushort %0, %1, %2, 0 offen offset:1024" : "=v"(v9) : "v"(xo + lane * 2), "s"(sx));
}
DI void ln_row(u32x4 v, unsigned v9, unsigned smem, int wave, int lane) {
  float f[9];
  sfor<4>([&](auto E) { f[2 * E] = bflo(v[(int)E]); f[2 * E + 1] = bfhi(v[(int)E]); });
  f[8] = bflo(v9);
  const float mean = wave_sum(((f[0] + f[1]) + (f[2] + f[3])) + ((f[4] + f[5]) + (f[6] + f[7])) + f[8]) * (1.0f / D);
  float q = 0.f;
  sfor<9>([&](auto E) { const float d = f[E] - mean; q += d * d; });
  const float var = wave_sum(q) * (1.0f / D);
  const float mb = rbf(mean), rs = LOG2E * __builtin_amdgcn_rcpf(rbf(__builtin_amdgcn_sqrtf(rbf(rbf(var) + 1e-5f))));
  sfor<4>([&](auto E) { v[(int)E] = cvtpk((f[2 * E] - mb) * rs, (f[2 * E + 1] - mb) * rs); });
  *(LDS u32x4*)(smem + lane * 128 + wave * 16) = v;
  *(LDS unsigned short*)(smem + 8192 + (lane >> 3) * 128 + wave * 16 + (lane & 7) * 2) = (unsigned short)cvtpk((f[8] - mb) * rs, 0.f);
}
DI void ln_publish(char* ws, unsigned smem, unsigned tag, int gid, int lane) {
  u32x4 t[9];
  const unsigned fo = spin(rfl(FL_X + FS * gid));
  sfor<9>([&](auto I) { t[I] = *(LDS u32x4*)(smem + 1024 * I + lane * 16); });
  const unsigned o = (unsigned)((gid >> 1) * 18432 + (gid & 1) * 128 + (lane >> 3) * 256 + (lane & 7) * 16);
  const rsrc_t sxn = mk_rsrc(ws + WS_XN, XN_B);
  sfor<9>([&](auto I) { store16_sc1<0>(t[I], sxn, o, 2048 * I); });
  publish(tag, fo, mk_rsrc(ws + WS_FL, FL_B));
}

template <bool QKV>
DI void body(const Args& A, unsigned smem, unsigned tag, int rt, int col0, int wave, int lane, unsigned tch0) {
  using P = Plan<QKV>;
  constexpr int NCG = P::NCG, KS = P::KS, NSLOT = P::NSLOT, IPS = P::IPS, NIT = P::NIT, SPU = P::SPU, SB = P::SB, NT = P::NT, NIT2 = P::NIT2;
  constexpr int PL1 = QKV ? 3 : 4;
  constexpr unsigned RS = QKV ? 3u * D * 2u : F * 2u, WB = QKV ? D * 3 * D * 2 : D * F * 2;
  char* ws = A.ws;
  const int bid = blockIdx.x, isU = wave >> 2, w3 = wave & 3;
  const rsrc_t sF = mk_rsrc(ws + WS_FL, FL_B), sxn = mk_rsrc(ws + WS_XN, XN_B);
  const int cgl = ((lane & 7) >> 1) ^ ((lane >> 4) & 3);
  const int pc = 2 * cgl + (lane & 1);
  const unsigned wr = (unsigned)((16 * (w3 & 1) + 4 * (w3 >> 1) + 8 * (lane >> 5) + ((lane >> 3) & 3)) * RS + col0 * 2);
  const unsigned wa = wr + (QKV ? min(pc, 5) : pc) * 16, wv = QKV && isU ? wr + min(max(pc, 2), 4) * 16 : wa;
  const char* wA = QKV ? A.wqkv : A.wg;
  const char* wB = QKV ? A.wqkv + 64 : A.wu;
  const rsrc_t sw = mk_rsrc(isU ? wB : wA, QKV && isU ? WB - 64 : WB);
  const unsigned m0b = smem + isU * (SB / 2) + w3 * 1024;
  auto w1 = [&](auto ST, auto B, unsigned m0, unsigned vo, rsrc_t srd) {
    constexpr int a = CV(ST), b = CV(B), su = a / SPU;
    constexpr unsigned sucol = QKV ? (su == 0 ? 1152u : su == 1 ? 2304u : 0u) : su * 128u;
    dma16<(a % NSLOT) * SB + b * 4096, 32 * (KS * (a % SPU) + b) * RS + sucol>(m0, vo, srd);
  };
  if (isU) {
    const rsrc_t sa = mk_rsrc(wA, WB);
    sfor<NSLOT>([&](auto ST) { sfor<KS>([&](auto B) { w1(ST, B, m0b - SB / 2, wa, sa); w1(ST, B, m0b, wv, sw); }); });
  }
  const int t16 = rt * 16 + 2 * wave;
  const unsigned xs = spin(t16 * 18432), l16 = lane * 16;
  u32x4 xf[18][2];
  auto xload = [&](auto AA) { constexpr int a = CV(AA); load16_sc1<(a / 2) * 1024 + (a & 1) * 18432>(xf[a / 2][a & 1], l16, xs, sxn); };
  if (wave == 0) {
    const unsigned fo = FL_X + FS * (rt * 32 + (lane & 31));
    __builtin_amdgcn_s_sleep(20);
    unsigned f; load4_sc1(f, fo, sF); flags_wait<0, 1>(f, fo, tag, ~0ull, sF);
  }
  asm volatile("s_barrier" ::: "memory");
  STAMP(4);
  sfor<P::NPRO>(xload);
  GAP();
  const int b2 = bid & 7, tile = bid >> 3, nc2 = QKV ? 768 : (tile - 3 * rt) * 256;
  const rsrc_t sh = mk_rsrc(ws + WS_H, HBUF_B), sS = mk_rsrc(ws + WS_SLAB, SLAB_B);
  const rsrc_t sWd = mk_rsrc(A.wd + 576 * 1152 * b2, 576 * 1152), sWo = mk_rsrc(A.wo + 72 * 1152 * b2, 64 * 1152), sWp = mk_rsrc(A.wo + (72 * b2 + 64) * 1152, 8 * 1152);
  const unsigned m0c = spin(smem + wave * 1024), m0d = spin(smem + 8192 + w3 * 1024 + isU * P::SB2);
  const unsigned hb = spin((t16 * KSTOT + 18 * b2) * 1024), ho = spin((t16 * KSTOT + 144 + 3 * b2) * 1024), hst = spin((t16 * KSTOT + (col0 >> 5)) * 1024);
  const unsigned oso = spin(t16 * 16 * 1152 + nc2), sl = spin(((tile * 8 + b2) * 8 + wave) * SLAB_W);
  const unsigned xt = spin(((wave - b2) & 7) >> 1), guest = spin((wave - b2) & 1);
  const unsigned fme = spin(FL_H + ((rt * 8 + wave) * 96 + (QKV ? 12 * b2 : 4 * (col0 / 192))) * FS);
  const unsigned tab = smem + LDS_X + wave * 2048 + l16;
  const bool wdo = tile * 8 + wave >= 162;
  unsigned ab[NCG];
  u32x4 t0;
  {
    const unsigned l = lane, b0 = smem + (l >> 2) * 128 + ((l >> 3) & 3) * 32 + 8 * (l & 3);
    t0[0] = (8 * w3 + (l >> 3)) * 1152 + nc2 + isU * 128 + pc * 16;
    t0[1] = FL_H + ((rt * 8 + wave) * 96 + 12 * b2 + (l < 12 ? l : 0)) * FS;
    t0[2] = b0;
    t0[3] = (l & 15) * 1152 + (l >> 4) * 8;
    *(LDS u32x4*)tab = t0;
    if constexpr (!QKV) *(LDS u32x2*)(tab + 1024) = u32x2{
      pick<unsigned>(l < 9, (rt * 36 + w3 * 9 + l) * RS + col0 * 2, 0x80000000u),
      pick<unsigned>(l < 11, (wdo ? tile * 8 + wave - 162 : tile * 8 + wave) * 4096 + l * 128, 0x80000000u)};
    if constexpr (QKV) sfor<3>([&](auto C) { ab[C] = b0 ^ (CV(C) * 32); pin(ab[C]); });
    else sfor<8>([&](auto J) { ab[J] = (b0 ^ ((CV(J) >> 1) * 32)) + (CV(J) & 1) * (SB / 2); pin(ab[J]); });
  }
  u32x4 hf[21][2], hp[2], gp;
  unsigned fl, cur, wv3, tch[2], tk[3][2], cb[NT];
  u32x2 xres[4], pk[5][2], hl[10];
  unsigned l8 = lane * 8;
  if constexpr (QKV) pin(l8);
  const unsigned stg = smem + wave * 5120 + l8;
  auto ldtab = [&](int i) { return *(volatile LDS unsigned*)(tab + 1024 + 4 * i); };
  auto vmem = [&](auto K, auto AA, auto BB, auto NN) {
    constexpr int kind = CV(K), a = CV(AA), b = CV(BB);
    if constexpr (kind == W1) w1(AA, BB, m0b, wv, sw);
    else if constexpr (kind == XF) xload(AA);
    else if constexpr (kind == TOUCH) {
      if constexpr (b == 2) load4_sc1(tch[0], (l16 & 0xf0) * 8 + (l16 & 0x100) * (KSTOT * 4) + (l16 >> 9) * 0x80000000u + hst + 4096, sh);
      else if constexpr (b == 0) load4s<288 * (a % 2) * RS + (a / 2) * 128>(tch[0], tch[0], sw);
      else load4s<a * 11 * 128>(tch[1], tch[1], mk_rsrc(wdo ? A.wo + 72 * 1152 * b2 : A.wd + 576 * 1152 * b2, wdo ? 72 * 1152 - 4096 : 576 * 1152 - 4096));
    } else if constexpr (kind == XRES) load8_if(xres[a], t0[3], oso + (a & 1) * 16 * 1152 + (a < 2 ? 32 * b2 : 256 + 32 * xt), mk_rsrc(A.x, S * D * 2), a < 2 ? 0u : guest);
    else if constexpr (kind == W2) {
      constexpr bool isO = a >= P::O2 && a < P::O2 + 3;
      constexpr int e = P::hstep(a), row = isO ? (a - P::O2 == 1 ? 32 : 0) : 192 * ((e % 6) / 2) + 64 * (e / 6) + 32 * (e & 1);
      const rsrc_t r = !isO ? sWd : a - P::O2 < 2 ? sWo : sWp;
      if constexpr (b == 0) dma16<(a % P::NSL2) * P::SB2, row * 1152>(m0c, t0[0], r);
      else if constexpr (b == 1 && !(isO && a == 2)) dma16<(a % P::NSL2) * P::SB2, row * 1152>(m0d, wv3, r);
      else dma16<(a % P::NSL2) * P::SB2 + 8192, row * 1152 + 256>(smem + w3 * 1024, t0[0] - isU * 128, r);
    } else if constexpr (kind == H2) {
      constexpr int e = P::hstep(a);
      if constexpr (a >= P::O2 && a < P::O2 + 3) load16_sc1<(b * KSTOT + a - P::O2) * 1024>(hf[a][b], l16, ho, sh);
      else load16_sc1<(b * KSTOT + 6 * ((e % 6) / 2) + 2 * (e / 6) + (e & 1)) * 1024>(hf[a][b], l16, hb, sh);
    } else if constexpr (kind == HALO) load8x5_sc1(hl + 5 * a, mk_rsrc(ws + WS_HALO + (bid - 200) * 5120 + a * 2560, rt > 0 ? 2560 : 0), l8, wave);
    else if constexpr (kind == SAMPLE) load4_sc1(fl, t0[1], sF);
    else if constexpr (kind == CHECK) flags_wait<CV(NN), QKV ? 4 : 2>(fl, t0[1], tag, a == 0 ? 0x111ull : a == 1 ? 0x222ull : a == 2 ? 0x444ull : a == 4 ? 0x8ull : wave == 0 && rt > 0 ? 0x80ull : 0ull, sF);
    else if constexpr (kind == PUB) publish(tag, fme + a * FS, sF);
    else if constexpr (kind == PUBH) publish_if(tag, fme + (96 + 7) * FS, sF, wave ^ 7);
  };
  f32x4 acc[NCG][2], acc2[NT][2];
  bf16x4 al[DA], ah[DA];
  auto aread = [&](auto N, auto T) {
    constexpr int n = CV(N);
    bf16x4 r;
    if constexpr (n >= NIT) r = trread(cb[P::ti2(n - NIT)], ((P::st2(n - NIT) % P::NSL2) % P::WIN) * P::SB2 + CV(T) * 2048);
    else if constexpr (QKV) { constexpr int j = P::jof(n); r = trread(ab[j < 3 ? j : j - 2], ((n / IPS) & 1) * SB + (j / 3) * (SB / 2) + (P::sof(n) % KS) * 4096 + CV(T) * 2048); }
    else r = trread(n < 144 ? ab[P::jof(n)] : cur, (P::sof(n) % KS) * 4096 + CV(T) * 2048);
    if constexpr (CV(T) == 0) al[n % DA] = r; else ah[n % DA] = r;
  };
  auto rebase = [&](auto M) {
    constexpr int m = CV(M);
    if constexpr (m >= NIT) {
      constexpr int p = m - NIT, i = P::ti2(p);
      if constexpr (P::pwin2(p) >= 0) cb[i] = v_addk<(unsigned)((P::win2(p) - P::pwin2(p)) * P::WIN * P::SB2)>(cb[i]);
      else { const unsigned ph = i < 8 ? (i + b2 + 1) & 7 : i; cb[i] = v_xors(t0[2], (ph & 3) * 32 + (ph >> 2) * 4096); }
    } else if constexpr (QKV) ab[P::jof(m)] = v_addk<(m / IPS) % 4 == 0 ? (unsigned)(-2 * SB) : 2u * SB>(ab[P::jof(m)]);
    else if constexpr (m < 144) ab[P::jof(m)] = v_addk<SB>(ab[P::jof(m)]);
    else cur = v_addk<(unsigned)((((m / IPS) & 1) - 1) * SB + (P::jof(m) & 1) * (SB / 2))>(ab[P::jof(m) & ~1]);
  };
  float ee, eg, eh[2];
  const float k2 = __builtin_bit_cast(float, spin(__builtin_bit_cast(unsigned, LOG2E * LOG2E)));
  auto epi = [&](auto SU, auto E) {
    constexpr int e = CV(E), w = e / 11, ph = e % 11, p = w >> 2, th = (w >> 1) & 1, r = 2 * (w & 1) + (ph >= 4 && ph != 5);
    if constexpr (ph == 0 || ph == 4) { if constexpr (ph == 4) eh[0] = ee; ee = v_expn(acc[2 * p][th][r]); }
    if constexpr (ph == 1 || ph == 6) eg = v_mul(acc[2 * p][th][r], acc[2 * p + 1][th][r]);
    if constexpr (ph == 2 || ph == 7) ee = v_fmak(ee, k2);
    if constexpr (ph == 3 || ph == 8) ee = v_rcp(ee);
    if constexpr (ph == 5) eh[0] = v_mul(eg, eh[0]);
    if constexpr (ph == 9) eh[1] = v_mul(eg, ee);
    if constexpr (ph == 10) {
      hp[th][2 * (p & 1) + (w & 1)] = v_cvtpk(eh[0], eh[1]);
      if constexpr ((p & 1) && (w & 1)) store16_sc1<(p >> 1) * 1024>(hp[th], sh, l16, hst + (th * KSTOT + 2 * CV(SU)) * 1024);
    }
  };
  auto filler = [&](auto N, auto T) {
    constexpr int n = CV(N), t = CV(T);
    if constexpr (t == 1 && P::rebase(n + 4)) rebase(ic<n + 4>{});
    else if constexpr (!QKV) sfor<3>([&](auto SU) { constexpr int e = P::eidx(CV(SU), n, t); if constexpr (e >= 0) epi(SU, ic<e>{}); });
    GAP();
  };
  auto epiq = [&](auto SU, auto E, auto T) {
    constexpr int su = CV(SU), e = CV(E) - 4, t = CV(T), cg = e / 3, th = e % 3;
    if constexpr (e >= 0 && cg < 5 && th < 2) {
      pk[cg][th][t] = v_cvtpk(acc[cg][th][2 * t], acc[cg][th][2 * t + 1]);
      if constexpr (su == 0 && t == 1) *(LDS u32x2*)(stg + LDS_K + th * 2560 + 512 * cg) = pk[cg][th];
      if constexpr (su < 2 && t == 1 && th == 1) store8_sc1<512 * cg, 7>(pk[cg][1], mk_rsrc(ws + WS_HALO + (bid - 192) * 5120 + su * 2560, 2560), l8, wave);
      GAP();
    }
  };
  GAP(); wait_barrier<P::NPRO - 2>(); GAP();
  keep(tch0);
  STAMP(5);
  __builtin_amdgcn_s_setprio(PL1 - 1);
  sfor<DA - 1>([&](auto N) { aread(N, ic<0>{}); aread(N, ic<1>{}); });
  GAP();
  sfor<NIT>([&](auto N) {
    constexpr int n = CV(N), st = n / IPS, it = n % IPS, j = P::jof(n), sg = P::sof(n), su = st / SPU, c = st % SPU, m = n + DA - 1;
    constexpr bool more = m < NIT || !QKV;
    if constexpr (QKV && n == NIT - (DA - 1)) { lds_barrier(); GAP(); }
    constexpr bool early = !QKV && (st == 2 || st == 3);
    if constexpr (early && n == P::eat(st - 2, st == 2 ? 153 : 65) - 1) { wait_vm<P::wait1(n, W1, st + 1)>(); GAP(); }
    if constexpr (it == P::BAR && st < P::NST - 1) { wait_barrier<early ? 63 : P::wait1(n, W1, st + 1)>(); GAP(); }
    if constexpr (!QKV && n == NIT - (DA - 1)) { wait_barrier<cmin(P::wait1(n, W2, 8), P::wait1(n, H2, 0))>(); GAP(); }
    { constexpr int pp = (it + DA - 1) % IPS; if constexpr (pp % (IPS / PL1) == 0) { __builtin_amdgcn_s_setprio(PL1 - 1 - pp / (IPS / PL1)); GAP(); } }
    if constexpr (su == 0 && j == 0 && n > 0) { wait_vm<P::wait1(n, XF, 2 * sg + 1)>(); GAP(); }
    const bf16x8 a = cat8(al[n % DA], ah[n % DA]);
    constexpr bool swp = QKV && su == 1;
    sfor<2>([&](auto T) {
      if constexpr (more) { aread(ic<m>{}, T); GAP(); }
      const bf16x8 b = as_bf(xf[sg][T]);
      if constexpr (sg == 0) mfma_new(acc[j][T], swp ? b : a, swp ? a : b); else mfma_acc(acc[j][T], swp ? b : a, swp ? a : b);
      GAP();
      filler(N, T);
      if constexpr (QKV && c == 5) epiq(ic<su>{}, ic<it>{}, T);
      if constexpr (QKV && c == 0 && su > 0) epiq(ic<su - 1>{}, ic<it + IPS>{}, T);
    });
    if constexpr (QKV) {
      if constexpr (st == 16 && it < 10) *(LDS u32x2*)(stg + LDS_V + (it % 2) * 2560 + 512 * (it / 2)) = pk[it / 2][it % 2];
      if constexpr (n == 14 * IPS) t0 = *(volatile LDS u32x4*)tab;
    } else {
      if constexpr (st >= 1 && st <= 3 && it == 24) { tch[0] = ldtab(0); tch[1] = ldtab(1); }
      if constexpr (st >= 1 && st <= 3 && it == 41) { tk[st - 1][0] = tch[0]; tk[st - 1][1] = tch[1]; }
      if constexpr (n == 200 || n == 230 || n == 354) { constexpr int q = n == 200 ? 0 : n == 230 ? 1 : 2; pin(tk[q][0]); pin(tk[q][1]); }
      if constexpr (n == 320) t0 = *(volatile LDS u32x4*)tab;
    }
    constexpr Op o = P::at1(n);
    if constexpr (o.kind != NONE) vmem(ic<o.kind>{}, ic<o.a>{}, ic<o.b>{}, ic<P::wait1(n, SAMPLE, 9)>{});
    GAP();
    if constexpr (n % (NIT / 9) == NIT / 9 - 1) STAMP(6 + n / (NIT / 9));
  });
  if constexpr (QKV) {
    mfma_drain();
    sfor<4>([&](auto J) { epiq(ic<2>{}, ic<16 + CV(J) / 2>{}, ic<CV(J) % 2>{}); });
    vmem(ic<SAMPLE>{}, ic<0>{}, ic<0>{}, ic<0>{});
    const int i16 = lane & 15, q4 = lane >> 4, odd = lane & 1;
    const bool cur0 = 4 * q4 + odd <= i16, cur1 = 4 * q4 + 2 + odd <= i16;
    const unsigned s0 = odd ? 0x01000c0cu : 0x0c0c0100u, s1 = odd ? 0x03020c0cu : 0x0c0c0302u;
    const unsigned sel[4] = {cur0 ? 0x0c0c0c0cu : s0, cur1 ? 0x0c0c0c0cu : s1, cur0 ? s0 : 0x0c0c0c0cu, cur1 ? s1 : 0x0c0c0c0cu};
    constexpr float SC = 0.23570226039551584f / LOG2E;
    auto ld2 = [&](unsigned a0, unsigned a1) { const u32x2 u = *(volatile LDS u32x2*)a0, v = *(volatile LDS u32x2*)a1; return as_bf(u32x4{u[0], u[1], v[0], v[1]}); };
    const float cp = t16 == 0 ? -1.0e30f : 0.f;
    unsigned ge[2], lt[2];
    sfor<2>([&](auto I) { ge[I] = (unsigned)((CV(I) - q4) >> 31); lt[I] = ~ge[I]; pin(lt[I]); pin(ge[I]); });
    auto mge = [&](unsigned v, auto C) { constexpr int c = CV(C); if constexpr (c <= 0) return v; else return v & ge[(c - 1) / 4]; };
    auto mlt = [&](unsigned v, auto C) { constexpr int c = CV(C); if constexpr (c <= 0) return 0u; else return v & lt[(c - 1) / 4]; };
    float ca[4], cc[4];
    sfor<4>([&](auto I) { const bool par = odd == (CV(I) & 1); ca[I] = par && cur0 == (CV(I) < 2) ? SC : 0.f; cc[I] = par && cur1 == (CV(I) < 2) ? SC : 0.f; pin(ca[I]); pin(cc[I]); });
    wv3 = t0[0] + 256 + isU * (32 * 1152 - 128); pin(wv3);
    wait_vm<1>();
    sfor<10>([&](auto I) { pin(hl[I]); });
    if (wave == 0) sfor<10>([&](auto I) { *(volatile LDS u32x2*)(stg + (CV(I) < 5 ? LDS_K : LDS_V) - 2560 + 512 * (CV(I) % 5)) = hl[I]; });
    f32x4 sp[8], sc[8];
    float l0[8], l1[8], mx[8];
    bf16x8 pb[8], kb[4][3], vb[2][5];
    sfor<12>([&](auto I) { constexpr int h = CV(I) / 3, g = CV(I) % 3; kb[h][g] = ld2(stg + LDS_K + (g - 1) * 2560 + 512 * h, stg + LDS_K + (g - 1) * 2560 + 512 * h + 512); });
    GAP();
    auto pf = [&](auto I) { constexpr Op o = P::w2op(CV(I)); GAP(); vmem(ic<o.kind>{}, ic<o.a>{}, ic<o.b>{}, ic<0>{}); GAP(); };
    auto ph = [&](auto I) { GAP(); vmem(ic<H2>{}, ic<3 + CV(I)>{}, ic<0>{}, ic<0>{}); vmem(ic<H2>{}, ic<3 + CV(I)>{}, ic<1>{}, ic<0>{}); GAP(); };
    pf(ic<0>{});
    sfor<4>([&](auto HH) {
      constexpr int hd = CV(HH);
      sfor<2>([&](auto TH) {
        const bf16x8 qb = as_bf(u32x4{mge(pk[hd][TH][0], ic<2 * hd>{}), mge(pk[hd][TH][1], ic<2 * hd - 2>{}), mlt(pk[hd + 1][TH][0], ic<2 * hd + 2>{}), mlt(pk[hd + 1][TH][1], ic<2 * hd>{})});
        sp[2 * hd + TH] = CV(TH) ? mfma(kb[hd][1], qb, f32x4{}) : mfma(kb[hd][0], qb, f32x4{cp, cp, cp, cp});
        sc[2 * hd + TH] = mfma(kb[hd][1 + TH], qb, f32x4{});
      });
      pf(ic<1 + hd>{});
    });
    sfor<10>([&](auto I) { constexpr int th = CV(I) / 5, j = CV(I) % 5; vb[th][j] = ld2(stg + LDS_V + (th - 1) * 2560 + 512 * j, stg + LDS_V + th * 2560 + 512 * j); });
    GAP();
    lds_barrier();
    vmem(ic<CHECK>{}, ic<0>{}, ic<0>{}, ic<5>{});
    sfor<8>([&](auto E) {
      l0[E] = fma4(sc[E][0], sc[E][1], sp[E][0], sp[E][1], ca);
      l1[E] = fma4(sc[E][2], sc[E][3], sp[E][2], sp[E][3], cc);
      mx[E] = vmax(l0[E], l1[E]); pin(mx[E]); pin(l0[E]); pin(l1[E]);
      if constexpr (CV(E) % 3 == 0) pf(ic<5 + CV(E) / 3>{});
    });
    ph(ic<0>{});
    xq8<true>(mx, 0.f);
    ph(ic<1>{});
    sfor<8>([&](auto E) { l0[E] = __builtin_amdgcn_exp2f(l0[E] - mx[E]); l1[E] = __builtin_amdgcn_exp2f(l1[E] - mx[E]); mx[E] = l0[E] + l1[E]; });
    ph(ic<2>{});
    xq8<false>(mx, 1.f / LOG2E);
    ph(ic<3>{});
    sfor<8>([&](auto E) {
      const unsigned pp = cvtpk(l0[E] * mx[E], l1[E] * mx[E]);
      pb[E] = as_bf(u32x4{__builtin_amdgcn_perm(pp, pp, sel[0]), __builtin_amdgcn_perm(pp, pp, sel[1]),
                          __builtin_amdgcn_perm(pp, pp, sel[2]), __builtin_amdgcn_perm(pp, pp, sel[3])});
    });
    ph(ic<4>{});
    sfor<8>([&](auto E) { sp[E] = mfma(vb[CV(E) & 1][CV(E) >> 1], pb[E], f32x4{}); sc[E] = mfma(vb[CV(E) & 1][(CV(E) >> 1) + 1], pb[E], f32x4{}); });
    ph(ic<5>{});
    sfor<NT>([&](auto I) { rebase(ic<NIT + CV(I)>{}); });
    wait_vm<12>();
    sfor<2>([&](auto TH) {
      unsigned ow[12];
      sfor<12>([&](auto E) {
        constexpr int j = CV(E) >> 1, e = CV(E) & 1;
        unsigned a = 0u, b = 0u;
        if constexpr (j < 4) a = mge(cvtpk(sp[2 * j + TH][2 * e], sp[2 * j + TH][2 * e + 1]), ic<2 * j - 2 * e>{});
        if constexpr (j > 0 && j < 5) b = mlt(cvtpk(sc[2 * j - 2 + TH][2 * e], sc[2 * j - 2 + TH][2 * e + 1]), ic<2 * j - 2 * e>{});
        ow[E] = a | b;
      });
      sfor<3>([&](auto M) { hf[M][TH] = u32x4{ow[4 * M], ow[4 * M + 1], ow[4 * M + 2], ow[4 * M + 3]}; pin(hf[M][TH]); store16_sc1<1024 * CV(M)>(hf[M][TH], sh, l16, ho + TH * KSTOT * 1024); });
    });
    STAMP(16);
    GAP(); lds_barrier(); GAP();
    sfor<DA - 1>([&](auto N) { aread(ic<NIT + CV(N)>{}, ic<0>{}); aread(ic<NIT + CV(N)>{}, ic<1>{}); });
    __builtin_amdgcn_s_setprio(3);
    GAP();
    STAMP(17);
  }
  sfor<NIT2>([&](auto M) {
    constexpr int m = CV(M), n = NIT + m, st = P::st2(m), i = P::ti2(m);
    if constexpr (P::bar2(m) >= 0) { wait_barrier<P::wait2(m, W2, P::bar2(m))>(); GAP(); }
    {
      constexpr int lo = [] { int k = m; while (k >= 0 && P::bar2(k) < 0) --k; return k < 0 ? -3 : k; }(), hi = [] { int k = m + 1; while (k < NIT2 && P::bar2(k) < 0) ++k; return k; }();
      constexpr int len = (hi - lo + 3) / 4;
      if constexpr ((m - lo) % len == 0) { __builtin_amdgcn_s_setprio(3 - (m - lo) / len); GAP(); }
    }
    if constexpr (P::first2(m) && st > 0) { wait_vm<P::wait2(m, H2, st)>(); GAP(); }
    const bf16x8 a = cat8(al[n % DA], ah[n % DA]);
    sfor<2>([&](auto T) {
      if constexpr (m + DA - 1 < NIT2) { aread(ic<n + DA - 1>{}, T); GAP(); }
      if constexpr (st == 0) mfma_new(acc2[i][T], a, as_bf(hf[st][T])); else mfma_acc(acc2[i][T], a, as_bf(hf[st][T]));
      GAP();
      filler(ic<n>{}, T);
      constexpr int c = m - P::M2 - 7;
      if constexpr (c >= 0 && c / 6 < NT - 1 && c % 6 < 4 && CV(T) == 0) {
        constexpr int tl = P::gt(c / 6), e = c % 6;
        gp[e] = v_cvtpk(acc2[tl][e >> 1][2 * (e & 1)], acc2[tl][e >> 1][2 * (e & 1) + 1]);
        if constexpr (e == 3) store16_sc1<0>(gp, sS, l16, sl + tl * 1024);
        GAP();
      }
    });
    if constexpr (!QKV && m == 118) pin(tch[0]);
    constexpr Op o = P::at2(m);
    if constexpr (o.kind != NONE) { vmem(ic<o.kind>{}, ic<o.a>{}, ic<o.b>{}, ic<P::wait2(m, SAMPLE, 9)>{}); GAP(); }
    if constexpr (m % (3 * NT) == 3 * NT - 1) STAMP(18 + m / (3 * NT));
  });
  unsigned fo = FL_S + (tile * 8 + wave) * 8 * FS + ((16 * (b2 + 7) - l16) & 0x70) * (FS / 16), f;
  if (!QKV && wave == 0 && bid == 0) {
    unsigned g[4];
    sfor<4>([&](auto I) { load4_sc1(g[I], FL_X + 64 * FS * I + 8 * l16, sF); });
    sfor<4>([&](auto I) { flags_wait<0, 2>(g[I], FL_X + 64 * FS * I + 8 * l16, tag, ~0ull, sF); });
    flag_only(tag, FL_EPOCH, sF);
  }
  load4_sc1(f, fo, sF);
  publish(tag, (unsigned)(FL_S + ((tile * 8 + wave) * 8 + b2) * FS), sF);
  STAMP(26);
  mfma_drain();
  pin(acc2[7][0]); pin(acc2[7][1]);
  sfor<QKV ? 4 : 2>([&](auto I) { pin(xres[I]); });
  u32x4 pv[7], pw[8];
  const rsrc_t sQ = mk_rsrc(ws + WS_SLAB + 8192 + xt * 1024, QKV && !guest ? SLAB_B - 12288 + 1024 : 0u);
  if constexpr (QKV) load16_sc1<0>(pw[7], l16, sl, sQ);
  lane = lane_now();
  const unsigned one = (lane >> 4) == ((lane >> 2) & 3) ? (lane & 1 ? 0x3f800000u : 0x3f80u) : 0u, e0 = lane & 2 ? 0u : one, e1 = lane & 2 ? one : 0u;
  const bf16x8 elo = as_bf(u32x4{e0, e1, 0u, 0u}), ehi = as_bf(u32x4{0u, 0u, e0, e1});
  const bf16x8 xb = as_bf(u32x4{xres[0][0], xres[0][1], xres[1][0], xres[1][1]});
  f32x4 o0 = mfma(elo, xb, acc2[7][0]), o1 = mfma(ehi, xb, acc2[7][1]);
  const unsigned xr = smem + LDS_X + wave * 2048;
  unsigned tw = xr + (lane & 15) * 48 + (lane >> 4) * 8, trd = xr + (lane >> 1) * 48 + (lane & 1) * 16, tst = (lane >> 1) * 1152 + (lane & 1) * 16;
  pin(tw); pin(trd); pin(tst); pin(o0); pin(o1);
  gather7<QKV>(pv, pw, f, fo, l16, l16 + 4096, tag, b2 + 7, spin((tile * 64 + wave) * SLAB_W), sS, sQ, sF);
  STAMP(28);
  const rsrc_t sO = mk_rsrc(A.out, S * D * 2);
  auto finish = [&](auto N, const u32x4* p, unsigned so) {
    sfor<CV(N)>([&](auto J) { o0 = mfma(elo, as_bf(p[J]), o0); o1 = mfma(ehi, as_bf(p[J]), o1); });
    *(LDS u32x2*)tw = u32x2{cvtpk(o0[0], o0[1]), cvtpk(o0[2], o0[3])}; *(LDS u32x2*)(tw + 768) = u32x2{cvtpk(o1[0], o1[1]), cvtpk(o1[2], o1[3])};
    asm volatile("" ::: "memory");
    store16(*(LDS u32x4*)trd, sO, tst, so);
  };
  if constexpr (QKV) pin(pw[7]);
  finish(ic<7>{}, pv, oso + 32 * b2);
  if (QKV && !guest) {
    const bf16x8 x8 = as_bf(u32x4{xres[2][0], xres[2][1], xres[3][0], xres[3][1]});
    o0 = mfma(elo, x8, f32x4{}); o1 = mfma(ehi, x8, f32x4{});
    finish(ic<8>{}, pw, oso + 256 + 32 * xt);
  }
  STAMP(29);
  if constexpr (QKV) asm volatile(".Lpsk_end_a:"); else asm volatile(".Lpsk_end_b:");
}

__global__ __launch_bounds__(512, 2) void fused(Args A) {
  __shared__ __attribute__((aligned(256))) char smem_[LDS_B];
  const unsigned smem = (unsigned)(size_t)(LDS char*)smem_;
  const int wave = rfl(threadIdx.x >> 6), lane = threadIdx.x & 63, bid = blockIdx.x, xcd = bid & 7, r = bid >> 3;
  char* ws = A.ws;
  STAMP(0);
  u32x4 v; unsigned v9, ep, tc;
  ln_load(v, v9, A.x, bid * 8 + wave, lane);
  load4_sc1(ep, FL_EPOCH, mk_rsrc(ws + WS_FL, FL_B));
  asm volatile("s_barrier" ::: "memory");
  const bool qkv = r >= 24;
  const int rt = qkv ? r - 24 : r / 3, col0 = qkv ? 72 * xcd : (xcd * 3 + r % 3) * 192;
  {
    u64 pc; unsigned na, nb;
    asm volatile("s_getpc_b64 %0\n.Lpsk_begin:\n\ts_mov_b32 %1, .Lpsk_end_a-.Lpsk_begin\n\ts_mov_b32 %2, .Lpsk_end_b-.Lpsk_begin" : "=s"(pc), "=s"(na), "=s"(nb));
    const int w3 = wave & 3;
    const unsigned id = (w3 - 1) * 64 + lane;
    unsigned vo;
    if (qkv) { const unsigned k = (wave - 1) * 64 + lane; vo = pick<unsigned>(k < 432, (rt * 216 + (k >> 1)) * 1152 + (k & 1) * 128, 0x80000000u); }
    else vo = pick<unsigned>(id < 72, (rt * 72 + id) * (F * 2), pick<unsigned>(id < 108, (rt * 36 + id - 72) * (F * 2) + 128, 0x80000000u));
    vo += col0 * 2;
    const bool cw = wave == 4 && r < 16;
    if (cw) vo = (r * 64 + lane) * 128;
    const unsigned nby = cw ? (na > nb ? na : nb) : qkv ? (wave ? D * 3 * D * 2 : 0) : w3 ? D * F * 2 : 0;
    load4(tc, vo, mk_rsrc(cw ? (const char*)pc : qkv ? A.wqkv : wave < 4 ? A.wg : A.wu, nby));
  }
  wait_vm<1>();
  pin(v); pin(v9); pin(ep);
  STAMP(1);
  const unsigned tag = rfl(ep) * 1664525u + 1013904223u;
  ln_row(v, v9, smem, wave, lane);
  lds_barrier();
  STAMP(2);
  if (wave == 0) { ln_publish(ws, smem, tag, bid, lane); STAMP(3); }
  asm volatile("s_barrier" ::: "memory");
  if (!qkv) body<false>(A, smem, tag, rt, col0, wave, lane, tc);
  else body<true>(A, smem, tag, rt, col0, wave, lane, tc);
}

}
extern "C" void kernel_launch(void* const* d_in, const int* in_sizes, int n_in, void* d_out, int out_size, void* d_ws, size_t ws_size,
                              hipStream_t stream) {
  psk::Args A;
  A.x = (const char*)d_in[0]; A.wqkv = (const char*)d_in[1]; A.wo = (const char*)d_in[2]; A.wg = (const char*)d_in[4];
  A.wu = (const char*)d_in[5]; A.wd = (const char*)d_in[6]; A.ws = (char*)d_ws; A.out = (char*)d_out;
  psk::fused<<<256, 512, 0, stream>>>(A);
}
```

```cpp
#include <hip/hip_runtime.h>
#include <utility>
#pragma clang diagnostic ignored "-Wint-to-pointer-cast"

typedef float    f32x4  __attribute__((ext_vector_type(4)));
typedef float    f32x2  __attribute__((ext_vector_type(2)));
typedef __bf16   bf16x8 __attribute__((ext_vector_type(8)));
typedef __bf16   bf16x4 __attribute__((ext_vector_type(4)));
typedef __bf16   bf16x2 __attribute__((ext_vector_type(2)));
typedef unsigned u32x4  __attribute__((ext_vector_type(4)));
typedef unsigned u32x2  __attribute__((ext_vector_type(2)));
typedef unsigned long long u64;
typedef __amdgpu_buffer_rsrc_t rsrc_t;
#define LDS __attribute__((address_space(3)))
#define DI __device__ __forceinline__
#define GAP() __builtin_amdgcn_sched_barrier(0)
#define CV(X) (decltype(X)::value)

namespace psk {

constexpr int S = 2048, D = 576, F = 4608;
constexpr int KSTOT = 168;
constexpr unsigned HBUF_B = 128u * KSTOT * 1024u;
constexpr unsigned SLAB_W = 12288, SLAB_B = 32u * 8u * 8u * SLAB_W;
constexpr unsigned XN_B = S * D * 2u, HALO_B = 64u * 5120u;
constexpr unsigned FS = 128;
constexpr unsigned FL_EPOCH = 0, FL_X = FS, FL_H = FL_X + 256 * FS, FL_S = FL_H + 72 * 96 * FS, FL_B = FL_S + 32 * 64 * FS;
constexpr size_t WS_H = 0, WS_SLAB = WS_H + HBUF_B, WS_XN = WS_SLAB + SLAB_B, WS_HALO = WS_XN + XN_B, WS_FL = WS_HALO + HALO_B, WS_DBG = WS_FL + FL_B;
constexpr unsigned SPIN_CAP = 1u << 18;

template <int N> using ic = std::integral_constant<int, N>;
template <class Fn, int... Is> DI void sfor_impl(Fn&& f, std::integer_sequence<int, Is...>) { (f(ic<Is>{}), ...); }
template <int N, class Fn> DI void sfor(Fn&& f) { sfor_impl(f, std::make_integer_sequence<int, N>{}); }

DI unsigned rfl(unsigned x) { return __builtin_amdgcn_readfirstlane(x); }
DI rsrc_t mk_rsrc(const void* p, unsigned bytes) { return __builtin_amdgcn_make_buffer_rsrc((void*)p, (short)0, (int)bytes, 0x00020000); }
DI unsigned cvtpk(float lo, float hi) { f32x2 v = {lo, hi}; return __builtin_bit_cast(unsigned, __builtin_convertvector(v, bf16x2)); }
DI float rbf(float x) { return __builtin_bit_cast(float, cvtpk(x, x) << 16); }
DI float bflo(unsigned v) { return __builtin_bit_cast(float, v << 16); }
DI float bfhi(unsigned v) { return __builtin_bit_cast(float, v & 0xffff0000u); }
DI bf16x4 trread(unsigned addr, int off) { return __builtin_amdgcn_ds_read_tr16_b64_v4bf16((LDS bf16x4*)(addr + off)); }
DI bf16x8 cat8(bf16x4 a, bf16x4 b) { return __builtin_shufflevector(a, b, 0, 1, 2, 3, 4, 5, 6, 7); }
DI bf16x8 as_bf(u32x4 v) { return __builtin_bit_cast(bf16x8, v); }
DI f32x4 mfma(bf16x8 a, bf16x8 b, f32x4 c) { return __builtin_amdgcn_mfma_f32_16x16x32_bf16(a, b, c, 0, 0, 0); }
DI void mfma_acc(f32x4& c, bf16x8 a, bf16x8 b) { asm volatile("v_mfma_f32_16x16x32_bf16 %0, %1, %2, %0" : "+v"(c) : "v"(a), "v"(b)); }
DI void mfma_new(f32x4& c, bf16x8 a, bf16x8 b) { asm volatile("v_mfma_f32_16x16x32_bf16 %0, %1, %2, 0" : "=v"(c) : "v"(a), "v"(b)); }
DI void mfma_drain() { asm volatile("s_nop 7\n\ts_nop 1"); GAP(); }
template <class T> DI void pin(T& x) { asm volatile("" : "+v"(x)); }
template <class T> DI void keep(T x) { asm volatile("" ::"v"(x)); }
DI unsigned lane_now() { unsigned l; asm volatile("v_mbcnt_lo_u32_b32 %0, -1, 0\n\tv_mbcnt_hi_u32_b32 %0, -1, %0" : "=v"(l)); return l; }
DI unsigned spin(unsigned x) { asm volatile("" : "+s"(x)); return x; }
template <int N> DI void wait_vm() { if constexpr (N < 63) asm volatile("s_waitcnt vmcnt(%0)" ::"i"(N) : "memory"); }
template <int N> DI void wait_barrier() { asm volatile("s_waitcnt vmcnt(%0) lgkmcnt(0)\n\ts_barrier" ::"i"(N) : "memory"); }
DI void lds_barrier() { asm volatile("s_waitcnt lgkmcnt(0)\n\ts_barrier" ::: "memory"); }
DI float v_mul(float a, float b) { float r; asm volatile("v_mul_f32 %0, %1, %2" : "=v"(r) : "v"(a), "v"(b)); return r; }
DI float v_expn(float a) { float r; asm volatile("v_exp_f32_e64 %0, -%1" : "=v"(r) : "v"(a)); return r; }
DI float v_fmak(float a, float k) { float r; asm volatile("v_fma_f32 %0, %1, %2, %2" : "=v"(r) : "v"(a), "s"(k)); return r; }
DI unsigned v_xors(unsigned a, unsigned k) { unsigned r; asm volatile("v_xor_b32 %0, %2, %1" : "=v"(r) : "v"(a), "s"(k)); return r; }
template <unsigned K> DI unsigned v_addk(unsigned a) { unsigned r; asm volatile("v_add_u32 %0, %2, %1" : "=v"(r) : "v"(a), "i"(K)); return r; }
DI float v_rcp(float a) { float r; asm volatile("v_rcp_f32 %0, %1" : "=v"(r) : "v"(a)); return r; }
DI unsigned v_cvtpk(float a, float b) { unsigned r; asm volatile("v_cvt_pk_bf16_f32 %0, %1, %2" : "=v"(r) : "v"(a), "v"(b)); return r; }

#define STAMP(i) do {} while (0)

template <int LOFF, unsigned GOFF> DI void dma16(unsigned m0base, unsigned voff, rsrc_t srd) {
  unsigned t;
  asm volatile("s_add_u32 m0, %1, %2\n\ts_mov_b32 %0, %3\n\tbuffer_load_dwordx4 %4, %5, %0 offen lds"
               : "=&s"(t) : "s"(m0base), "i"(LOFF), "i"(GOFF), "v"(voff), "s"(srd) : "scc", "memory");
}
template <int GOFF> DI void load16_sc1(u32x4& d, unsigned voff, unsigned sbase, rsrc_t srd) {
  unsigned t;
  asm volatile("s_add_u32 %1, %3, %4\n\tbuffer_load_dwordx4 %0, %2, %5, %1 offen sc1" : "=v"(d), "=&s"(t) : "v"(voff), "s"(sbase), "i"(GOFF), "s"(srd) : "scc");
}
template <int OFF> DI void store16_sc1(u32x4 d, rsrc_t srd, unsigned voff, unsigned soff) {
  asm volatile("buffer_store_dwordx4 %0, %1, %2, %3 offen offset:%4 sc1\n\ts_nop 1" ::"v"(d), "v"(voff), "s"(srd), "s"(soff), "i"(OFF) : "memory");
}
DI void store16(u32x4 d, rsrc_t srd, unsigned voff, unsigned soff) {
  asm volatile("buffer_store_dwordx4 %0, %1, %2, %3 offen sc1\n\ts_nop 1" ::"v"(d), "v"(voff), "s"(srd), "s"(soff) : "memory");
}
DI void load16(u32x4& d, unsigned voff, rsrc_t srd) { asm volatile("buffer_load_dwordx4 %0, %1, %2, 0 offen" : "=v"(d) : "v"(voff), "s"(srd)); }
DI void load4(unsigned& d, unsigned voff, rsrc_t srd) { asm volatile("buffer_load_dword %0, %1, %2, 0 offen" : "=v"(d) : "v"(voff), "s"(srd)); }
template <unsigned SOFF> DI void load4s(unsigned& d, unsigned voff, rsrc_t srd) {
  unsigned t;
  asm volatile("s_mov_b32 %1, %4\n\tbuffer_load_dword %0, %2, %3, %1 offen" : "=v"(d), "=&s"(t) : "v"(voff), "s"(srd), "i"(SOFF));
}
DI void load4_sc1(unsigned& d, unsigned voff, rsrc_t srd) { asm volatile("buffer_load_dword %0, %1, %2, 0 offen sc1" : "=v"(d) : "v"(voff), "s"(srd)); }
template <int N, int SLEEP = 2> DI void flags_wait(unsigned& f, unsigned voff, unsigned tag, u64 mask, rsrc_t srd) {
  unsigned cnt;
  asm volatile("s_waitcnt vmcnt(%6)\n\ts_mov_b32 %1, %7\n"
               "1:\n\tv_cmp_ne_u32 vcc, %3, %0\n\ts_nop 1\n\ts_and_b64 vcc, vcc, %4\n\ts_cbranch_scc0 2f\n\t"
               "s_sleep %8\n\tbuffer_load_dword %0, %2, %5, 0 offen sc1\n\ts_waitcnt vmcnt(0)\n\ts_sub_u32 %1, %1, 1\n\ts_cbranch_scc0 1b\n"
               "2:"
               : "+v"(f), "=&s"(cnt) : "v"(voff), "s"(tag), "s"(mask), "s"(srd), "i"(N), "i"(SPIN_CAP), "i"(SLEEP) : "vcc", "scc", "memory");
}
DI void publish(unsigned tag, unsigned soff, rsrc_t srd) {
  unsigned t;
  asm volatile("v_mov_b32 %0, %1\n\ts_waitcnt vmcnt(0)\n\ts_mov_b64 exec, 1\n\tbuffer_store_dword %0, off, %3, %2 sc1\n\ts_mov_b64 exec, -1"
               : "=&v"(t) : "s"(tag), "s"(soff), "s"(srd) : "memory");
}

template <bool TWO> DI void gather7(u32x4 (&p)[7], u32x4 (&q)[8], unsigned& f, unsigned fo, unsigned v0, unsigned v1, unsigned tag, unsigned src0, unsigned s0, rsrc_t sp, rsrc_t sq, rsrc_t sf) {
  unsigned pend, rdy, t, cnt;
  if constexpr (TWO) {
#define X(R) "buffer_load_dwordx4 " R ", %20, %26, %17 offen sc1\n"
  asm volatile("s_mov_b32 %15, 0x7f\n\ts_mov_b32 %18, %28\n"
               "1:\n\tv_cmp_eq_u32 vcc, %22, %14\n\ts_nop 1\n\ts_and_b32 %16, vcc_lo, %15\n\ts_andn2_b32 %15, %15, %16\n\t"
               "s_bitcmp1_b32 %16, 0\n\ts_cbranch_scc0 3f\n\ts_sub_u32 %17, %23, 0\n\ts_and_b32 %17, %17, 7\n\ts_mul_i32 %17, %17, %29\n\ts_add_u32 %17, %17, %24\n\t"
               "buffer_load_dwordx4 %0, %20, %25, %17 offen offset:0 sc1\n\t" X("%7") "3:\n\t"
               "s_bitcmp1_b32 %16, 1\n\ts_cbranch_scc0 3f\n\ts_sub_u32 %17, %23, 1\n\ts_and_b32 %17, %17, 7\n\ts_mul_i32 %17, %17, %29\n\ts_add_u32 %17, %17, %24\n\t"
               "buffer_load_dwordx4 %1, %20, %25, %17 offen offset:1024 sc1\n\t" X("%8") "3:\n\t"
               "s_bitcmp1_b32 %16, 2\n\ts_cbranch_scc0 3f\n\ts_sub_u32 %17, %23, 2\n\ts_and_b32 %17, %17, 7\n\ts_mul_i32 %17, %17, %29\n\ts_add_u32 %17, %17, %24\n\t"
               "buffer_load_dwordx4 %2, %20, %25, %17 offen offset:2048 sc1\n\t" X("%9") "3:\n\t"
               "s_bitcmp1_b32 %16, 3\n\ts_cbranch_scc0 3f\n\ts_sub_u32 %17, %23, 3\n\ts_and_b32 %17, %17, 7\n\ts_mul_i32 %17, %17, %29\n\ts_add_u32 %17, %17, %24\n\t"
               "buffer_load_dwordx4 %3, %20, %25, %17 offen offset:3072 sc1\n\t" X("%10") "3:\n\t"
               "s_bitcmp1_b32 %16, 4\n\ts_cbranch_scc0 3f\n\ts_sub_u32 %17, %23, 4\n\ts_and_b32 %17, %17, 7\n\ts_mul_i32 %17, %17, %29\n\ts_add_u32 %17, %17, %24\n\t"
               "buffer_load_dwordx4 %4, %21, %25, %17 offen offset:0 sc1\n\t" X("%11") "3:\n\t"
               "s_bitcmp1_b32 %16, 5\n\ts_cbranch_scc0 3f\n\ts_sub_u32 %17, %23, 5\n\ts_and_b32 %17, %17, 7\n\ts_mul_i32 %17, %17, %29\n\ts_add_u32 %17, %17, %24\n\t"
               "buffer_load_dwordx4 %5, %21, %25, %17 offen offset:1024 sc1\n\t" X("%12") "3:\n\t"
               "s_bitcmp1_b32 %16, 6\n\ts_cbranch_scc0 3f\n\ts_sub_u32 %17, %23, 6\n\ts_and_b32 %17, %17, 7\n\ts_mul_i32 %17, %17, %29\n\ts_add_u32 %17, %17, %24\n\t"
               "buffer_load_dwordx4 %6, %21, %25, %17 offen offset:2048 sc1\n\t" X("%13") "3:\n\t"
               "s_cmp_eq_u32 %15, 0\n\ts_cbranch_scc1 2f\n\ts_sleep 1\n\t"
               "s_mov_b32 exec_lo, %15\n\ts_mov_b32 exec_hi, 0\n\tbuffer_load_dword %14, %19, %27, 0 offen sc1\n\ts_mov_b64 exec, -1\n\t"
               "s_waitcnt vmcnt(0)\n\ts_sub_u32 %18, %18, 1\n\ts_cbranch_scc0 1b\n2:\n\ts_waitcnt vmcnt(0)"
               : "=&v"(p[0]), "=&v"(p[1]), "=&v"(p[2]), "=&v"(p[3]), "=&v"(p[4]), "=&v"(p[5]), "=&v"(p[6]), "=&v"(q[0]), "=&v"(q[1]), "=&v"(q[2]), "=&v"(q[3]), "=&v"(q[4]), "=&v"(q[5]), "=&v"(q[6]), "+v"(f), "=&s"(pend), "=&s"(rdy), "=&s"(t), "=&s"(cnt)
               : "v"(fo), "v"(v0), "v"(v1), "s"(tag), "s"(src0), "s"(s0), "s"(sp), "s"(sq), "s"(sf), "i"(SPIN_CAP), "i"(8 * SLAB_W) : "vcc", "scc", "memory");
#undef X
  } else {
#define X(R) "\n"
  asm volatile("s_mov_b32 %15, 0x7f\n\ts_mov_b32 %18, %28\n"
               "1:\n\tv_cmp_eq_u32 vcc, %22, %14\n\ts_nop 1\n\ts_and_b32 %16, vcc_lo, %15\n\ts_andn2_b32 %15, %15, %16\n\t"
               "s_bitcmp1_b32 %16, 0\n\ts_cbranch_scc0 3f\n\ts_sub_u32 %17, %23, 0\n\ts_and_b32 %17, %17, 7\n\ts_mul_i32 %17, %17, %29\n\ts_add_u32 %17, %17, %24\n\t"
               "buffer_load_dwordx4 %0, %20, %25, %17 offen offset:0 sc1\n\t" X("%7") "3:\n\t"
               "s_bitcmp1_b32 %16, 1\n\ts_cbranch_scc0 3f\n\ts_sub_u32 %17, %23, 1\n\ts_and_b32 %17, %17, 7\n\ts_mul_i32 %17, %17, %29\n\ts_add_u32 %17, %17, %24\n\t"
               "buffer_load_dwordx4 %1, %20, %25, %17 offen offset:1024 sc1\n\t" X("%8") "3:\n\t"
               "s_bitcmp1_b32 %16, 2\n\ts_cbranch_scc0 3f\n\ts_sub_u32 %17, %23, 2\n\ts_and_b32 %17, %17, 7\n\ts_mul_i32 %17, %17, %29\n\ts_add_u32 %17, %17, %24\n\t"
               "buffer_load_dwordx4 %2, %20, %25, %17 offen offset:2048 sc1\n\t" X("%9") "3:\n\t"
               "s_bitcmp1_b32 %16, 3\n\ts_cbranch_scc0 3f\n\ts_sub_u32 %17, %23, 3\n\ts_and_b32 %17, %17, 7\n\ts_mul_i32 %17, %17, %29\n\ts_add_u32 %17, %17, %24\n\t"
               "buffer_load_dwordx4 %3, %20, %25, %17 offen offset:3072 sc1\n\t" X("%10") "3:\n\t"
               "s_bitcmp1_b32 %16, 4\n\ts_cbranch_scc0 3f\n\ts_sub_u32 %17, %23, 4\n\ts_and_b32 %17, %17, 7\n\ts_mul_i32 %17, %17, %29\n\ts_add_u32 %17, %17, %24\n\t"
               "buffer_load_dwordx4 %4, %21, %25, %17 offen offset:0 sc1\n\t" X("%11") "3:\n\t"
               "s_bitcmp1_b32 %16, 5\n\ts_cbranch_scc0 3f\n\ts_sub_u32 %17, %23, 5\n\ts_and_b32 %17, %17, 7\n\ts_mul_i32 %17, %17, %29\n\ts_add_u32 %17, %17, %24\n\t"
               "buffer_load_dwordx4 %5, %21, %25, %17 offen offset:1024 sc1\n\t" X("%12") "3:\n\t"
               "s_bitcmp1_b32 %16, 6\n\ts_cbranch_scc0 3f\n\ts_sub_u32 %17, %23, 6\n\ts_and_b32 %17, %17, 7\n\ts_mul_i32 %17, %17, %29\n\ts_add_u32 %17, %17, %24\n\t"
               "buffer_load_dwordx4 %6, %21, %25, %17 offen offset:2048 sc1\n\t" X("%13") "3:\n\t"
               "s_cmp_eq_u32 %15, 0\n\ts_cbranch_scc1 2f\n\ts_sleep 1\n\t"
               "s_mov_b32 exec_lo, %15\n\ts_mov_b32 exec_hi, 0\n\tbuffer_load_dword %14, %19, %27, 0 offen sc1\n\ts_mov_b64 exec, -1\n\t"
               "s_waitcnt vmcnt(0)\n\ts_sub_u32 %18, %18, 1\n\ts_cbranch_scc0 1b\n2:\n\ts_waitcnt vmcnt(0)"
               : "=&v"(p[0]), "=&v"(p[1]), "=&v"(p[2]), "=&v"(p[3]), "=&v"(p[4]), "=&v"(p[5]), "=&v"(p[6]), "=&v"(q[0]), "=&v"(q[1]), "=&v"(q[2]), "=&v"(q[3]), "=&v"(q[4]), "=&v"(q[5]), "=&v"(q[6]), "+v"(f), "=&s"(pend), "=&s"(rdy), "=&s"(t), "=&s"(cnt)
               : "v"(fo), "v"(v0), "v"(v1), "s"(tag), "s"(src0), "s"(s0), "s"(sp), "s"(sq), "s"(sf), "i"(SPIN_CAP), "i"(8 * SLAB_W) : "vcc", "scc", "memory");
#undef X
  }
}
template <int OFF, int W> DI void store8_sc1(u32x2 d, rsrc_t srd, unsigned voff, unsigned wave) {
  asm volatile("s_cmp_lg_u32 %4, %5\n\ts_cbranch_scc1 1f\n\tbuffer_store_dwordx2 %0, %1, %2, 0 offen offset:%3 sc1\n1:" ::"v"(d), "v"(voff), "s"(srd), "i"(OFF), "s"(wave), "i"(W) : "scc", "memory");
}
DI void load8x5_sc1(u32x2* d, rsrc_t srd, unsigned voff, unsigned wave) {
  asm volatile("s_cmp_lg_u32 %7, 0\n\ts_cbranch_scc1 1f\n\tbuffer_load_dwordx2 %0, %5, %6, 0 offen sc1\n\tbuffer_load_dwordx2 %1, %5, %6, 0 offen offset:512 sc1\n\t"
               "buffer_load_dwordx2 %2, %5, %6, 0 offen offset:1024 sc1\n\tbuffer_load_dwordx2 %3, %5, %6, 0 offen offset:1536 sc1\n\tbuffer_load_dwordx2 %4, %5, %6, 0 offen offset:2048 sc1\n1:"
               : "=&v"(d[0]), "=&v"(d[1]), "=&v"(d[2]), "=&v"(d[3]), "=&v"(d[4]) : "v"(voff), "s"(srd), "s"(wave) : "scc");
}
DI void publish_if(unsigned tag, unsigned soff, rsrc_t srd, unsigned off) {
  unsigned t;
  asm volatile("s_cmp_lg_u32 %4, 0\n\ts_cbranch_scc1 1f\n\tv_mov_b32 %0, %1\n\ts_waitcnt vmcnt(0)\n\ts_mov_b64 exec, 1\n\tbuffer_store_dword %0, off, %3, %2 sc1\n\ts_mov_b64 exec, -1\n1:"
               : "=&v"(t) : "s"(tag), "s"(soff), "s"(srd), "s"(off) : "scc", "memory");
}
DI void flag_only(unsigned tag, unsigned soff, rsrc_t srd) {
  unsigned t;
  asm volatile("v_mov_b32 %0, %1\n\ts_mov_b64 exec, 1\n\tbuffer_store_dword %0, off, %3, %2 sc1\n\ts_mov_b64 exec, -1" : "=&v"(t) : "s"(tag), "s"(soff), "s"(srd) : "memory");
}
DI void load8_if(u32x2& d, unsigned voff, unsigned soff, rsrc_t srd, unsigned off) {
  asm volatile("s_cmp_lg_u32 %4, 0\n\ts_cbranch_scc1 1f\n\tbuffer_load_dwordx2 %0, %1, %2, %3 offen\n1:" : "=v"(d) : "v"(voff), "s"(srd), "s"(soff), "s"(off) : "scc");
}

template <class T> DI T pick(bool c, T a, T b) { return c ? a : b; }
DI unsigned vand(unsigned a, unsigned b) { return a & b; }
DI float fma4(float a, float b, float c, float d, const float (&k)[4]) {
  float r = a * k[0]; pin(r); r = __builtin_fmaf(b, k[1], r); r = __builtin_fmaf(c, k[2], r); return __builtin_fmaf(d, k[3], r);
}
DI float vmax(float a, float b) { float r; asm("v_max_f32 %0, %1, %2" : "=v"(r) : "v"(a), "v"(b)); return r; }
template <bool MAX> DI float x16(float v) {
  const unsigned u = __builtin_bit_cast(unsigned, v);
  auto a = __builtin_amdgcn_permlane16_swap(u, u, false, false);
  const float p = __builtin_bit_cast(float, (unsigned)a[0]), q = __builtin_bit_cast(float, (unsigned)a[1]);
  return MAX ? vmax(p, q) : p + q;
}
template <bool MAX> DI float x32(float v) {
  const unsigned u = __builtin_bit_cast(unsigned, v);
  auto a = __builtin_amdgcn_permlane32_swap(u, u, false, false);
  const float p = __builtin_bit_cast(float, (unsigned)a[0]), q = __builtin_bit_cast(float, (unsigned)a[1]);
  return MAX ? vmax(p, q) : p + q;
}
template <int W> DI void swp(float& a, float& b) {
  const unsigned x = __builtin_bit_cast(unsigned, a), y = __builtin_bit_cast(unsigned, b);
  if constexpr (W == 16) { auto r = __builtin_amdgcn_permlane16_swap(x, y, false, false); a = __builtin_bit_cast(float, (unsigned)r[0]); b = __builtin_bit_cast(float, (unsigned)r[1]); }
  else { auto r = __builtin_amdgcn_permlane32_swap(x, y, false, false); a = __builtin_bit_cast(float, (unsigned)r[0]); b = __builtin_bit_cast(float, (unsigned)r[1]); }
}
template <bool MAX> DI void xq8(float (&m)[8], float k) {
  auto op = [](float p, float q) { return MAX ? vmax(p, q) : p + q; };
  float a[4], d[4];
  sfor<4>([&](auto I) { swp<16>(m[2 * I], m[2 * I + 1]); }); GAP();
  sfor<4>([&](auto I) { a[I] = op(m[2 * I], m[2 * I + 1]); pin(a[I]); }); GAP();
  sfor<2>([&](auto I) { swp<32>(a[2 * I], a[2 * I + 1]); }); GAP();
  sfor<2>([&](auto I) { float c = op(a[2 * I], a[2 * I + 1]); if constexpr (!MAX) c = __builtin_amdgcn_rcpf(c) * k; d[2 * I] = c; d[2 * I + 1] = c; pin(d[2 * I + 1]); }); GAP();
  sfor<2>([&](auto I) { swp<32>(d[2 * I], d[2 * I + 1]); }); GAP();
  sfor<4>([&](auto I) { m[2 * I] = d[I]; m[2 * I + 1] = d[I]; pin(m[2 * I + 1]); }); GAP();
  sfor<4>([&](auto I) { swp<16>(m[2 * I], m[2 * I + 1]); }); GAP();
}
template <int CTRL> DI float dpp_add(float v) { return v + __builtin_bit_cast(float, __builtin_amdgcn_update_dpp(0, __builtin_bit_cast(int, v), CTRL, 0xf, 0xf, true)); }
DI float wave_sum(float v) { return x32<false>(x16<false>(dpp_add<0x140>(dpp_add<0x141>(dpp_add<0x4e>(dpp_add<0xb1>(v)))))); }

constexpr int LDS_V = 46080 + 2560, LDS_K = 92160 + 2560;
constexpr int LDS_X = 6 * 24576, LDS_B = LDS_X + 16384;
constexpr int DA = 4, PB = 52, SM = 67;
constexpr float LOG2E = 1.4426950408889634f;
constexpr int cmin(int a, int b) { return a < b ? a : b; }

enum { NONE, W1, XF, TOUCH, W2, H2, SAMPLE,   CHECK, PUB, PUBH, XRES, HALO };
struct Op { int kind, a, b; };
template <bool QKV> struct Plan {
  static constexpr int NCG = QKV ? 5 : 8, KS = QKV ? 6 : 9, NST = 54 / KS, SPU = 18 / KS, NSLOT = QKV ? 3 : 2;
  static constexpr int SB = QKV ? 6 * 5120 : 9 * 8192, IPS = KS * NCG, NIT = NST * IPS, BAR = IPS - (DA - 1), NPRO = 6;
  static constexpr bool kmajor(int n) { return QKV ? n % 90 < 75 : n < 96; }
  static constexpr int jof(int n) { return kmajor(n) ? n % NCG : QKV ? (n % 15) / 3 : n < 144 ? (n - 96) / 6 : (n % IPS) / KS; }
  static constexpr int sof(int n) { return kmajor(n) ? (n % (18 * NCG)) / NCG : QKV ? 15 + n % 3 : n < 144 ? 12 + n % 6 : KS * ((n / IPS) % SPU) + n % KS; }
  static constexpr int slot(int st) { return QKV ? (st + 1) % 3 : st % 2; }
  static constexpr int NT = QKV ? 12 : 8, NIT2 = 21 * NT, A2 = QKV ? 9 : 6, M1 = A2 * NT, M2 = M1 + (15 - A2) * (NT - 1), M3 = M2 + 6 * (NT - 1);
  static constexpr int SB2 = NT * 1024, NSL2 = 147456 / SB2, WIN = QKV ? 4 : 8, O2 = QKV ? 0 : 12;
  static constexpr int gt(int k) { return QKV ? (k + 8) % 12 : k; }
  static constexpr int st2(int m) { return m < M1 ? m / NT : m < M2 ? A2 + (m - M1) / (NT - 1) : m < M3 ? 15 + (m - M2) % 6 : A2 + m - M3; }
  static constexpr int ti2(int m) { return m < M1 ? m % NT : m < M3 ? gt(m < M2 ? (m - M1) % (NT - 1) : (m - M2) / 6) : 7; }
  static constexpr bool first2(int m) { return m < M1 ? m % NT == 0 : m < M2 ? (m - M1) % (NT - 1) == 0 : m < M2 + 6; }
  static constexpr int win2(int m) { return (st2(m) % NSL2) / WIN; }
  static constexpr int pwin2(int m) { for (int i = m - 1; i >= 0; --i) if (ti2(i) == ti2(m)) return win2(i); return -1; }
  static constexpr int bar2(int m) {
    if (QKV) return m == 57 ? 11 : m == M1 - 3 ? 14 : m == M2 - 3 ? 20 : -1;
    return m == 66 ? 17 : m == M2 ? 20 : -1;
  }
  static constexpr int hstep(int a) { return a < O2 ? a : a - 3; }
  static constexpr bool rebase(int m) {
    if (m >= NIT) return m - NIT < NIT2 && win2(m - NIT) != pwin2(m - NIT) && !(QKV && m - NIT < NT);
    if (QKV) return (m / IPS) % 3 != 1 && m >= IPS && m % IPS < 5;
    return m < 144 ? (m >= 72 && m < 80) : m % 9 == 0;
  }
  static constexpr int estart(int su) { return su == 0 ? 108 : 144 * su + 92; }
  static constexpr int eidx(int su, int n, int t) {
    if (n < estart(su) || n > estart(su) + 110 || (t == 1 && rebase(n + 4))) return -1;
    int c = 0;
    for (int i = estart(su); i < n; ++i) c += rebase(i + 4) ? 1 : 2;
    return c + t < 176 ? c + t : -1;
  }
  static constexpr int eat(int su, int e) { for (int n = estart(su);; ++n) if (eidx(su, n, 0) == e || eidx(su, n, 1) == e) return n; }
  static constexpr Op w2op(int i) {
    if (!QKV) return Op{W2, i, 0};
    if (i < 5) return i == 2 ? Op{W2, 0, 1} : i == 4 ? Op{W2, 2, 1} : Op{W2, i < 2 ? i : 2, 0};
    return (i - 5) % 3 == 2 ? Op{W2, 3 + (i - 5) / 3 * 2, 1} : Op{W2, 3 + (i - 5) / 3 * 2 + (i - 5) % 3, 0};
  }
  static constexpr int NPRE2 = QKV ? 0 : 21;
  static constexpr Op pre2(int i) { return i < 9 ? w2op(i) : Op{H2, (i - 9) / 2, (i - 9) & 1}; }
  static constexpr Op at1(int n) {
    const int st = n / IPS, it = n % IPS;
    if (QKV) {
      if (n % 2 == 0 && NPRO + n / 2 < 36) return Op{XF, NPRO + n / 2, 0};
      if (st == 2 && it % 2 == 0 && it < 16) return Op{W1, 3 + it / 8, (it / 2) % 4};
      if (st > 2 && st + 2 < NST && it % 3 == 1 && it < 12) return Op{W1, st + 2, it / 3};
      if (n == 206) return Op{PUBH, 0, 0};
      if (n == 232) return Op{SAMPLE, 0, 0};
      if (n == 254) return Op{CHECK, 3, 0};
      if (n == 255 || n == 257) return Op{HALO, (n - 255) / 2, 0};
      return Op{NONE, 0, 0};
    }
    if (n % 3 == 0 && NPRO + n / 3 < 36) return Op{XF, NPRO + n / 3, 0};
    if (st >= 1 && st <= 4) { const int i0 = st == 3 ? 10 : 1; if (it >= i0 && (it - i0) % 3 == 0 && (it - i0) / 3 < 9) return Op{W1, st + 1, (it - i0) / 3}; }
    if (n == 224) return Op{PUB, 0, 0};
    if (st >= 1 && st <= 3 && it == (st == 3 ? 37 : 31)) return Op{TOUCH, st + 2, 0};
    if (st >= 1 && st <= 3 && it == (st == 3 ? 39 : 33)) return Op{TOUCH, st - 1, 1};
    if (n == 330) return Op{SAMPLE, 0, 0};
    if (n == 352) return Op{PUB, 1, 0};
    if (n == 353) return Op{CHECK, 0, 0};
    if (n == 407) return Op{SAMPLE, 1, 0};
    if (n == 431) return Op{CHECK, 1, 0};
    if (n >= 358 && n % 3 == 1 && (n - 358) / 3 < 21) return pre2((n - 358) / 3);
    return Op{NONE, 0, 0};
  }
  static constexpr Op at2(int m) {
    if (QKV) {
      if (m == 12) return Op{PUB, 3, 0};
      if (m >= 14 && m < 44 && m % 3 == 2) return w2op(8 + (m - 14) / 3);
      if (m == 43) return Op{W2, 11, 2};
      if (m == 47) return Op{SAMPLE, 1, 0};
      if (m >= 58 && m < 68 && m % 2 == 0) return m == 60 ? Op{W2, 12, 2} : w2op(18 + (m - 58) / 2);
      if (m == 69) return Op{CHECK, 1, 0};
      if (m >= 70 && m < 94 && m % 2 == 0) return Op{H2, 9 + (m - 70) / 4, ((m - 70) / 2) & 1};
      if (m >= 106 && m < 124 && m % 2 == 0) return w2op(23 + (m - 106) / 2);
      if (m == 129) return Op{SAMPLE, 2, 0};
      if (m == 149) return Op{CHECK, 2, 0};
      if (m >= 150 && m < 168 && m % 3 != 2) { const int e = (m - 150) - (m - 150) / 3; return Op{H2, 15 + e / 2, e & 1}; }
      if (m >= 190 && m < 198 && m % 2 == 0) return Op{XRES, (m - 190) / 2, 0};
      return Op{NONE, 0, 0};
    }
    if (m < 42 && m % 2 == 0) { const int e = m / 2; return e < 18 ? (e % 3 == 2 ? w2op(9 + e / 3) : Op{H2, 6 + e / 3, e % 3}) : w2op(e - 3); }
    if (m == 31) return Op{SAMPLE, 1, 0};
    if (m == PB + 2) return Op{CHECK, 4, 0};
    if (m > PB + 2 && m < PB + 14 && m % 2 == 1) return Op{H2, 12 + (m - PB - 3) / 4, ((m - PB - 3) / 2) & 1};
    if (m == PB) return Op{PUB, 2, 0};
    if (m == PB + 1) return Op{TOUCH, 0, 2};
    if (m == SM) return Op{SAMPLE, 2, 0};
    if (m >= 70 && m < 76 && m % 2 == 0) return w2op(18 + (m - 70) / 2);
    if (m == SM + 20) return Op{CHECK, 2, 0};
    if (m > SM + 20 && m <= SM + 32) return Op{H2, 15 + (m - SM - 21) / 2, (m - SM - 21) & 1};
    if (m == 122 || m == 124) return Op{XRES, (m - 122) / 2, 0};
    return Op{NONE, 0, 0};
  }
  static constexpr int wait1(int n0, int kind, int amax) {
    int cnt = 0, need = -1;
    for (int i = -NPRO; i < n0; ++i) {
      const Op o = i < 0 ? Op{XF, i + NPRO, 0} : at1(i);
      if (o.kind == PUB) need = -1;
      if (o.kind == NONE || o.kind > SAMPLE) continue;
      ++cnt;
      if (o.kind == kind && o.a <= amax) need = cnt;
    }
    return need < 0 || cnt - need > 62 ? 63 : cnt - need;
  }
  static constexpr int wait2(int m0, int kind, int amax) {
    int cnt = 0, need = -1;
    for (int i = -NPRE2; i < m0; ++i) {
      const Op o = i < 0 ? pre2(i + NPRE2) : at2(i);
      if (o.kind == PUB) need = -1;
      if (o.kind == NONE || o.kind > SAMPLE) continue;
      ++cnt;
      if (o.kind == kind && o.a <= amax) need = cnt;
    }
    return need < 0 || cnt - need > 62 ? 63 : cnt - need;
  }
};
using PF = Plan<false>;
static_assert(PF::eat(0, 175) < 224 - 16 && PF::eat(1, 175) < 352 - 16 && PF::eat(2, 175) < PF::NIT + PB - 8, "a unit is published after its last store");
static_assert(PF::eat(0, 44) >= 96 + 26 && PF::eat(0, 88) >= 96 + 38 && PF::eat(0, 132) >= 96 + 50, "unit 0: accumulators complete before they are read");
static_assert(PF::eat(0, 39) < 144 && PF::eat(0, 83) < 162 && PF::eat(0, 127) < 180 && PF::eat(0, 171) < 198, "unit 0: accumulators read before they restart");
static_assert(PF::eat(1, 44) >= 216 + 38 && PF::eat(1, 88) >= 216 + 56 && PF::eat(1, 132) >= 216 + 74 && PF::eat(1, 171) < 288 + 54, "unit 1");
static_assert(PF::eat(0, 153) > 144 + 30 && PF::eat(0, 153) < 213 && PF::eat(1, 65) > 216 + 40 && PF::eat(1, 65) < 285, "early weight waits follow the last request");
static_assert(PF::eat(2, 44) >= 360 + 38 && PF::eat(2, 88) >= 360 + 56 && PF::eat(2, 132) >= 360 + 74, "unit 2");

struct Args { const char* x; const char* wqkv; const char* wo; const char* wg; const char* wu; const char* wd; char* ws; char* out; };

DI void ln_load(u32x4& v, unsigned& v9, const char* x, int row, int lane) {
  const rsrc_t sx = mk_rsrc(x, S * D * 2);
  const unsigned xo = (unsigned)(row * 1152);
  load16(v, xo + lane * 16, sx);
  asm volatile("buffer_load_ushort %0, %1, %2, 0 offen offset:1024" : "=v"(v9) : "v"(xo + lane * 2), "s"(sx));
}
DI void ln_row(u32x4 v, unsigned v9, unsigned smem, int wave, int lane) {
  float f[9];
  sfor<4>([&](auto E) { f[2 * E] = bflo(v[(int)E]); f[2 * E + 1] = bfhi(v[(int)E]); });
  f[8] = bflo(v9);
  const float mean = wave_sum(((f[0] + f[1]) + (f[2] + f[3])) + ((f[4] + f[5]) + (f[6] + f[7])) + f[8]) * (1.0f / D);
  float q = 0.f;
  sfor<9>([&](auto E) { const float d = f[E] - mean; q += d * d; });
  const float var = wave_sum(q) * (1.0f / D);
  const float mb = rbf(mean), rs = LOG2E * __builtin_amdgcn_rcpf(rbf(__builtin_amdgcn_sqrtf(rbf(rbf(var) + 1e-5f))));
  sfor<4>([&](auto E) { v[(int)E] = cvtpk((f[2 * E] - mb) * rs, (f[2 * E + 1] - mb) * rs); });
  *(LDS u32x4*)(smem + lane * 128 + wave * 16) = v;
  *(LDS unsigned short*)(smem + 8192 + (lane >> 3) * 128 + wave * 16 + (lane & 7) * 2) = (unsigned short)cvtpk((f[8] - mb) * rs, 0.f);
}
DI void ln_publish(char* ws, unsigned smem, unsigned tag, int gid, int lane) {
  u32x4 t[9];
  const unsigned fo = spin(rfl(FL_X + FS * gid));
  sfor<9>([&](auto I) { t[I] = *(LDS u32x4*)(smem + 1024 * I + lane * 16); });
  const unsigned o = (unsigned)((gid >> 1) * 18432 + (gid & 1) * 128 + (lane >> 3) * 256 + (lane & 7) * 16);
  const rsrc_t sxn = mk_rsrc(ws + WS_XN, XN_B);
  sfor<9>([&](auto I) { store16_sc1<0>(t[I], sxn, o, 2048 * I); });
  publish(tag, fo, mk_rsrc(ws + WS_FL, FL_B));
}

template <bool QKV>
DI void body(const Args& A, unsigned smem, unsigned tag, int rt, int col0, int wave, int lane, unsigned tch0) {
  using P = Plan<QKV>;
  constexpr int NCG = P::NCG, KS = P::KS, IPS = P::IPS, NIT = P::NIT, SPU = P::SPU, SB = P::SB, NT = P::NT, NIT2 = P::NIT2;
  constexpr int PL1 = QKV ? 3 : 4;
  constexpr unsigned RS = QKV ? 3u * D * 2u : F * 2u, WB = QKV ? D * 3 * D * 2 : D * F * 2;
  char* ws = A.ws;
  const int bid = blockIdx.x, isU = wave >> 2, w3 = wave & 3;
  const rsrc_t sF = mk_rsrc(ws + WS_FL, FL_B), sxn = mk_rsrc(ws + WS_XN, XN_B);
  const int cgl = ((lane & 7) >> 1) ^ ((lane >> 4) & 3);
  const int pc = 2 * cgl + (lane & 1);
  const unsigned wv = (unsigned)(((QKV ? 32 * isU : 0) + 16 * (w3 & 1) + 4 * (w3 >> 1) + 8 * (lane >> 5) + ((lane >> 3) & 3)) * RS + col0 * 2 + pc * 16);
  const unsigned wu = (unsigned)((8 * ((lane >> 3) & 3) + 4 * (lane >> 5) + ((lane >> 1) & 3)) * RS + col0 * 2 + 128);
  const char* wA = QKV ? A.wqkv : A.wg;
  const rsrc_t sw = mk_rsrc(isU && !QKV ? A.wu : wA, WB);
  const unsigned m0b = smem + isU * (QKV ? 5120 : SB / 2) + w3 * 1024;
  const int us = min(wave, 5);
  const unsigned m0u = smem + 4096 + us * 5120, wus = wu + us * 32 * RS;
  auto w1 = [&](auto ST, auto B, unsigned m0, unsigned vo, rsrc_t srd) {
    constexpr int a = CV(ST), b = CV(B) % (QKV ? 3 : 9), su = a / SPU;
    constexpr unsigned sucol = QKV ? (su == 0 ? 1152u : su == 1 ? 2304u : 0u) : su * 128u;
    dma16<P::slot(a) * SB + b * (QKV ? 10240 : 4096), 32 * (KS * (a % SPU) + (QKV ? 2 : 1) * b) * RS + sucol>(m0, vo, srd);
  };
  if (isU) {
    const rsrc_t sa = mk_rsrc(wA, WB);
    if constexpr (!QKV) sfor<2>([&](auto ST) { sfor<9>([&](auto B) { w1(ST, B, m0b - SB / 2, wv, sa); w1(ST, B, m0b, wv, sw); }); });
    else sfor<3>([&](auto ST) {
      sfor<3>([&](auto B) { w1(ST, B, m0b - 5120, wv - 32 * RS, sw); w1(ST, B, m0b, wv, sw); });
      w1(ST, ic<3>{}, smem + 4096 + w3 * 5120, wu + w3 * 32 * RS, sw); w1(ST, ic<3>{}, m0u, wus, sw);
    });
  }
  const int t16 = rt * 16 + 2 * wave;
  const unsigned xs = spin(t16 * 18432), l16 = lane * 16;
  u32x4 xf[18][2];
  auto xload = [&](auto AA) { constexpr int a = CV(AA); load16_sc1<(a / 2) * 1024 + (a & 1) * 18432>(xf[a / 2][a & 1], l16, xs, sxn); };
  if (wave == 0) {
    const unsigned fo = FL_X + FS * (rt * 32 + (lane & 31));
    __builtin_amdgcn_s_sleep(20);
    unsigned f; load4_sc1(f, fo, sF); flags_wait<0, 1>(f, fo, tag, ~0ull, sF);
  }
  asm volatile("s_barrier" ::: "memory");
  STAMP(4);
  sfor<P::NPRO>(xload);
  GAP();
  const int b2 = bid & 7, tile = bid >> 3, nc2 = QKV ? 768 : (tile - 3 * rt) * 256;
  const rsrc_t sh = mk_rsrc(ws + WS_H, HBUF_B), sS = mk_rsrc(ws + WS_SLAB, SLAB_B);
  const rsrc_t sWd = mk_rsrc(A.wd + 576 * 1152 * b2, 576 * 1152), sWo = mk_rsrc(A.wo + 72 * 1152 * b2, 64 * 1152), sWp = mk_rsrc(A.wo + (72 * b2 + 64) * 1152, 8 * 1152);
  const unsigned m0c = spin(smem + wave * 1024), m0d = spin(smem + 8192 + w3 * 1024 + isU * P::SB2);
  const unsigned hb = spin((t16 * KSTOT + 18 * b2) * 1024), ho = spin((t16 * KSTOT + 144 + 3 * b2) * 1024), hst = spin((t16 * KSTOT + (col0 >> 5)) * 1024);
  const unsigned oso = spin(t16 * 16 * 1152 + nc2), sl = spin(((tile * 8 + b2) * 8 + wave) * SLAB_W);
  const unsigned xt = spin(((wave - b2) & 7) >> 1), guest = spin((wave - b2) & 1);
  const unsigned fme = spin(FL_H + ((rt * 8 + wave) * 96 + (QKV ? 12 * b2 : 4 * (col0 / 192))) * FS);
  const unsigned tab = smem + LDS_X + wave * 2048 + l16;
  const bool wdo = tile * 8 + wave >= 162;
  unsigned ab[NCG];
  u32x4 t0;
  {
    const unsigned l = lane, b0 = smem + (l >> 2) * 128 + ((l >> 3) & 3) * 32 + 8 * (l & 3);
    t0[0] = (8 * w3 + (l >> 3)) * 1152 + nc2 + isU * 128 + pc * 16;
    t0[1] = FL_H + ((rt * 8 + wave) * 96 + 12 * b2 + (l < 12 ? l : 0)) * FS;
    t0[2] = b0;
    t0[3] = (l & 15) * 1152 + (l >> 4) * 8;
    *(LDS u32x4*)tab = t0;
    if constexpr (!QKV) *(LDS u32x2*)(tab + 1024) = u32x2{
      pick<unsigned>(l < 9, (rt * 36 + w3 * 9 + l) * RS + col0 * 2, 0x80000000u),
      pick<unsigned>(l < 11, (wdo ? tile * 8 + wave - 162 : tile * 8 + wave) * 4096 + l * 128, 0x80000000u)};
    if constexpr (QKV) sfor<5>([&](auto C) { ab[C] = (CV(C) < 4 ? b0 ^ (CV(C) * 32) : smem + 4096 + 8 * l) + SB; pin(ab[C]); });
    else sfor<8>([&](auto J) { ab[J] = (b0 ^ ((CV(J) >> 1) * 32)) + (CV(J) & 1) * (SB / 2); pin(ab[J]); });
  }
  u32x4 hf[21][2], hp[2], gp;
  unsigned fl, cur, wv3, tch[2], tk[3][2], cb[NT];
  u32x2 xres[4], pk[5][2], hl[10];
  unsigned l8 = lane * 8;
  if constexpr (QKV) pin(l8);
  const unsigned stg = smem + wave * 5120 + l8;
  auto ldtab = [&](int i) { return *(volatile LDS unsigned*)(tab + 1024 + 4 * i); };
  auto vmem = [&](auto K, auto AA, auto BB, auto NN) {
    constexpr int kind = CV(K), a = CV(AA), b = CV(BB);
    if constexpr (kind == W1) w1(AA, BB, QKV && b == 3 ? m0u : m0b, QKV && b == 3 ? wus : wv, sw);
    else if constexpr (kind == XF) xload(AA);
    else if constexpr (kind == TOUCH) {
      if constexpr (b == 2) load4_sc1(tch[0], (l16 & 0xf0) * 8 + (l16 & 0x100) * (KSTOT * 4) + (l16 >> 9) * 0x80000000u + hst + 4096, sh);
      else if constexpr (b == 0) load4s<288 * (a % 2) * RS + (a / 2) * 128>(tch[0], tch[0], sw);
      else load4s<a * 11 * 128>(tch[1], tch[1], mk_rsrc(wdo ? A.wo + 72 * 1152 * b2 : A.wd + 576 * 1152 * b2, wdo ? 72 * 1152 - 4096 : 576 * 1152 - 4096));
    } else if constexpr (kind == XRES) load8_if(xres[a], t0[3], oso + (a & 1) * 16 * 1152 + (a < 2 ? 32 * b2 : 256 + 32 * xt), mk_rsrc(A.x, S * D * 2), a < 2 ? 0u : guest);
    else if constexpr (kind == W2) {
      constexpr bool isO = a >= P::O2 && a < P::O2 + 3;
      constexpr int e = P::hstep(a), row = isO ? (a - P::O2 == 1 ? 32 : 0) : 192 * ((e % 6) / 2) + 64 * (e / 6) + 32 * (e & 1);
      const rsrc_t r = !isO ? sWd : a - P::O2 < 2 ? sWo : sWp;
      if constexpr (b == 0) dma16<(a % P::NSL2) * P::SB2, row * 1152>(m0c, t0[0], r);
      else if constexpr (b == 1 && !(isO && a == 2)) dma16<(a % P::NSL2) * P::SB2, row * 1152>(m0d, wv3, r);
      else dma16<(a % P::NSL2) * P::SB2 + 8192, row * 1152 + 256>(smem + w3 * 1024, t0[0] - isU * 128, r);
    } else if constexpr (kind == H2) {
      constexpr int e = P::hstep(a);
      if constexpr (a >= P::O2 && a < P::O2 + 3) load16_sc1<(b * KSTOT + a - P::O2) * 1024>(hf[a][b], l16, ho, sh);
      else load16_sc1<(b * KSTOT + 6 * ((e % 6) / 2) + 2 * (e / 6) + (e & 1)) * 1024>(hf[a][b], l16, hb, sh);
    } else if constexpr (kind == HALO) load8x5_sc1(hl + 5 * a, mk_rsrc(ws + WS_HALO + (bid - 200) * 5120 + a * 2560, rt > 0 ? 2560 : 0), l8, wave);
    else if constexpr (kind == SAMPLE) load4_sc1(fl, t0[1], sF);
    else if constexpr (kind == CHECK) flags_wait<CV(NN), QKV ? 4 : 2>(fl, t0[1], tag, a == 0 ? 0x111ull : a == 1 ? 0x222ull : a == 2 ? 0x444ull : a == 4 ? 0x8ull : wave == 0 && rt > 0 ? 0x80ull : 0ull, sF);
    else if constexpr (kind == PUB) publish(tag, fme + a * FS, sF);
    else if constexpr (kind == PUBH) publish_if(tag, fme + (96 + 7) * FS, sF, wave ^ 7);
  };
  f32x4 acc[NCG][2], acc2[NT][2];
  bf16x4 al[DA], ah[DA];
  auto aread = [&](auto N, auto T) {
    constexpr int n = CV(N);
    bf16x4 r;
    if constexpr (n >= NIT) r = trread(cb[P::ti2(n - NIT)], ((P::st2(n - NIT) % P::NSL2) % P::WIN) * P::SB2 + CV(T) * 2048);
    else if constexpr (QKV) r = trread(ab[P::jof(n)], (P::sof(n) % 12) * 5120 + CV(T) * (P::jof(n) < 4 ? 2048 : 512));
    else r = trread(n < 144 ? ab[P::jof(n)] : cur, (P::sof(n) % KS) * 4096 + CV(T) * 2048);
    if constexpr (CV(T) == 0) al[n % DA] = r; else ah[n % DA] = r;
  };
  auto rebase = [&](auto M) {
    constexpr int m = CV(M);
    if constexpr (m >= NIT) {
      constexpr int p = m - NIT, i = P::ti2(p);
      if constexpr (P::pwin2(p) >= 0) cb[i] = v_addk<(unsigned)((P::win2(p) - P::pwin2(p)) * P::WIN * P::SB2)>(cb[i]);
      else { const unsigned ph = i < 8 ? (i + b2 + 1) & 7 : i; cb[i] = v_xors(t0[2], (ph & 3) * 32 + (ph >> 2) * 4096); }
    } else if constexpr (QKV) ab[P::jof(m)] = v_addk<(m / IPS) % 3 == 0 ? (unsigned)SB : (unsigned)-SB>(ab[P::jof(m)]);
    else if constexpr (m < 144) ab[P::jof(m)] = v_addk<SB>(ab[P::jof(m)]);
    else cur = v_addk<(unsigned)((((m / IPS) & 1) - 1) * SB + (P::jof(m) & 1) * (SB / 2))>(ab[P::jof(m) & ~1]);
  };
  float ee, eg, eh[2];
  const float k2 = __builtin_bit_cast(float, spin(__builtin_bit_cast(unsigned, LOG2E * LOG2E)));
  auto epi = [&](auto SU, auto E) {
    constexpr int e = CV(E), w = e / 11, ph = e % 11, p = w >> 2, th = (w >> 1) & 1, r = 2 * (w & 1) + (ph >= 4 && ph != 5);
    if constexpr (ph == 0 || ph == 4) { if constexpr (ph == 4) eh[0] = ee; ee = v_expn(acc[2 * p][th][r]); }
    if constexpr (ph == 1 || ph == 6) eg = v_mul(acc[2 * p][th][r], acc[2 * p + 1][th][r]);
    if constexpr (ph == 2 || ph == 7) ee = v_fmak(ee, k2);
    if constexpr (ph == 3 || ph == 8) ee = v_rcp(ee);
    if constexpr (ph == 5) eh[0] = v_mul(eg, eh[0]);
    if constexpr (ph == 9) eh[1] = v_mul(eg, ee);
    if constexpr (ph == 10) {
      hp[th][2 * (p & 1) + (w & 1)] = v_cvtpk(eh[0], eh[1]);
      if constexpr ((p & 1) && (w & 1)) store16_sc1<(p >> 1) * 1024>(hp[th], sh, l16, hst + (th * KSTOT + 2 * CV(SU)) * 1024);
    }
  };
  auto filler = [&](auto N, auto T) {
    constexpr int n = CV(N), t = CV(T);
    if constexpr (t == 1 && P::rebase(n + 4)) rebase(ic<n + 4>{});
    else if constexpr (!QKV) sfor<3>([&](auto SU) { constexpr int e = P::eidx(CV(SU), n, t); if constexpr (e >= 0) epi(SU, ic<e>{}); });
    GAP();
  };
  auto epiq = [&](auto SU, auto E, auto T) {
    constexpr int su = CV(SU), e = CV(E) - 4, t = CV(T), cg = e / 3, th = e % 3;
    if constexpr (e >= 0 && cg < 5 && th < 2) {
      pk[cg][th][t] = v_cvtpk(acc[cg][th][2 * t], acc[cg][th][2 * t + 1]);
      if constexpr (su == 0 && t == 1) *(LDS u32x2*)(stg + LDS_K + th * 2560 + 512 * cg) = pk[cg][th];
      if constexpr (su < 2 && t == 1 && th == 1) store8_sc1<512 * cg, 7>(pk[cg][1], mk_rsrc(ws + WS_HALO + (bid - 192) * 5120 + su * 2560, 2560), l8, wave);
      GAP();
    }
  };
  GAP(); wait_barrier<P::NPRO - 2>(); GAP();
  keep(tch0);
  STAMP(5);
  __builtin_amdgcn_s_setprio(PL1 - 1);
  sfor<DA - 1>([&](auto N) { aread(N, ic<0>{}); aread(N, ic<1>{}); });
  GAP();
  sfor<NIT>([&](auto N) {
    constexpr int n = CV(N), st = n / IPS, it = n % IPS, j = P::jof(n), sg = P::sof(n), su = st / SPU, m = n + DA - 1;
    constexpr bool more = m < NIT || !QKV;
    if constexpr (QKV && n == NIT - (DA - 1)) { lds_barrier(); GAP(); }
    constexpr bool early = !QKV && (st == 2 || st == 3);
    if constexpr (early && n == P::eat(st - 2, st == 2 ? 153 : 65) - 1) { wait_vm<P::wait1(n, W1, st + 1)>(); GAP(); }
    if constexpr (it == P::BAR && st < P::NST - 1) { wait_barrier<early ? 63 : P::wait1(n, W1, st + 1)>(); GAP(); }
    if constexpr (!QKV && n == NIT - (DA - 1)) { wait_barrier<cmin(P::wait1(n, W2, 8), P::wait1(n, H2, 0))>(); GAP(); }
    { constexpr int pp = (it + DA - 1) % IPS; if constexpr (pp % (IPS / PL1) == 0) { __builtin_amdgcn_s_setprio(PL1 - 1 - pp / (IPS / PL1)); GAP(); } }
    if constexpr (su == 0 && j == 0 && n > 0) { wait_vm<P::wait1(n, XF, 2 * sg + 1)>(); GAP(); }
    const bf16x8 a = cat8(al[n % DA], ah[n % DA]);
    constexpr bool swp = QKV && su == 1;
    sfor<2>([&](auto T) {
      if constexpr (more) { aread(ic<m>{}, T); GAP(); }
      const bf16x8 b = as_bf(xf[sg][T]);
      if constexpr (sg == 0) mfma_new(acc[j][T], swp ? b : a, swp ? a : b); else mfma_acc(acc[j][T], swp ? b : a, swp ? a : b);
      GAP();
      filler(N, T);
      if constexpr (QKV && n % 90 >= 75) epiq(ic<su>{}, ic<n % 90 - 75>{}, T);
      if constexpr (QKV && n % 90 < 5 && su > 0) epiq(ic<su - 1>{}, ic<n % 90 + 15>{}, T);
    });
    if constexpr (QKV) {
      if constexpr (st == 8 && it < 10) *(LDS u32x2*)(stg + LDS_V + (it % 2) * 2560 + 512 * (it / 2)) = pk[it / 2][it % 2];
      if constexpr (n == 220) t0 = *(volatile LDS u32x4*)tab;
    } else {
      if constexpr (st >= 1 && st <= 3 && it == 24) { tch[0] = ldtab(0); tch[1] = ldtab(1); }
      if constexpr (st >= 1 && st <= 3 && it == 41) { tk[st - 1][0] = tch[0]; tk[st - 1][1] = tch[1]; }
      if constexpr (n == 200 || n == 230 || n == 354) { constexpr int q = n == 200 ? 0 : n == 230 ? 1 : 2; pin(tk[q][0]); pin(tk[q][1]); }
      if constexpr (n == 320) t0 = *(volatile LDS u32x4*)tab;
    }
    constexpr Op o = P::at1(n);
    if constexpr (o.kind != NONE) vmem(ic<o.kind>{}, ic<o.a>{}, ic<o.b>{}, ic<P::wait1(n, SAMPLE, 9)>{});
    GAP();
    if constexpr (n % (NIT / 9) == NIT / 9 - 1) STAMP(6 + n / (NIT / 9));
  });
  if constexpr (QKV) {
    mfma_drain();
    sfor<4>([&](auto J) { epiq(ic<2>{}, ic<16 + CV(J) / 2>{}, ic<CV(J) % 2>{}); });
    vmem(ic<SAMPLE>{}, ic<0>{}, ic<0>{}, ic<0>{});
    const int i16 = lane & 15, q4 = lane >> 4, odd = lane & 1;
    const bool cur0 = 4 * q4 + odd <= i16, cur1 = 4 * q4 + 2 + odd <= i16;
    const unsigned s0 = odd ? 0x01000c0cu : 0x0c0c0100u, s1 = odd ? 0x03020c0cu : 0x0c0c0302u;
    const unsigned sel[4] = {cur0 ? 0x0c0c0c0cu : s0, cur1 ? 0x0c0c0c0cu : s1, cur0 ? s0 : 0x0c0c0c0cu, cur1 ? s1 : 0x0c0c0c0cu};
    constexpr float SC = 0.23570226039551584f / LOG2E;
    auto ld2 = [&](unsigned a0, unsigned a1) { const u32x2 u = *(volatile LDS u32x2*)a0, v = *(volatile LDS u32x2*)a1; return as_bf(u32x4{u[0], u[1], v[0], v[1]}); };
    const float cp = t16 == 0 ? -1.0e30f : 0.f;
    unsigned ge[2], lt[2];
    sfor<2>([&](auto I) { ge[I] = (unsigned)((CV(I) - q4) >> 31); lt[I] = ~ge[I]; pin(lt[I]); pin(ge[I]); });
    auto mge = [&](unsigned v, auto C) { constexpr int c = CV(C); if constexpr (c <= 0) return v; else return v & ge[(c - 1) / 4]; };
    auto mlt = [&](unsigned v, auto C) { constexpr int c = CV(C); if constexpr (c <= 0) return 0u; else return v & lt[(c - 1) / 4]; };
    float ca[4], cc[4];
    sfor<4>([&](auto I) { const bool par = odd == (CV(I) & 1); ca[I] = par && cur0 == (CV(I) < 2) ? SC : 0.f; cc[I] = par && cur1 == (CV(I) < 2) ? SC : 0.f; pin(ca[I]); pin(cc[I]); });
    wv3 = t0[0] + 256 + isU * (32 * 1152 - 128); pin(wv3);
    wait_vm<1>();
    sfor<10>([&](auto I) { pin(hl[I]); });
    if (wave == 0) sfor<10>([&](auto I) { *(volatile LDS u32x2*)(stg + (CV(I) < 5 ? LDS_K : LDS_V) - 2560 + 512 * (CV(I) % 5)) = hl[I]; });
    f32x4 sp[8], sc[8];
    float l0[8], l1[8], mx[8];
    bf16x8 pb[8], kb[4][3], vb[2][5];
    sfor<12>([&](auto I) { constexpr int h = CV(I) / 3, g = CV(I) % 3; kb[h][g] = ld2(stg + LDS_K + (g - 1) * 2560 + 512 * h, stg + LDS_K + (g - 1) * 2560 + 512 * h + 512); });
    GAP();
    auto pf = [&](auto I) { constexpr Op o = P::w2op(CV(I)); GAP(); vmem(ic<o.kind>{}, ic<o.a>{}, ic<o.b>{}, ic<0>{}); GAP(); };
    auto ph = [&](auto I) { GAP(); vmem(ic<H2>{}, ic<3 + CV(I)>{}, ic<0>{}, ic<0>{}); vmem(ic<H2>{}, ic<3 + CV(I)>{}, ic<1>{}, ic<0>{}); GAP(); };
    pf(ic<0>{});
    sfor<4>([&](auto HH) {
      constexpr int hd = CV(HH);
      sfor<2>([&](auto TH) {
        const bf16x8 qb = as_bf(u32x4{mge(pk[hd][TH][0], ic<2 * hd>{}), mge(pk[hd][TH][1], ic<2 * hd - 2>{}), mlt(pk[hd + 1][TH][0], ic<2 * hd + 2>{}), mlt(pk[hd + 1][TH][1], ic<2 * hd>{})});
        sp[2 * hd + TH] = CV(TH) ? mfma(kb[hd][1], qb, f32x4{}) : mfma(kb[hd][0], qb, f32x4{cp, cp, cp, cp});
        sc[2 * hd + TH] = mfma(kb[hd][1 + TH], qb, f32x4{});
      });
      pf(ic<1 + hd>{});
    });
    sfor<10>([&](auto I) { constexpr int th = CV(I) / 5, j = CV(I) % 5; vb[th][j] = ld2(stg + LDS_V + (th - 1) * 2560 + 512 * j, stg + LDS_V + th * 2560 + 512 * j); });
    GAP();
    lds_barrier();
    vmem(ic<CHECK>{}, ic<0>{}, ic<0>{}, ic<5>{});
    sfor<8>([&](auto E) {
      l0[E] = fma4(sc[E][0], sc[E][1], sp[E][0], sp[E][1], ca);
      l1[E] = fma4(sc[E][2], sc[E][3], sp[E][2], sp[E][3], cc);
      mx[E] = vmax(l0[E], l1[E]); pin(mx[E]); pin(l0[E]); pin(l1[E]);
      if constexpr (CV(E) % 3 == 0) pf(ic<5 + CV(E) / 3>{});
    });
    ph(ic<0>{});
    xq8<true>(mx, 0.f);
    ph(ic<1>{});
    sfor<8>([&](auto E) { l0[E] = __builtin_amdgcn_exp2f(l0[E] - mx[E]); l1[E] = __builtin_amdgcn_exp2f(l1[E] - mx[E]); mx[E] = l0[E] + l1[E]; });
    ph(ic<2>{});
    xq8<false>(mx, 1.f / LOG2E);
    ph(ic<3>{});
    sfor<8>([&](auto E) {
      const unsigned pp = cvtpk(l0[E] * mx[E], l1[E] * mx[E]);
      pb[E] = as_bf(u32x4{__builtin_amdgcn_perm(pp, pp, sel[0]), __builtin_amdgcn_perm(pp, pp, sel[1]),
                          __builtin_amdgcn_perm(pp, pp, sel[2]), __builtin_amdgcn_perm(pp, pp, sel[3])});
    });
    ph(ic<4>{});
    sfor<8>([&](auto E) { sp[E] = mfma(vb[CV(E) & 1][CV(E) >> 1], pb[E], f32x4{}); sc[E] = mfma(vb[CV(E) & 1][(CV(E) >> 1) + 1], pb[E], f32x4{}); });
    ph(ic<5>{});
    sfor<NT>([&](auto I) { rebase(ic<NIT + CV(I)>{}); });
    wait_vm<12>();
    sfor<2>([&](auto TH) {
      unsigned ow[12];
      sfor<12>([&](auto E) {
        constexpr int j = CV(E) >> 1, e = CV(E) & 1;
        unsigned a = 0u, b = 0u;
        if constexpr (j < 4) a = mge(cvtpk(sp[2 * j + TH][2 * e], sp[2 * j + TH][2 * e + 1]), ic<2 * j - 2 * e>{});
        if constexpr (j > 0 && j < 5) b = mlt(cvtpk(sc[2 * j - 2 + TH][2 * e], sc[2 * j - 2 + TH][2 * e + 1]), ic<2 * j - 2 * e>{});
        ow[E] = a | b;
      });
      sfor<3>([&](auto M) { hf[M][TH] = u32x4{ow[4 * M], ow[4 * M + 1], ow[4 * M + 2], ow[4 * M + 3]}; pin(hf[M][TH]); store16_sc1<1024 * CV(M)>(hf[M][TH], sh, l16, ho + TH * KSTOT * 1024); });
    });
    STAMP(16);
    GAP(); lds_barrier(); GAP();
    sfor<DA - 1>([&](auto N) { aread(ic<NIT + CV(N)>{}, ic<0>{}); aread(ic<NIT + CV(N)>{}, ic<1>{}); });
    __builtin_amdgcn_s_setprio(3);
    GAP();
    STAMP(17);
  }
  sfor<NIT2>([&](auto M) {
    constexpr int m = CV(M), n = NIT + m, st = P::st2(m), i = P::ti2(m);
    if constexpr (P::bar2(m) >= 0) { wait_barrier<P::wait2(m, W2, P::bar2(m))>(); GAP(); }
    {
      constexpr int lo = [] { int k = m; while (k >= 0 && P::bar2(k) < 0) --k; return k < 0 ? -3 : k; }(), hi = [] { int k = m + 1; while (k < NIT2 && P::bar2(k) < 0) ++k; return k; }();
      constexpr int len = (hi - lo + 3) / 4;
      if constexpr ((m - lo) % len == 0) { __builtin_amdgcn_s_setprio(3 - (m - lo) / len); GAP(); }
    }
    if constexpr (P::first2(m) && st > 0) { wait_vm<P::wait2(m, H2, st)>(); GAP(); }
    const bf16x8 a = cat8(al[n % DA], ah[n % DA]);
    sfor<2>([&](auto T) {
      if constexpr (m + DA - 1 < NIT2) { aread(ic<n + DA - 1>{}, T); GAP(); }
      if constexpr (st == 0) mfma_new(acc2[i][T], a, as_bf(hf[st][T])); else mfma_acc(acc2[i][T], a, as_bf(hf[st][T]));
      GAP();
      filler(ic<n>{}, T);
      constexpr int c = m - P::M2 - 7;
      if constexpr (c >= 0 && c / 6 < NT - 1 && c % 6 < 4 && CV(T) == 0) {
        constexpr int tl = P::gt(c / 6), e = c % 6;
        gp[e] = v_cvtpk(acc2[tl][e >> 1][2 * (e & 1)], acc2[tl][e >> 1][2 * (e & 1) + 1]);
        if constexpr (e == 3) store16_sc1<0>(gp, sS, l16, sl + tl * 1024);
        GAP();
      }
    });
    if constexpr (!QKV && m == 118) pin(tch[0]);
    constexpr Op o = P::at2(m);
    if constexpr (o.kind != NONE) { vmem(ic<o.kind>{}, ic<o.a>{}, ic<o.b>{}, ic<P::wait2(m, SAMPLE, 9)>{}); GAP(); }
    if constexpr (m % (3 * NT) == 3 * NT - 1) STAMP(18 + m / (3 * NT));
  });
  unsigned fo = FL_S + (tile * 8 + wave) * 8 * FS + ((16 * (b2 + 7) - l16) & 0x70) * (FS / 16), f;
  if (!QKV && wave == 0 && bid == 0) {
    unsigned g[4];
    sfor<4>([&](auto I) { load4_sc1(g[I], FL_X + 64 * FS * I + 8 * l16, sF); });
    sfor<4>([&](auto I) { flags_wait<0, 2>(g[I], FL_X + 64 * FS * I + 8 * l16, tag, ~0ull, sF); });
    flag_only(tag, FL_EPOCH, sF);
  }
  load4_sc1(f, fo, sF);
  publish(tag, (unsigned)(FL_S + ((tile * 8 + wave) * 8 + b2) * FS), sF);
  STAMP(26);
  mfma_drain();
  pin(acc2[7][0]); pin(acc2[7][1]);
  sfor<QKV ? 4 : 2>([&](auto I) { pin(xres[I]); });
  u32x4 pv[7], pw[8];
  const rsrc_t sQ = mk_rsrc(ws + WS_SLAB + 8192 + xt * 1024, QKV && !guest ? SLAB_B - 12288 + 1024 : 0u);
  if constexpr (QKV) load16_sc1<0>(pw[7], l16, sl, sQ);
  lane = lane_now();
  const unsigned one = (lane >> 4) == ((lane >> 2) & 3) ? (lane & 1 ? 0x3f800000u : 0x3f80u) : 0u, e0 = lane & 2 ? 0u : one, e1 = lane & 2 ? one : 0u;
  const bf16x8 elo = as_bf(u32x4{e0, e1, 0u, 0u}), ehi = as_bf(u32x4{0u, 0u, e0, e1});
  const bf16x8 xb = as_bf(u32x4{xres[0][0], xres[0][1], xres[1][0], xres[1][1]});
  f32x4 o0 = mfma(elo, xb, acc2[7][0]), o1 = mfma(ehi, xb, acc2[7][1]);
  const unsigned xr = smem + LDS_X + wave * 2048;
  unsigned tw = xr + (lane & 15) * 48 + (lane >> 4) * 8, trd = xr + (lane >> 1) * 48 + (lane & 1) * 16, tst = (lane >> 1) * 1152 + (lane & 1) * 16;
  pin(tw); pin(trd); pin(tst); pin(o0); pin(o1);
  gather7<QKV>(pv, pw, f, fo, l16, l16 + 4096, tag, b2 + 7, spin((tile * 64 + wave) * SLAB_W), sS, sQ, sF);
  STAMP(28);
  const rsrc_t sO = mk_rsrc(A.out, S * D * 2);
  auto finish = [&](auto N, const u32x4* p, unsigned so) {
    sfor<CV(N)>([&](auto J) { o0 = mfma(elo, as_bf(p[J]), o0); o1 = mfma(ehi, as_bf(p[J]), o1); });
    *(LDS u32x2*)tw = u32x2{cvtpk(o0[0], o0[1]), cvtpk(o0[2], o0[3])}; *(LDS u32x2*)(tw + 768) = u32x2{cvtpk(o1[0], o1[1]), cvtpk(o1[2], o1[3])};
    asm volatile("" ::: "memory");
    store16(*(LDS u32x4*)trd, sO, tst, so);
  };
  if constexpr (QKV) pin(pw[7]);
  finish(ic<7>{}, pv, oso + 32 * b2);
  if (QKV && !guest) {
    const bf16x8 x8 = as_bf(u32x4{xres[2][0], xres[2][1], xres[3][0], xres[3][1]});
    o0 = mfma(elo, x8, f32x4{}); o1 = mfma(ehi, x8, f32x4{});
    finish(ic<8>{}, pw, oso + 256 + 32 * xt);
  }
  STAMP(29);
  if constexpr (QKV) asm volatile(".Lpsk_end_a:"); else asm volatile(".Lpsk_end_b:");
}

__global__ __launch_bounds__(512, 2) void fused(Args A) {
  __shared__ __attribute__((aligned(256))) char smem_[LDS_B];
  const unsigned smem = (unsigned)(size_t)(LDS char*)smem_;
  const int wave = rfl(threadIdx.x >> 6), lane = threadIdx.x & 63, bid = blockIdx.x, xcd = bid & 7, r = bid >> 3;
  char* ws = A.ws;
  STAMP(0);
  u32x4 v; unsigned v9, ep, tc;
  ln_load(v, v9, A.x, bid * 8 + wave, lane);
  load4_sc1(ep, FL_EPOCH, mk_rsrc(ws + WS_FL, FL_B));
  asm volatile("s_barrier" ::: "memory");
  const bool qkv = r >= 24;
  const int rt = qkv ? r - 24 : r / 3, col0 = qkv ? 72 * xcd : (xcd * 3 + r % 3) * 192;
  {
    u64 pc; unsigned na, nb;
    asm volatile("s_getpc_b64 %0\n.Lpsk_begin:\n\ts_mov_b32 %1, .Lpsk_end_a-.Lpsk_begin\n\ts_mov_b32 %2, .Lpsk_end_b-.Lpsk_begin" : "=s"(pc), "=s"(na), "=s"(nb));
    const int w3 = wave & 3;
    const unsigned id = (w3 - 1) * 64 + lane;
    unsigned vo;
    if (qkv) { const unsigned k = (wave - 1) * 64 + lane; vo = pick<unsigned>(k < 432, (rt * 216 + (k >> 1)) * 1152 + (k & 1) * 128, 0x80000000u); }
    else vo = pick<unsigned>(id < 72, (rt * 72 + id) * (F * 2), pick<unsigned>(id < 108, (rt * 36 + id - 72) * (F * 2) + 128, 0x80000000u));
    vo += col0 * 2;
    const bool cw = wave == 4 && r < 16;
    if (cw) vo = (r * 64 + lane) * 128;
    const unsigned nby = cw ? (na > nb ? na : nb) : qkv ? (wave ? D * 3 * D * 2 : 0) : w3 ? D * F * 2 : 0;
    load4(tc, vo, mk_rsrc(cw ? (const char*)pc : qkv ? A.wqkv : wave < 4 ? A.wg : A.wu, nby));
  }
  wait_vm<1>();
  pin(v); pin(v9); pin(ep);
  STAMP(1);
  const unsigned tag = rfl(ep) * 1664525u + 1013904223u;
  ln_row(v, v9, smem, wave, lane);
  lds_barrier();
  STAMP(2);
  if (wave == 0) { ln_publish(ws, smem, tag, bid, lane); STAMP(3); }
  asm volatile("s_barrier" ::: "memory");
  if (!qkv) body<false>(A, smem, tag, rt, col0, wave, lane, tc);
  else body<true>(A, smem, tag, rt, col0, wave, lane, tc);
}

}
extern "C" void kernel_launch(void* const* d_in, const int* in_sizes, int n_in, void* d_out, int out_size, void* d_ws, size_t ws_size,
                              hipStream_t stream) {
  psk::Args A;
  A.x = (const char*)d_in[0]; A.wqkv = (const char*)d_in[1]; A.wo = (const char*)d_in[2]; A.wg = (const char*)d_in[4];
  A.wu = (const char*)d_in[5]; A.wd = (const char*)d_in[6]; A.ws = (char*)d_ws; A.out = (char*)d_out;
  psk::fused<<<256, 512, 0, stream>>>(A);
}
```

```cpp
#include <hip/hip_runtime.h>
#include <utility>
#pragma clang diagnostic ignored "-Wint-to-pointer-cast"

typedef float    f32x4  __attribute__((ext_vector_type(4)));
typedef float    f32x2  __attribute__((ext_vector_type(2)));
typedef __bf16   bf16x8 __attribute__((ext_vector_type(8)));
typedef __bf16   bf16x4 __attribute__((ext_vector_type(4)));
typedef __bf16   bf16x2 __attribute__((ext_vector_type(2)));
typedef unsigned u32x4  __attribute__((ext_vector_type(4)));
typedef unsigned u32x2  __attribute__((ext_vector_type(2)));
typedef unsigned long long u64;
typedef __amdgpu_buffer_rsrc_t rsrc_t;
#define LDS __attribute__((address_space(3)))
#define DI __device__ __forceinline__
#define GAP() __builtin_amdgcn_sched_barrier(0)
#define CV(X) (decltype(X)::value)

#define W2X 1
#define GU 1
namespace psk {

constexpr int S = 2048, D = 576, F = 4608;
constexpr int KSTOT = 168;
constexpr unsigned HBUF_B = 128u * KSTOT * 1024u;
constexpr unsigned SLAB_W = 12288, SLAB_B = 32u * 8u * 8u * SLAB_W;
constexpr unsigned XN_B = S * D * 2u, HALO_B = 64u * 5120u;
constexpr unsigned FS = 128;
constexpr unsigned FL_EPOCH = 0, FL_X = FS, FL_H = FL_X + 256 * FS, FL_S = FL_H + 72 * 96 * FS, FL_B = FL_S + 32 * 64 * FS;
constexpr size_t WS_H = 0, WS_SLAB = WS_H + HBUF_B, WS_XN = WS_SLAB + SLAB_B, WS_HALO = WS_XN + XN_B, WS_FL = WS_HALO + HALO_B, WS_DBG = WS_FL + FL_B;
constexpr unsigned SPIN_CAP = 1u << 18;

template <int N> using ic = std::integral_constant<int, N>;
template <class Fn, int... Is> DI void sfor_impl(Fn&& f, std::integer_sequence<int, Is...>) { (f(ic<Is>{}), ...); }
template <int N, class Fn> DI void sfor(Fn&& f) { sfor_impl(f, std::make_integer_sequence<int, N>{}); }

DI unsigned rfl(unsigned x) { return __builtin_amdgcn_readfirstlane(x); }
DI rsrc_t mk_rsrc(const void* p, unsigned bytes) { return __builtin_amdgcn_make_buffer_rsrc((void*)p, (short)0, (int)bytes, 0x00020000); }
DI unsigned cvtpk(float lo, float hi) { f32x2 v = {lo, hi}; return __builtin_bit_cast(unsigned, __builtin_convertvector(v, bf16x2)); }
DI float rbf(float x) { return __builtin_bit_cast(float, cvtpk(x, x) << 16); }
DI float bflo(unsigned v) { return __builtin_bit_cast(float, v << 16); }
DI float bfhi(unsigned v) { return __builtin_bit_cast(float, v & 0xffff0000u); }
DI bf16x4 trread(unsigned addr, int off) { return __builtin_amdgcn_ds_read_tr16_b64_v4bf16((LDS bf16x4*)(addr + off)); }
DI bf16x8 cat8(bf16x4 a, bf16x4 b) { return __builtin_shufflevector(a, b, 0, 1, 2, 3, 4, 5, 6, 7); }
DI bf16x8 as_bf(u32x4 v) { return __builtin_bit_cast(bf16x8, v); }
DI f32x4 mfma(bf16x8 a, bf16x8 b, f32x4 c) { return __builtin_amdgcn_mfma_f32_16x16x32_bf16(a, b, c, 0, 0, 0); }
DI void mfma_acc(f32x4& c, bf16x8 a, bf16x8 b) { asm volatile("v_mfma_f32_16x16x32_bf16 %0, %1, %2, %0" : "+v"(c) : "v"(a), "v"(b)); }
DI void mfma_new(f32x4& c, bf16x8 a, bf16x8 b) { asm volatile("v_mfma_f32_16x16x32_bf16 %0, %1, %2, 0" : "=v"(c) : "v"(a), "v"(b)); }
DI void mfma_drain() { asm volatile("s_nop 7\n\ts_nop 1"); GAP(); }
template <class T> DI void pin(T& x) { asm volatile("" : "+v"(x)); }
template <class T> DI void keep(T x) { asm volatile("" ::"v"(x)); }
DI unsigned lane_now() { unsigned l; asm volatile("v_mbcnt_lo_u32_b32 %0, -1, 0\n\tv_mbcnt_hi_u32_b32 %0, -1, %0" : "=v"(l)); return l; }
DI unsigned spin(unsigned x) { asm volatile("" : "+s"(x)); return x; }
template <int N> DI void wait_vm() { if constexpr (N < 63) asm volatile("s_waitcnt vmcnt(%0)" ::"i"(N) : "memory"); }
template <int N> DI void wait_barrier() { asm volatile("s_waitcnt vmcnt(%0) lgkmcnt(0)\n\ts_barrier" ::"i"(N) : "memory"); }
DI void lds_barrier() { asm volatile("s_waitcnt lgkmcnt(0)\n\ts_barrier" ::: "memory"); }
DI float v_mul(float a, float b) { float r; asm volatile("v_mul_f32 %0, %1, %2" : "=v"(r) : "v"(a), "v"(b)); return r; }
DI float v_expn(float a) { float r; asm volatile("v_exp_f32_e64 %0, -%1" : "=v"(r) : "v"(a)); return r; }
DI float v_fmak(float a, float k) { float r; asm volatile("v_fma_f32 %0, %1, %2, %2" : "=v"(r) : "v"(a), "s"(k)); return r; }
DI unsigned v_xors(unsigned a, unsigned k) { unsigned r; asm volatile("v_xor_b32 %0, %2, %1" : "=v"(r) : "v"(a), "s"(k)); return r; }
template <unsigned K> DI unsigned v_addk(unsigned a) { unsigned r; asm volatile("v_add_u32 %0, %2, %1" : "=v"(r) : "v"(a), "i"(K)); return r; }
DI float v_rcp(float a) { float r; asm volatile("v_rcp_f32 %0, %1" : "=v"(r) : "v"(a)); return r; }
DI unsigned v_cvtpk(float a, float b) { unsigned r; asm volatile("v_cvt_pk_bf16_f32 %0, %1, %2" : "=v"(r) : "v"(a), "v"(b)); return r; }

#define STAMP(i) do {} while (0)

template <int LOFF, unsigned GOFF> DI void dma16(unsigned m0base, unsigned voff, rsrc_t srd) {
  unsigned t;
  asm volatile("s_add_u32 m0, %1, %2\n\ts_mov_b32 %0, %3\n\tbuffer_load_dwordx4 %4, %5, %0 offen lds"
               : "=&s"(t) : "s"(m0base), "i"(LOFF), "i"(GOFF), "v"(voff), "s"(srd) : "scc", "memory");
}
template <int GOFF> DI void load16_sc1(u32x4& d, unsigned voff, unsigned sbase, rsrc_t srd) {
  unsigned t;
  asm volatile("s_add_u32 %1, %3, %4\n\tbuffer_load_dwordx4 %0, %2, %5, %1 offen sc1" : "=v"(d), "=&s"(t) : "v"(voff), "s"(sbase), "i"(GOFF), "s"(srd) : "scc");
}
template <int OFF> DI void store16_sc1(u32x4 d, rsrc_t srd, unsigned voff, unsigned soff) {
  asm volatile("buffer_store_dwordx4 %0, %1, %2, %3 offen offset:%4 sc1\n\ts_nop 1" ::"v"(d), "v"(voff), "s"(srd), "s"(soff), "i"(OFF) : "memory");
}
DI void store16(u32x4 d, rsrc_t srd, unsigned voff, unsigned soff) {
  asm volatile("buffer_store_dwordx4 %0, %1, %2, %3 offen sc1\n\ts_nop 1" ::"v"(d), "v"(voff), "s"(srd), "s"(soff) : "memory");
}
DI void load16(u32x4& d, unsigned voff, rsrc_t srd) { asm volatile("buffer_load_dwordx4 %0, %1, %2, 0 offen" : "=v"(d) : "v"(voff), "s"(srd)); }
DI void load4(unsigned& d, unsigned voff, rsrc_t srd) { asm volatile("buffer_load_dword %0, %1, %2, 0 offen" : "=v"(d) : "v"(voff), "s"(srd)); }
template <unsigned SOFF> DI void load4s(unsigned& d, unsigned voff, rsrc_t srd) {
  unsigned t;
  asm volatile("s_mov_b32 %1, %4\n\tbuffer_load_dword %0, %2, %3, %1 offen" : "=v"(d), "=&s"(t) : "v"(voff), "s"(srd), "i"(SOFF));
}
DI void load4_sc1(unsigned& d, unsigned voff, rsrc_t srd) { asm volatile("buffer_load_dword %0, %1, %2, 0 offen sc1" : "=v"(d) : "v"(voff), "s"(srd)); }
template <int N, int SLEEP = 2> DI void flags_wait(unsigned& f, unsigned voff, unsigned tag, u64 mask, rsrc_t srd) {
  unsigned cnt;
  asm volatile("s_waitcnt vmcnt(%6)\n\ts_mov_b32 %1, %7\n"
               "1:\n\tv_cmp_ne_u32 vcc, %3, %0\n\ts_nop 1\n\ts_and_b64 vcc, vcc, %4\n\ts_cbranch_scc0 2f\n\t"
               "s_sleep %8\n\tbuffer_load_dword %0, %2, %5, 0 offen sc1\n\ts_waitcnt vmcnt(0)\n\ts_sub_u32 %1, %1, 1\n\ts_cbranch_scc0 1b\n"
               "2:"
               : "+v"(f), "=&s"(cnt) : "v"(voff), "s"(tag), "s"(mask), "s"(srd), "i"(N), "i"(SPIN_CAP), "i"(SLEEP) : "vcc", "scc", "memory");
}
DI void publish(unsigned tag, unsigned soff, rsrc_t srd) {
  unsigned t;
  asm volatile("v_mov_b32 %0, %1\n\ts_waitcnt vmcnt(0)\n\ts_mov_b64 exec, 1\n\tbuffer_store_dword %0, off, %3, %2 sc1\n\ts_mov_b64 exec, -1"
               : "=&v"(t) : "s"(tag), "s"(soff), "s"(srd) : "memory");
}

template <bool TWO> DI void gather7(u32x4 (&p)[7], u32x4 (&q)[8], unsigned& f, unsigned fo, unsigned v0, unsigned v1, unsigned tag, unsigned src0, unsigned s0, rsrc_t sp, rsrc_t sq, rsrc_t sf) {
  unsigned pend, rdy, t, cnt;
  if constexpr (TWO) {
#define X(R) "buffer_load_dwordx4 " R ", %20, %26, %17 offen sc1\n"
  asm volatile("s_mov_b32 %15, 0x7f\n\ts_mov_b32 %18, %28\n"
               "1:\n\tv_cmp_eq_u32 vcc, %22, %14\n\ts_nop 1\n\ts_and_b32 %16, vcc_lo, %15\n\ts_andn2_b32 %15, %15, %16\n\t"
               "s_bitcmp1_b32 %16, 0\n\ts_cbranch_scc0 3f\n\ts_sub_u32 %17, %23, 0\n\ts_and_b32 %17, %17, 7\n\ts_mul_i32 %17, %17, %29\n\ts_add_u32 %17, %17, %24\n\t"
               "buffer_load_dwordx4 %0, %20, %25, %17 offen offset:0 sc1\n\t" X("%7") "3:\n\t"
               "s_bitcmp1_b32 %16, 1\n\ts_cbranch_scc0 3f\n\ts_sub_u32 %17, %23, 1\n\ts_and_b32 %17, %17, 7\n\ts_mul_i32 %17, %17, %29\n\ts_add_u32 %17, %17, %24\n\t"
               "buffer_load_dwordx4 %1, %20, %25, %17 offen offset:1024 sc1\n\t" X("%8") "3:\n\t"
               "s_bitcmp1_b32 %16, 2\n\ts_cbranch_scc0 3f\n\ts_sub_u32 %17, %23, 2\n\ts_and_b32 %17, %17, 7\n\ts_mul_i32 %17, %17, %29\n\ts_add_u32 %17, %17, %24\n\t"
               "buffer_load_dwordx4 %2, %20, %25, %17 offen offset:2048 sc1\n\t" X("%9") "3:\n\t"
               "s_bitcmp1_b32 %16, 3\n\ts_cbranch_scc0 3f\n\ts_sub_u32 %17, %23, 3\n\ts_and_b32 %17, %17, 7\n\ts_mul_i32 %17, %17, %29\n\ts_add_u32 %17, %17, %24\n\t"
               "buffer_load_dwordx4 %3, %20, %25, %17 offen offset:3072 sc1\n\t" X("%10") "3:\n\t"
               "s_bitcmp1_b32 %16, 4\n\ts_cbranch_scc0 3f\n\ts_sub_u32 %17, %23, 4\n\ts_and_b32 %17, %17, 7\n\ts_mul_i32 %17, %17, %29\n\ts_add_u32 %17, %17, %24\n\t"
               "buffer_load_dwordx4 %4, %21, %25, %17 offen offset:0 sc1\n\t" X("%11") "3:\n\t"
               "s_bitcmp1_b32 %16, 5\n\ts_cbranch_scc0 3f\n\ts_sub_u32 %17, %23, 5\n\ts_and_b32 %17, %17, 7\n\ts_mul_i32 %17, %17, %29\n\ts_add_u32 %17, %17, %24\n\t"
               "buffer_load_dwordx4 %5, %21, %25, %17 offen offset:1024 sc1\n\t" X("%12") "3:\n\t"
               "s_bitcmp1_b32 %16, 6\n\ts_cbranch_scc0 3f\n\ts_sub_u32 %17, %23, 6\n\ts_and_b32 %17, %17, 7\n\ts_mul_i32 %17, %17, %29\n\ts_add_u32 %17, %17, %24\n\t"
               "buffer_load_dwordx4 %6, %21, %25, %17 offen offset:2048 sc1\n\t" X("%13") "3:\n\t"
               "s_cmp_eq_u32 %15, 0\n\ts_cbranch_scc1 2f\n\ts_sleep 1\n\t"
               "s_mov_b32 exec_lo, %15\n\ts_mov_b32 exec_hi, 0\n\tbuffer_load_dword %14, %19, %27, 0 offen sc1\n\ts_mov_b64 exec, -1\n\t"
               "s_waitcnt vmcnt(0)\n\ts_sub_u32 %18, %18, 1\n\ts_cbranch_scc0 1b\n2:\n\ts_waitcnt vmcnt(0)"
               : "=&v"(p[0]), "=&v"(p[1]), "=&v"(p[2]), "=&v"(p[3]), "=&v"(p[4]), "=&v"(p[5]), "=&v"(p[6]), "=&v"(q[0]), "=&v"(q[1]), "=&v"(q[2]), "=&v"(q[3]), "=&v"(q[4]), "=&v"(q[5]), "=&v"(q[6]), "+v"(f), "=&s"(pend), "=&s"(rdy), "=&s"(t), "=&s"(cnt)
               : "v"(fo), "v"(v0), "v"(v1), "s"(tag), "s"(src0), "s"(s0), "s"(sp), "s"(sq), "s"(sf), "i"(SPIN_CAP), "i"(8 * SLAB_W) : "vcc", "scc", "memory");
#undef X
  } else {
#define X(R) "\n"
  asm volatile("s_mov_b32 %15, 0x7f\n\ts_mov_b32 %18, %28\n"
               "1:\n\tv_cmp_eq_u32 vcc, %22, %14\n\ts_nop 1\n\ts_and_b32 %16, vcc_lo, %15\n\ts_andn2_b32 %15, %15, %16\n\t"
               "s_bitcmp1_b32 %16, 0\n\ts_cbranch_scc0 3f\n\ts_sub_u32 %17, %23, 0\n\ts_and_b32 %17, %17, 7\n\ts_mul_i32 %17, %17, %29\n\ts_add_u32 %17, %17, %24\n\t"
               "buffer_load_dwordx4 %0, %20, %25, %17 offen offset:0 sc1\n\t" X("%7") "3:\n\t"
               "s_bitcmp1_b32 %16, 1\n\ts_cbranch_scc0 3f\n\ts_sub_u32 %17, %23, 1\n\ts_and_b32 %17, %17, 7\n\ts_mul_i32 %17, %17, %29\n\ts_add_u32 %17, %17, %24\n\t"
               "buffer_load_dwordx4 %1, %20, %25, %17 offen offset:1024 sc1\n\t" X("%8") "3:\n\t"
               "s_bitcmp1_b32 %16, 2\n\ts_cbranch_scc0 3f\n\ts_sub_u32 %17, %23, 2\n\ts_and_b32 %17, %17, 7\n\ts_mul_i32 %17, %17, %29\n\ts_add_u32 %17, %17, %24\n\t"
               "buffer_load_dwordx4 %2, %20, %25, %17 offen offset:2048 sc1\n\t" X("%9") "3:\n\t"
               "s_bitcmp1_b32 %16, 3\n\ts_cbranch_scc0 3f\n\ts_sub_u32 %17, %23, 3\n\ts_and_b32 %17, %17, 7\n\ts_mul_i32 %17, %17, %29\n\ts_add_u32 %17, %17, %24\n\t"
               "buffer_load_dwordx4 %3, %20, %25, %17 offen offset:3072 sc1\n\t" X("%10") "3:\n\t"
               "s_bitcmp1_b32 %16, 4\n\ts_cbranch_scc0 3f\n\ts_sub_u32 %17, %23, 4\n\ts_and_b32 %17, %17, 7\n\ts_mul_i32 %17, %17, %29\n\ts_add_u32 %17, %17, %24\n\t"
               "buffer_load_dwordx4 %4, %21, %25, %17 offen offset:0 sc1\n\t" X("%11") "3:\n\t"
               "s_bitcmp1_b32 %16, 5\n\ts_cbranch_scc0 3f\n\ts_sub_u32 %17, %23, 5\n\ts_and_b32 %17, %17, 7\n\ts_mul_i32 %17, %17, %29\n\ts_add_u32 %17, %17, %24\n\t"
               "buffer_load_dwordx4 %5, %21, %25, %17 offen offset:1024 sc1\n\t" X("%12") "3:\n\t"
               "s_bitcmp1_b32 %16, 6\n\ts_cbranch_scc0 3f\n\ts_sub_u32 %17, %23, 6\n\ts_and_b32 %17, %17, 7\n\ts_mul_i32 %17, %17, %29\n\ts_add_u32 %17, %17, %24\n\t"
               "buffer_load_dwordx4 %6, %21, %25, %17 offen offset:2048 sc1\n\t" X("%13") "3:\n\t"
               "s_cmp_eq_u32 %15, 0\n\ts_cbranch_scc1 2f\n\ts_sleep 1\n\t"
               "s_mov_b32 exec_lo, %15\n\ts_mov_b32 exec_hi, 0\n\tbuffer_load_dword %14, %19, %27, 0 offen sc1\n\ts_mov_b64 exec, -1\n\t"
               "s_waitcnt vmcnt(0)\n\ts_sub_u32 %18, %18, 1\n\ts_cbranch_scc0 1b\n2:\n\ts_waitcnt vmcnt(0)"
               : "=&v"(p[0]), "=&v"(p[1]), "=&v"(p[2]), "=&v"(p[3]), "=&v"(p[4]), "=&v"(p[5]), "=&v"(p[6]), "=&v"(q[0]), "=&v"(q[1]), "=&v"(q[2]), "=&v"(q[3]), "=&v"(q[4]), "=&v"(q[5]), "=&v"(q[6]), "+v"(f), "=&s"(pend), "=&s"(rdy), "=&s"(t), "=&s"(cnt)
               : "v"(fo), "v"(v0), "v"(v1), "s"(tag), "s"(src0), "s"(s0), "s"(sp), "s"(sq), "s"(sf), "i"(SPIN_CAP), "i"(8 * SLAB_W) : "vcc", "scc", "memory");
#undef X
  }
}
template <int OFF, int W> DI void store8_sc1(u32x2 d, rsrc_t srd, unsigned voff, unsigned wave) {
  asm volatile("s_cmp_lg_u32 %4, %5\n\ts_cbranch_scc1 1f\n\tbuffer_store_dwordx2 %0, %1, %2, 0 offen offset:%3 sc1\n1:" ::"v"(d), "v"(voff), "s"(srd), "i"(OFF), "s"(wave), "i"(W) : "scc", "memory");
}
DI void load8x5_sc1(u32x2* d, rsrc_t srd, unsigned voff, unsigned wave) {
  asm volatile("s_cmp_lg_u32 %7, 0\n\ts_cbranch_scc1 1f\n\tbuffer_load_dwordx2 %0, %5, %6, 0 offen sc1\n\tbuffer_load_dwordx2 %1, %5, %6, 0 offen offset:512 sc1\n\t"
               "buffer_load_dwordx2 %2, %5, %6, 0 offen offset:1024 sc1\n\tbuffer_load_dwordx2 %3, %5, %6, 0 offen offset:1536 sc1\n\tbuffer_load_dwordx2 %4, %5, %6, 0 offen offset:2048 sc1\n1:"
               : "=&v"(d[0]), "=&v"(d[1]), "=&v"(d[2]), "=&v"(d[3]), "=&v"(d[4]) : "v"(voff), "s"(srd), "s"(wave) : "scc");
}
DI void publish_if(unsigned tag, unsigned soff, rsrc_t srd, unsigned off) {
  unsigned t;
  asm volatile("s_cmp_lg_u32 %4, 0\n\ts_cbranch_scc1 1f\n\tv_mov_b32 %0, %1\n\ts_waitcnt vmcnt(0)\n\ts_mov_b64 exec, 1\n\tbuffer_store_dword %0, off, %3, %2 sc1\n\ts_mov_b64 exec, -1\n1:"
               : "=&v"(t) : "s"(tag), "s"(soff), "s"(srd), "s"(off) : "scc", "memory");
}
DI void flag_only(unsigned tag, unsigned soff, rsrc_t srd) {
  unsigned t;
  asm volatile("v_mov_b32 %0, %1\n\ts_mov_b64 exec, 1\n\tbuffer_store_dword %0, off, %3, %2 sc1\n\ts_mov_b64 exec, -1" : "=&v"(t) : "s"(tag), "s"(soff), "s"(srd) : "memory");
}
DI void load8_if(u32x2& d, unsigned voff, unsigned soff, rsrc_t srd, unsigned off) {
  asm volatile("s_cmp_lg_u32 %4, 0\n\ts_cbranch_scc1 1f\n\tbuffer_load_dwordx2 %0, %1, %2, %3 offen\n1:" : "=v"(d) : "v"(voff), "s"(srd), "s"(soff), "s"(off) : "scc");
}

template <class T> DI T pick(bool c, T a, T b) { return c ? a : b; }
DI unsigned vand(unsigned a, unsigned b) { return a & b; }
DI float fma4(float a, float b, float c, float d, const float (&k)[4]) {
  float r = a * k[0]; pin(r); r = __builtin_fmaf(b, k[1], r); r = __builtin_fmaf(c, k[2], r); return __builtin_fmaf(d, k[3], r);
}
DI float vmax(float a, float b) { float r; asm("v_max_f32 %0, %1, %2" : "=v"(r) : "v"(a), "v"(b)); return r; }
template <bool MAX> DI float x16(float v) {
  const unsigned u = __builtin_bit_cast(unsigned, v);
  auto a = __builtin_amdgcn_permlane16_swap(u, u, false, false);
  const float p = __builtin_bit_cast(float, (unsigned)a[0]), q = __builtin_bit_cast(float, (unsigned)a[1]);
  return MAX ? vmax(p, q) : p + q;
}
template <bool MAX> DI float x32(float v) {
  const unsigned u = __builtin_bit_cast(unsigned, v);
  auto a = __builtin_amdgcn_permlane32_swap(u, u, false, false);
  const float p = __builtin_bit_cast(float, (unsigned)a[0]), q = __builtin_bit_cast(float, (unsigned)a[1]);
  return MAX ? vmax(p, q) : p + q;
}
template <int W> DI void swp(float& a, float& b) {
  const unsigned x = __builtin_bit_cast(unsigned, a), y = __builtin_bit_cast(unsigned, b);
  if constexpr (W == 16) { auto r = __builtin_amdgcn_permlane16_swap(x, y, false, false); a = __builtin_bit_cast(float, (unsigned)r[0]); b = __builtin_bit_cast(float, (unsigned)r[1]); }
  else { auto r = __builtin_amdgcn_permlane32_swap(x, y, false, false); a = __builtin_bit_cast(float, (unsigned)r[0]); b = __builtin_bit_cast(float, (unsigned)r[1]); }
}
template <bool MAX> DI void xq8(float (&m)[8], float k) {
  auto op = [](float p, float q) { return MAX ? vmax(p, q) : p + q; };
  float a[4], d[4];
  sfor<4>([&](auto I) { swp<16>(m[2 * I], m[2 * I + 1]); }); GAP();
  sfor<4>([&](auto I) { a[I] = op(m[2 * I], m[2 * I + 1]); pin(a[I]); }); GAP();
  sfor<2>([&](auto I) { swp<32>(a[2 * I], a[2 * I + 1]); }); GAP();
  sfor<2>([&](auto I) { float c = op(a[2 * I], a[2 * I + 1]); if constexpr (!MAX) c = __builtin_amdgcn_rcpf(c) * k; d[2 * I] = c; d[2 * I + 1] = c; pin(d[2 * I + 1]); }); GAP();
  sfor<2>([&](auto I) { swp<32>(d[2 * I], d[2 * I + 1]); }); GAP();
  sfor<4>([&](auto I) { m[2 * I] = d[I]; m[2 * I + 1] = d[I]; pin(m[2 * I + 1]); }); GAP();
  sfor<4>([&](auto I) { swp<16>(m[2 * I], m[2 * I + 1]); }); GAP();
}
template <int CTRL> DI float dpp_add(float v) { return v + __builtin_bit_cast(float, __builtin_amdgcn_update_dpp(0, __builtin_bit_cast(int, v), CTRL, 0xf, 0xf, true)); }
DI float wave_sum(float v) { return x32<false>(x16<false>(dpp_add<0x140>(dpp_add<0x141>(dpp_add<0x4e>(dpp_add<0xb1>(v)))))); }

constexpr int LDS_V = 46080 + 2560, LDS_K = 92160 + 2560;
constexpr int LDS_X = 6 * 24576, LDS_B = LDS_X + 16384;
constexpr int DA = 4, PB = 52, SM = 67;
constexpr float LOG2E = 1.4426950408889634f;
constexpr int cmin(int a, int b) { return a < b ? a : b; }

enum { NONE, W1, XF, TOUCH, W2, H2, SAMPLE,   CHECK, PUB, PUBH, XRES, HALO };
struct Op { int kind, a, b; };
template <bool QKV> struct Plan {
  static constexpr int NCG = QKV ? 5 : 8, KS = QKV ? 6 : 9, NST = 54 / KS, SPU = 18 / KS, NSLOT = QKV ? 3 : 2;
  static constexpr int SB = QKV ? 6 * 5120 : 9 * 8192, IPS = KS * NCG, NIT = NST * IPS, BAR = IPS - (DA - 1), NPRO = 6;
  static constexpr bool kmajor(int n) { return QKV ? n % 90 < 75 : n < 96; }
  static constexpr int jof(int n) { return kmajor(n) ? n % NCG : QKV ? (n % 15) / 3 : GU ? (n < 144 ? (n - 96) / 12 : (n % IPS) / 18) * 2 + n % 2 : n < 144 ? (n - 96) / 6 : (n % IPS) / KS; }
  static constexpr int sof(int n) { return kmajor(n) ? (n % (18 * NCG)) / NCG : QKV ? 15 + n % 3 : GU ? (n < 144 ? 12 + (n % 12) / 2 : KS * ((n / IPS) % SPU) + (n % 18) / 2) : n < 144 ? 12 + n % 6 : KS * ((n / IPS) % SPU) + n % KS; }
  static constexpr int slot(int st) { return QKV ? (st + 1) % 3 : st % 2; }
  static constexpr int NT = QKV ? 12 : 8, NIT2 = 21 * NT, A2 = QKV ? 9 : 6, M1 = A2 * NT, M2 = M1 + (15 - A2) * (NT - 1), M3 = M2 + 6 * (NT - 1);
  static constexpr int SB2 = NT * 1024, NSL2 = 147456 / SB2, WIN = QKV ? 4 : 8, O2 = QKV ? 0 : 12;
  static constexpr int gt(int k) { return QKV ? (k + 8) % 12 : k; }
  static constexpr int st2(int m) { return m < M1 ? m / NT : m < M2 ? A2 + (m - M1) / (NT - 1) : m < M3 ? 15 + (m - M2) % 6 : A2 + m - M3; }
  static constexpr int ti2(int m) { return m < M1 ? m % NT : m < M3 ? gt(m < M2 ? (m - M1) % (NT - 1) : (m - M2) / 6) : 7; }
  static constexpr bool first2(int m) { return m < M1 ? m % NT == 0 : m < M2 ? (m - M1) % (NT - 1) == 0 : m < M2 + 6; }
  static constexpr int win2(int m) { return (st2(m) % NSL2) / WIN; }
  static constexpr int pwin2(int m) { for (int i = m - 1; i >= 0; --i) if (ti2(i) == ti2(m)) return win2(i); return -1; }
  static constexpr int bar2(int m) {
    if (QKV) return m == 57 ? 11 : m == M1 - 3 ? 14 : m == M2 - 3 ? 20 : -1;
    return m == 66 ? 17 : m == M2 ? 20 : -1;
  }
  static constexpr int hstep(int a) { return a < O2 ? a : a - 3; }
  static constexpr bool rebase(int m) {
    if (m >= NIT) return m - NIT < NIT2 && win2(m - NIT) != pwin2(m - NIT) && !(QKV && m - NIT < NT);
    if (QKV) return (m / IPS) % 3 != 1 && m >= IPS && m % IPS < 5;
    return m < 144 ? (m >= 72 && m < 80) : GU ? m % 18 < 2 : m % 9 == 0;
  }
  static constexpr int estart(int su) { return su == 0 ? 108 : 144 * su + 92; }
  static constexpr int eidx(int su, int n, int t) {
    if (n < estart(su) || n > estart(su) + 100 || (t == 1 && rebase(n + 4))) return -1;
    int c = 0;
    for (int i = estart(su); i < n; ++i) c += rebase(i + 4) ? 1 : 2;
    return c + t < 176 ? c + t : -1;
  }
  static constexpr int eat(int su, int e) { int n = estart(su); for (int c = 0; (c += rebase(n + 4) ? 1 : 2) <= e;) ++n; return n; }
  static constexpr Op w2op(int i) {
    if (!QKV) return Op{W2, i, 0};
    if (i < 5) return i == 2 ? Op{W2, 0, 1} : i == 4 ? Op{W2, 2, 1} : Op{W2, i < 2 ? i : 2, 0};
    return (i - 5) % 3 == 2 ? Op{W2, 3 + (i - 5) / 3 * 2, 1} : Op{W2, 3 + (i - 5) / 3 * 2 + (i - 5) % 3, 0};
  }
  static constexpr int NPRE2 = QKV ? 0 : 21;
  static constexpr Op pre2(int i) { return i < 9 ? w2op(i) : Op{H2, (i - 9) / 2, (i - 9) & 1}; }
  static constexpr Op at1(int n) {
    const int st = n / IPS, it = n % IPS;
    if (QKV) {
      if (n % 2 == 0 && NPRO + n / 2 < 36) return Op{XF, NPRO + n / 2, 0};
      if (st == 2 && it % 2 == 0 && it < 16) return Op{W1, 3 + it / 8, (it / 2) % 4};
      if (st > 2 && st + 2 < NST && it % 3 == 1 && it < 12) return Op{W1, st + 2, it / 3};
      if (n == 206) return Op{PUBH, 0, 0};
      if (n == 232) return Op{SAMPLE, 0, 0};
      if (n == 254) return Op{CHECK, 3, 0};
      if (n == 255 || n == 257) return Op{HALO, (n - 255) / 2, 0};
      return Op{NONE, 0, 0};
    }
    if (n % 3 == 0 && NPRO + n / 3 < 36) return Op{XF, NPRO + n / 3, 0};
    if (st >= 1 && st <= 4) { const int i0 = st == 3 ? 10 : 1; if (it >= i0 && (it - i0) % 3 == 0 && (it - i0) / 3 < 9) return Op{W1, st + 1, (it - i0) / 3}; }
    if (n == 224) return Op{PUB, 0, 0};
    if (st >= 1 && st <= 3 && it == (st == 3 ? 37 : 31)) return Op{TOUCH, st + 2, 0};
    if (st >= 1 && st <= 3 && it == (st == 3 ? 39 : 33)) return Op{TOUCH, st - 1, 1};
    if (n == 330) return Op{SAMPLE, 0, 0};
    if (n == 352) return Op{PUB, 1, 0};
    if (n == 353) return Op{CHECK, 0, 0};
    if (n == 407) return Op{SAMPLE, 1, 0};
    if (n == 431) return Op{CHECK, 1, 0};
    if (n >= 358 && n % 3 == 1 && (n - 358) / 3 < 21) return pre2((n - 358) / 3);
    return Op{NONE, 0, 0};
  }
  static constexpr Op at2(int m) {
    if (QKV) {
      if (m == 12) return Op{PUB, 3, 0};
      if (m >= 14 && m < 44 && m % 3 == 2) return w2op(8 + (m - 14) / 3);
      if (m == 43) return Op{W2, 11, 2};
      if (m == 47) return Op{SAMPLE, 1, 0};
      if (m >= 58 && m < 68 && m % 2 == 0) return m == 60 ? Op{W2, 12, 2} : w2op(18 + (m - 58) / 2);
      if (m == 69) return Op{CHECK, 1, 0};
      if (m >= 70 && m < 94 && m % 2 == 0) return Op{H2, 9 + (m - 70) / 4, ((m - 70) / 2) & 1};
      if (m >= 106 && m < 124 && m % 2 == 0) return w2op(23 + (m - 106) / 2);
      if (m == 129) return Op{SAMPLE, 2, 0};
      if (m == 149) return Op{CHECK, 2, 0};
      if (m >= 150 && m < 168 && m % 3 != 2) { const int e = (m - 150) - (m - 150) / 3; return Op{H2, 15 + e / 2, e & 1}; }
      if (m >= 190 && m < 198 && m % 2 == 0) return Op{XRES, (m - 190) / 2, 0};
      return Op{NONE, 0, 0};
    }
    if (m < 42 && m % 2 == 0) { const int e = m / 2; return e < 18 ? (e % 3 == 2 ? w2op(9 + e / 3) : Op{H2, 6 + e / 3, e % 3}) : w2op(e - 3); }
    if (m == 31) return Op{SAMPLE, 1, 0};
    if (m == PB + 2) return Op{CHECK, 4, 0};
    if (m > PB + 2 && m < PB + 14 && m % 2 == 1) return Op{H2, 12 + (m - PB - 3) / 4, ((m - PB - 3) / 2) & 1};
    if (m == PB) return Op{PUB, 2, 0};
    if (m == PB + 1) return Op{TOUCH, 0, 2};
    if (m == SM) return Op{SAMPLE, 2, 0};
    if (m >= 70 && m < 76 && m % 2 == 0) return w2op(18 + (m - 70) / 2);
    if (m == SM + 20) return Op{CHECK, 2, 0};
    if (m > SM + 20 && m <= SM + 32) return Op{H2, 15 + (m - SM - 21) / 2, (m - SM - 21) & 1};
    if (m == 122 || m == 124) return Op{XRES, (m - 122) / 2, 0};
    return Op{NONE, 0, 0};
  }
  static constexpr int wait1(int n0, int kind, int amax) {
    int cnt = 0, need = -1;
    for (int i = -NPRO; i < n0; ++i) {
      const Op o = i < 0 ? Op{XF, i + NPRO, 0} : at1(i);
      if (o.kind == PUB) need = -1;
      if (o.kind == NONE || o.kind > SAMPLE) continue;
      ++cnt;
      if (o.kind == kind && o.a <= amax) need = cnt;
    }
    return need < 0 || cnt - need > 62 ? 63 : cnt - need;
  }
  static constexpr int wait2(int m0, int kind, int amax) {
    int cnt = 0, need = -1;
    for (int i = -NPRE2; i < m0; ++i) {
      const Op o = i < 0 ? pre2(i + NPRE2) : at2(i);
      if (o.kind == PUB) need = -1;
      if (o.kind == NONE || o.kind > SAMPLE) continue;
      ++cnt;
      if (o.kind == kind && o.a <= amax) need = cnt;
    }
    return need < 0 || cnt - need > 62 ? 63 : cnt - need;
  }
};
using PF = Plan<false>;
static_assert(PF::eat(0, 175) < 224 - 16 && PF::eat(1, 175) < 352 - 16 && PF::eat(2, 175) < PF::NIT + PB - 8, "a unit is published after its last store");
static_assert(PF::eat(0, 44) >= 96 + 26 && PF::eat(0, 88) >= 96 + 38 && PF::eat(0, 132) >= 96 + 50, "unit 0: accumulators complete before they are read");
static_assert(PF::eat(0, 39) < 144 && PF::eat(0, 83) < 162 && PF::eat(0, 127) < 180 && PF::eat(0, 171) < 198, "unit 0: accumulators read before they restart");
static_assert(PF::eat(1, 44) >= 216 + 38 && PF::eat(1, 88) >= 216 + 56 && PF::eat(1, 132) >= 216 + 74 && PF::eat(1, 171) < 288 + 54, "unit 1");
static_assert(PF::eat(0, 153) > 144 + 30 && PF::eat(0, 153) < 213 && PF::eat(1, 65) > 216 + 40 && PF::eat(1, 65) < 285, "early weight waits follow the last request");
static_assert(PF::eat(2, 44) >= 360 + 38 && PF::eat(2, 88) >= 360 + 56 && PF::eat(2, 132) >= 360 + 74, "unit 2");

struct Args { const char* x; const char* wqkv; const char* wo; const char* wg; const char* wu; const char* wd; char* ws; char* out; };

DI void ln_load(u32x4& v, unsigned& v9, const char* x, int row, int lane) {
  const rsrc_t sx = mk_rsrc(x, S * D * 2);
  const unsigned xo = (unsigned)(row * 1152);
  load16(v, xo + lane * 16, sx);
  asm volatile("buffer_load_ushort %0, %1, %2, 0 offen offset:1024" : "=v"(v9) : "v"(xo + lane * 2), "s"(sx));
}
DI void ln_row(u32x4 v, unsigned v9, unsigned smem, int wave, int lane) {
  float f[9];
  sfor<4>([&](auto E) { f[2 * E] = bflo(v[(int)E]); f[2 * E + 1] = bfhi(v[(int)E]); });
  f[8] = bflo(v9);
  const float mean = wave_sum(((f[0] + f[1]) + (f[2] + f[3])) + ((f[4] + f[5]) + (f[6] + f[7])) + f[8]) * (1.0f / D);
  float q = 0.f;
  sfor<9>([&](auto E) { const float d = f[E] - mean; q += d * d; });
  const float var = wave_sum(q) * (1.0f / D);
  const float mb = rbf(mean), rs = LOG2E * __builtin_amdgcn_rcpf(rbf(__builtin_amdgcn_sqrtf(rbf(rbf(var) + 1e-5f))));
  sfor<4>([&](auto E) { v[(int)E] = cvtpk((f[2 * E] - mb) * rs, (f[2 * E + 1] - mb) * rs); });
  *(LDS u32x4*)(smem + lane * 128 + wave * 16) = v;
  *(LDS unsigned short*)(smem + 8192 + (lane >> 3) * 128 + wave * 16 + (lane & 7) * 2) = (unsigned short)cvtpk((f[8] - mb) * rs, 0.f);
}
DI void ln_publish(char* ws, unsigned smem, unsigned tag, int gid, int lane) {
  u32x4 t[9];
  const unsigned fo = spin(rfl(FL_X + FS * gid));
  sfor<9>([&](auto I) { t[I] = *(LDS u32x4*)(smem + 1024 * I + lane * 16); });
  const unsigned o = (unsigned)((gid >> 1) * 18432 + (gid & 1) * 128 + (lane >> 3) * 256 + (lane & 7) * 16);
  const rsrc_t sxn = mk_rsrc(ws + WS_XN, XN_B);
  sfor<9>([&](auto I) { store16_sc1<0>(t[I], sxn, o, 2048 * I); });
  publish(tag, fo, mk_rsrc(ws + WS_FL, FL_B));
}

template <bool QKV>
DI void body(const Args& A, unsigned smem, unsigned tag, int rt, int col0, int wave, int lane, unsigned tch0) {
  using P = Plan<QKV>;
  constexpr int NCG = P::NCG, KS = P::KS, IPS = P::IPS, NIT = P::NIT, SPU = P::SPU, SB = P::SB, NT = P::NT, NIT2 = P::NIT2;
  constexpr int PL1 = QKV ? 3 : 4;
  constexpr unsigned RS = QKV ? 3u * D * 2u : F * 2u, WB = QKV ? D * 3 * D * 2 : D * F * 2;
  char* ws = A.ws;
  const int bid = blockIdx.x, isU = wave >> 2, w3 = wave & 3;
  const rsrc_t sF = mk_rsrc(ws + WS_FL, FL_B), sxn = mk_rsrc(ws + WS_XN, XN_B);
  const int cgl = ((lane & 7) >> 1) ^ ((lane >> 4) & 3);
  const int pc = 2 * cgl + (lane & 1);
  const unsigned wv = (unsigned)(((QKV ? 32 * isU : 0) + 16 * (w3 & 1) + 4 * (w3 >> 1) + 8 * (lane >> 5) + ((lane >> 3) & 3)) * RS + col0 * 2 + pc * 16);
  const unsigned wu = (unsigned)((8 * ((lane >> 3) & 3) + 4 * (lane >> 5) + ((lane >> 1) & 3)) * RS + col0 * 2 + 128);
  const char* wA = QKV ? A.wqkv : A.wg;
  const rsrc_t sw = mk_rsrc(isU && !QKV ? A.wu : wA, WB);
  const unsigned m0b = smem + isU * (QKV ? 5120 : SB / 2) + w3 * 1024;
  const int us = min(wave, 5);
  const unsigned m0u = smem + 4096 + us * 5120, wus = wu + us * 32 * RS;
  auto w1 = [&](auto ST, auto B, unsigned m0, unsigned vo, rsrc_t srd) {
    constexpr int a = CV(ST), b = CV(B) % (QKV ? 3 : 9), su = a / SPU;
    constexpr unsigned sucol = QKV ? (su == 0 ? 1152u : su == 1 ? 2304u : 0u) : su * 128u;
    dma16<P::slot(a) * SB + b * (QKV ? 10240 : 4096), 32 * (KS * (a % SPU) + (QKV ? 2 : 1) * b) * RS + sucol>(m0, vo, srd);
  };
  if (isU) {
    const rsrc_t sa = mk_rsrc(wA, WB);
    if constexpr (!QKV) sfor<2>([&](auto ST) { sfor<9>([&](auto B) { w1(ST, B, m0b - SB / 2, wv, sa); w1(ST, B, m0b, wv, sw); }); });
    else sfor<3>([&](auto ST) {
      sfor<3>([&](auto B) { w1(ST, B, m0b - 5120, wv - 32 * RS, sw); w1(ST, B, m0b, wv, sw); });
      w1(ST, ic<3>{}, smem + 4096 + w3 * 5120, wu + w3 * 32 * RS, sw); w1(ST, ic<3>{}, m0u, wus, sw);
    });
  }
  const int t16 = rt * 16 + 2 * wave;
  const unsigned xs = spin(t16 * 18432), l16 = lane * 16;
  u32x4 xf[18][2];
  auto xload = [&](auto AA) { constexpr int a = CV(AA); load16_sc1<(a / 2) * 1024 + (a & 1) * 18432>(xf[a / 2][a & 1], l16, xs, sxn); };
  if (wave == 0) {
    const unsigned fo = FL_X + FS * (rt * 32 + (lane & 31));
    __builtin_amdgcn_s_sleep(20);
    unsigned f; load4_sc1(f, fo, sF); flags_wait<0, 1>(f, fo, tag, ~0ull, sF);
  }
  asm volatile("s_barrier" ::: "memory");
  STAMP(4);
  sfor<P::NPRO>(xload);
  GAP();
  const int b2 = bid & 7, tile = bid >> 3, nc2 = QKV ? 768 : (tile - 3 * rt) * 256;
  const rsrc_t sh = mk_rsrc(ws + WS_H, HBUF_B), sS = mk_rsrc(ws + WS_SLAB, SLAB_B);
  const rsrc_t sWd = mk_rsrc(A.wd + 576 * 1152 * b2, 576 * 1152), sWo = mk_rsrc(A.wo + 72 * 1152 * b2, 64 * 1152), sWp = mk_rsrc(A.wo + (72 * b2 + 64) * 1152, 8 * 1152);
  const unsigned m0c = spin(smem + wave * 1024), m0d = spin(smem + 8192 + w3 * 1024 + isU * P::SB2);
  const unsigned hb = spin((t16 * KSTOT + 18 * b2) * 1024), ho = spin((t16 * KSTOT + 144 + 3 * b2) * 1024), hst = spin((t16 * KSTOT + (col0 >> 5)) * 1024);
  const unsigned oso = spin(t16 * 16 * 1152 + nc2), sl = spin(((tile * 8 + b2) * 8 + wave) * SLAB_W);
  const unsigned xt = spin(((wave - b2) & 7) >> 1), guest = spin((wave - b2) & 1);
  const unsigned fme = spin(FL_H + ((rt * 8 + wave) * 96 + (QKV ? 12 * b2 : 4 * (col0 / 192))) * FS);
  const unsigned tab = smem + LDS_X + wave * 2048 + l16;
  const bool wdo = tile * 8 + wave >= 162;
  unsigned ab[NCG];
  u32x4 t0;
  {
    const unsigned l = lane, b0 = smem + (l >> 2) * 128 + ((l >> 3) & 3) * 32 + 8 * (l & 3);
    t0[0] = (8 * w3 + (l >> 3)) * 1152 + nc2 + isU * 128 + pc * 16;
    t0[1] = FL_H + ((rt * 8 + wave) * 96 + 12 * b2 + (l < 12 ? l : 0)) * FS;
    t0[2] = b0;
    t0[3] = (l & 15) * 1152 + (l >> 4) * 8;
    *(LDS u32x4*)tab = t0;
    if constexpr (!QKV) *(LDS u32x2*)(tab + 1024) = u32x2{
      pick<unsigned>(l < 9, (rt * 36 + w3 * 9 + l) * RS + col0 * 2, 0x80000000u),
      pick<unsigned>(l < 11, (wdo ? tile * 8 + wave - 162 : tile * 8 + wave) * 4096 + l * 128, 0x80000000u)};
    if constexpr (QKV) sfor<5>([&](auto C) { ab[C] = (CV(C) < 4 ? b0 ^ (CV(C) * 32) : smem + 4096 + 8 * l) + SB; pin(ab[C]); });
    else sfor<8>([&](auto J) { ab[J] = (b0 ^ ((CV(J) >> 1) * 32)) + (CV(J) & 1) * (SB / 2); pin(ab[J]); });
  }
  u32x4 hf[21][2], hp[2], gp;
  unsigned fl, cur[2], wv3, tch[2], tk[3][2], cb[NT];
  u32x2 xres[4], pk[5][2], hl[10];
  unsigned l8 = lane * 8;
  if constexpr (QKV) pin(l8);
  const unsigned stg = smem + wave * 5120 + l8;
  auto ldtab = [&](int i) { return *(volatile LDS unsigned*)(tab + 1024 + 4 * i); };
  auto vmem = [&](auto K, auto AA, auto BB, auto NN) {
    constexpr int kind = CV(K), a = CV(AA), b = CV(BB);
    if constexpr (kind == W1) w1(AA, BB, QKV && b == 3 ? m0u : m0b, QKV && b == 3 ? wus : wv, sw);
    else if constexpr (kind == XF) xload(AA);
    else if constexpr (kind == TOUCH) {
      if constexpr (b == 2) load4_sc1(tch[0], (l16 & 0xf0) * 8 + (l16 & 0x100) * (KSTOT * 4) + (l16 >> 9) * 0x80000000u + hst + 4096, sh);
      else if constexpr (b == 0) load4s<288 * (a % 2) * RS + (a / 2) * 128>(tch[0], tch[0], sw);
      else load4s<a * 11 * 128>(tch[1], tch[1], mk_rsrc(wdo ? A.wo + 72 * 1152 * b2 : A.wd + 576 * 1152 * b2, wdo ? 72 * 1152 - 4096 : 576 * 1152 - 4096));
    } else if constexpr (kind == XRES) load8_if(xres[a], t0[3], oso + (a & 1) * 16 * 1152 + (a < 2 ? 32 * b2 : 256 + 32 * xt), mk_rsrc(A.x, S * D * 2), a < 2 ? 0u : guest);
    else if constexpr (kind == W2) {
      constexpr bool isO = a >= P::O2 && a < P::O2 + 3;
      constexpr int e = P::hstep(a), row = isO ? (a - P::O2 == 1 ? 32 : 0) : 192 * ((e % 6) / 2) + 64 * (e / 6) + 32 * (e & 1);
      const rsrc_t r = !isO ? sWd : a - P::O2 < 2 ? sWo : sWp;
      if constexpr (b == 0) dma16<(a % P::NSL2) * P::SB2, row * 1152>(m0c, t0[0], r);
      else if constexpr (b == 1 && !(isO && a == 2)) dma16<(a % P::NSL2) * P::SB2, row * 1152>(m0d, wv3, r);
      else dma16<(a % P::NSL2) * P::SB2 + 8192, row * 1152 + 256>(smem + w3 * 1024, t0[0] - isU * 128, r);
    } else if constexpr (kind == H2) {
      constexpr int e = P::hstep(a);
      if constexpr (a >= P::O2 && a < P::O2 + 3) load16_sc1<(b * KSTOT + a - P::O2) * 1024>(hf[a][b], l16, ho, sh);
      else load16_sc1<(b * KSTOT + 6 * ((e % 6) / 2) + 2 * (e / 6) + (e & 1)) * 1024>(hf[a][b], l16, hb, sh);
    } else if constexpr (kind == HALO) load8x5_sc1(hl + 5 * a, mk_rsrc(ws + WS_HALO + (bid - 200) * 5120 + a * 2560, rt > 0 ? 2560 : 0), l8, wave);
    else if constexpr (kind == SAMPLE) load4_sc1(fl, t0[1], sF);
    else if constexpr (kind == CHECK) flags_wait<CV(NN), QKV ? 4 : 2>(fl, t0[1], tag, a == 0 ? 0x111ull : a == 1 ? 0x222ull : a == 2 ? 0x444ull : a == 4 ? 0x8ull : wave == 0 && rt > 0 ? 0x80ull : 0ull, sF);
    else if constexpr (kind == PUB) publish(tag, fme + a * FS, sF);
    else if constexpr (kind == PUBH) publish_if(tag, fme + (96 + 7) * FS, sF, wave ^ 7);
  };
  f32x4 acc[NCG][2], acc2[NT][2];
  bf16x4 al[DA], ah[DA];
  auto aread = [&](auto N, auto T) {
    constexpr int n = CV(N);
    bf16x4 r;
    if constexpr (n >= NIT) r = trread(cb[P::ti2(n - NIT)], ((P::st2(n - NIT) % P::NSL2) % P::WIN) * P::SB2 + CV(T) * 2048);
    else if constexpr (QKV) r = trread(ab[P::jof(n)], (P::sof(n) % 12) * 5120 + CV(T) * (P::jof(n) < 4 ? 2048 : 512));
    else r = trread(n < 144 ? ab[P::jof(n)] : cur[P::jof(n) & 1], (P::sof(n) % KS) * 4096 + CV(T) * 2048);
    if constexpr (CV(T) == 0) al[n % DA] = r; else ah[n % DA] = r;
  };
  auto rebase = [&](auto M) {
    constexpr int m = CV(M);
    if constexpr (m >= NIT) {
      constexpr int p = m - NIT, i = P::ti2(p);
      if constexpr (P::pwin2(p) >= 0) cb[i] = v_addk<(unsigned)((P::win2(p) - P::pwin2(p)) * P::WIN * P::SB2)>(cb[i]);
      else { const unsigned ph = i < 8 ? (i + b2 + 1) & 7 : i; cb[i] = v_xors(t0[2], (ph & 3) * 32 + (ph >> 2) * 4096); }
    } else if constexpr (QKV) ab[P::jof(m)] = v_addk<(m / IPS) % 3 == 0 ? (unsigned)SB : (unsigned)-SB>(ab[P::jof(m)]);
    else if constexpr (m < 144) ab[P::jof(m)] = v_addk<SB>(ab[P::jof(m)]);
    else cur[P::jof(m) & 1] = v_addk<(unsigned)((((m / IPS) & 1) - 1) * SB + (P::jof(m) & 1) * (SB / 2))>(ab[P::jof(m) & ~1]);
  };
  float ee, eg, eh[2];
  const float k2 = __builtin_bit_cast(float, spin(__builtin_bit_cast(unsigned, LOG2E * LOG2E)));
  auto epi = [&](auto SU, auto E) {
    constexpr int e = CV(E), w = e / 11, ph = e % 11, p = w >> 2, th = (w >> 1) & 1, r = 2 * (w & 1) + (ph >= 4 && ph != 5);
    if constexpr (ph == 0 || ph == 4) { if constexpr (ph == 4) eh[0] = ee; ee = v_expn(acc[2 * p][th][r]); }
    if constexpr (ph == 1 || ph == 6) eg = v_mul(acc[2 * p][th][r], acc[2 * p + 1][th][r]);
    if constexpr (ph == 2 || ph == 7) ee = v_fmak(ee, k2);
    if constexpr (ph == 3 || ph == 8) ee = v_rcp(ee);
    if constexpr (ph == 5) eh[0] = v_mul(eg, eh[0]);
    if constexpr (ph == 9) eh[1] = v_mul(eg, ee);
    if constexpr (ph == 10) {
      hp[th][2 * (p & 1) + (w & 1)] = v_cvtpk(eh[0], eh[1]);
      if constexpr ((p & 1) && (w & 1)) store16_sc1<(p >> 1) * 1024>(hp[th], sh, l16, hst + (th * KSTOT + 2 * CV(SU)) * 1024);
    }
  };
  auto filler = [&](auto N, auto T) {
    constexpr int n = CV(N), t = CV(T);
    if constexpr (t == 1 && P::rebase(n + 4)) rebase(ic<n + 4>{});
    else if constexpr (!QKV) sfor<3>([&](auto SU) { constexpr int e = P::eidx(CV(SU), n, t); if constexpr (e >= 0) epi(SU, ic<e>{}); });
    GAP();
  };
  auto epiq = [&](auto SU, auto E, auto T) {
    constexpr int su = CV(SU), e = CV(E) - 4, t = CV(T), cg = e / 3, th = e % 3;
    if constexpr (e >= 0 && cg < 5 && th < 2) {
      pk[cg][th][t] = v_cvtpk(acc[cg][th][2 * t], acc[cg][th][2 * t + 1]);
      if constexpr (su == 0 && t == 1) *(LDS u32x2*)(stg + LDS_K + th * 2560 + 512 * cg) = pk[cg][th];
      if constexpr (su < 2 && t == 1 && th == 1) store8_sc1<512 * cg, 7>(pk[cg][1], mk_rsrc(ws + WS_HALO + (bid - 192) * 5120 + su * 2560, 2560), l8, wave);
      GAP();
    }
  };
  GAP(); wait_barrier<P::NPRO - 2>(); GAP();
  keep(tch0);
  STAMP(5);
  __builtin_amdgcn_s_setprio(PL1 - 1);
  sfor<DA - 1>([&](auto N) { aread(N, ic<0>{}); aread(N, ic<1>{}); });
  GAP();
  sfor<NIT>([&](auto N) {
    constexpr int n = CV(N), st = n / IPS, it = n % IPS, j = P::jof(n), sg = P::sof(n), su = st / SPU, m = n + DA - 1;
    constexpr bool more = m < NIT || !QKV;
    if constexpr (QKV && n == NIT - (DA - 1)) { lds_barrier(); GAP(); }
    constexpr bool early = !QKV && (st == 2 || st == 3);
    if constexpr (early && n == P::eat(st - 2, st == 2 ? 153 : 65) - 1) { wait_vm<P::wait1(n, W1, st + 1)>(); GAP(); }
    if constexpr (it == P::BAR && st < P::NST - 1) { wait_barrier<early ? 63 : P::wait1(n, W1, st + 1)>(); GAP(); }
    if constexpr (!QKV && n == NIT - (DA - 1)) { wait_barrier<cmin(P::wait1(n, W2, 8), P::wait1(n, H2, 0))>(); GAP(); }
    { constexpr int pp = (it + DA - 1) % IPS; if constexpr (pp % (IPS / PL1) == 0) { __builtin_amdgcn_s_setprio(PL1 - 1 - pp / (IPS / PL1)); GAP(); } }
    if constexpr (su == 0 && j == 0 && n > 0) { wait_vm<P::wait1(n, XF, 2 * sg + 1)>(); GAP(); }
    const bf16x8 a = cat8(al[n % DA], ah[n % DA]);
    constexpr bool swp = QKV && su == 1;
    sfor<2>([&](auto T) {
      if constexpr (more) { aread(ic<m>{}, T); GAP(); }
      if constexpr (W2X && more && CV(T) == 0 && n % 2 == 0) { __builtin_amdgcn_s_waitcnt(0xC07F | (3 << 8)); GAP(); }
      const bf16x8 b = as_bf(xf[sg][T]);
      if constexpr (sg == 0) mfma_new(acc[j][T], swp ? b : a, swp ? a : b); else mfma_acc(acc[j][T], swp ? b : a, swp ? a : b);
      GAP();
      filler(N, T);
      if constexpr (QKV && n % 90 >= 75) epiq(ic<su>{}, ic<n % 90 - 75>{}, T);
      if constexpr (QKV && n % 90 < 5 && su > 0) epiq(ic<su - 1>{}, ic<n % 90 + 15>{}, T);
    });
    if constexpr (QKV) {
      if constexpr (st == 8 && it < 10) *(LDS u32x2*)(stg + LDS_V + (it % 2) * 2560 + 512 * (it / 2)) = pk[it / 2][it % 2];
      if constexpr (n == 220) t0 = *(volatile LDS u32x4*)tab;
    } else {
      if constexpr (st >= 1 && st <= 3 && it == 24) { tch[0] = ldtab(0); tch[1] = ldtab(1); }
      if constexpr (st >= 1 && st <= 3 && it == 41) { tk[st - 1][0] = tch[0]; tk[st - 1][1] = tch[1]; }
      if constexpr (n == 200 || n == 230 || n == 354) { constexpr int q = n == 200 ? 0 : n == 230 ? 1 : 2; pin(tk[q][0]); pin(tk[q][1]); }
      if constexpr (n == 320) t0 = *(volatile LDS u32x4*)tab;
    }
    constexpr Op o = P::at1(n);
    if constexpr (o.kind != NONE) vmem(ic<o.kind>{}, ic<o.a>{}, ic<o.b>{}, ic<o.kind == CHECK ? P::wait1(n, SAMPLE, 9) : 0>{});
    GAP();
    if constexpr (n % (NIT / 9) == NIT / 9 - 1) STAMP(6 + n / (NIT / 9));
  });
  if constexpr (QKV) {
    mfma_drain();
    sfor<4>([&](auto J) { epiq(ic<2>{}, ic<16 + CV(J) / 2>{}, ic<CV(J) % 2>{}); });
    vmem(ic<SAMPLE>{}, ic<0>{}, ic<0>{}, ic<0>{});
    const int i16 = lane & 15, q4 = lane >> 4, odd = lane & 1;
    const bool cur0 = 4 * q4 + odd <= i16, cur1 = 4 * q4 + 2 + odd <= i16;
    const unsigned s0 = odd ? 0x01000c0cu : 0x0c0c0100u, s1 = odd ? 0x03020c0cu : 0x0c0c0302u;
    const unsigned sel[4] = {cur0 ? 0x0c0c0c0cu : s0, cur1 ? 0x0c0c0c0cu : s1, cur0 ? s0 : 0x0c0c0c0cu, cur1 ? s1 : 0x0c0c0c0cu};
    constexpr float SC = 0.23570226039551584f / LOG2E;
    auto ld2 = [&](unsigned a0, unsigned a1) { const u32x2 u = *(volatile LDS u32x2*)a0, v = *(volatile LDS u32x2*)a1; return as_bf(u32x4{u[0], u[1], v[0], v[1]}); };
    const float cp = t16 == 0 ? -1.0e30f : 0.f;
    unsigned ge[2], lt[2];
    sfor<2>([&](auto I) { ge[I] = (unsigned)((CV(I) - q4) >> 31); lt[I] = ~ge[I]; pin(lt[I]); pin(ge[I]); });
    auto mge = [&](unsigned v, auto C) { constexpr int c = CV(C); if constexpr (c <= 0) return v; else return v & ge[(c - 1) / 4]; };
    auto mlt = [&](unsigned v, auto C) { constexpr int c = CV(C); if constexpr (c <= 0) return 0u; else return v & lt[(c - 1) / 4]; };
    float ca[4], cc[4];
    sfor<4>([&](auto I) { const bool par = odd == (CV(I) & 1); ca[I] = par && cur0 == (CV(I) < 2) ? SC : 0.f; cc[I] = par && cur1 == (CV(I) < 2) ? SC : 0.f; pin(ca[I]); pin(cc[I]); });
    wv3 = t0[0] + 256 + isU * (32 * 1152 - 128); pin(wv3);
    wait_vm<1>();
    sfor<10>([&](auto I) { pin(hl[I]); });
    if (wave == 0) sfor<10>([&](auto I) { *(volatile LDS u32x2*)(stg + (CV(I) < 5 ? LDS_K : LDS_V) - 2560 + 512 * (CV(I) % 5)) = hl[I]; });
    f32x4 sp[8], sc[8];
    float l0[8], l1[8], mx[8];
    bf16x8 pb[8], kb[4][3], vb[2][5];
    sfor<12>([&](auto I) { constexpr int h = CV(I) / 3, g = CV(I) % 3; kb[h][g] = ld2(stg + LDS_K + (g - 1) * 2560 + 512 * h, stg + LDS_K + (g - 1) * 2560 + 512 * h + 512); });
    GAP();
    auto pf = [&](auto I) { constexpr Op o = P::w2op(CV(I)); GAP(); vmem(ic<o.kind>{}, ic<o.a>{}, ic<o.b>{}, ic<0>{}); GAP(); };
    auto ph = [&](auto I) { GAP(); vmem(ic<H2>{}, ic<3 + CV(I)>{}, ic<0>{}, ic<0>{}); vmem(ic<H2>{}, ic<3 + CV(I)>{}, ic<1>{}, ic<0>{}); GAP(); };
    pf(ic<0>{});
    sfor<4>([&](auto HH) {
      constexpr int hd = CV(HH);
      sfor<2>([&](auto TH) {
        const bf16x8 qb = as_bf(u32x4{mge(pk[hd][TH][0], ic<2 * hd>{}), mge(pk[hd][TH][1], ic<2 * hd - 2>{}), mlt(pk[hd + 1][TH][0], ic<2 * hd + 2>{}), mlt(pk[hd + 1][TH][1], ic<2 * hd>{})});
        sp[2 * hd + TH] = CV(TH) ? mfma(kb[hd][1], qb, f32x4{}) : mfma(kb[hd][0], qb, f32x4{cp, cp, cp, cp});
        sc[2 * hd + TH] = mfma(kb[hd][1 + TH], qb, f32x4{});
      });
      pf(ic<1 + hd>{});
    });
    sfor<10>([&](auto I) { constexpr int th = CV(I) / 5, j = CV(I) % 5; vb[th][j] = ld2(stg + LDS_V + (th - 1) * 2560 + 512 * j, stg + LDS_V + th * 2560 + 512 * j); });
    GAP();
    lds_barrier();
    vmem(ic<CHECK>{}, ic<0>{}, ic<0>{}, ic<5>{});
    sfor<8>([&](auto E) {
      l0[E] = fma4(sc[E][0], sc[E][1], sp[E][0], sp[E][1], ca);
      l1[E] = fma4(sc[E][2], sc[E][3], sp[E][2], sp[E][3], cc);
      mx[E] = vmax(l0[E], l1[E]); pin(mx[E]); pin(l0[E]); pin(l1[E]);
      if constexpr (CV(E) % 3 == 0) pf(ic<5 + CV(E) / 3>{});
    });
    ph(ic<0>{});
    xq8<true>(mx, 0.f);
    ph(ic<1>{});
    sfor<8>([&](auto E) { l0[E] = __builtin_amdgcn_exp2f(l0[E] - mx[E]); l1[E] = __builtin_amdgcn_exp2f(l1[E] - mx[E]); mx[E] = l0[E] + l1[E]; });
    ph(ic<2>{});
    xq8<false>(mx, 1.f / LOG2E);
    ph(ic<3>{});
    sfor<8>([&](auto E) {
      const unsigned pp = cvtpk(l0[E] * mx[E], l1[E] * mx[E]);
      pb[E] = as_bf(u32x4{__builtin_amdgcn_perm(pp, pp, sel[0]), __builtin_amdgcn_perm(pp, pp, sel[1]),
                          __builtin_amdgcn_perm(pp, pp, sel[2]), __builtin_amdgcn_perm(pp, pp, sel[3])});
    });
    ph(ic<4>{});
    sfor<8>([&](auto E) { sp[E] = mfma(vb[CV(E) & 1][CV(E) >> 1], pb[E], f32x4{}); sc[E] = mfma(vb[CV(E) & 1][(CV(E) >> 1) + 1], pb[E], f32x4{}); });
    ph(ic<5>{});
    sfor<NT>([&](auto I) { rebase(ic<NIT + CV(I)>{}); });
    wait_vm<12>();
    sfor<2>([&](auto TH) {
      unsigned ow[12];
      sfor<12>([&](auto E) {
        constexpr int j = CV(E) >> 1, e = CV(E) & 1;
        unsigned a = 0u, b = 0u;
        if constexpr (j < 4) a = mge(cvtpk(sp[2 * j + TH][2 * e], sp[2 * j + TH][2 * e + 1]), ic<2 * j - 2 * e>{});
        if constexpr (j > 0 && j < 5) b = mlt(cvtpk(sc[2 * j - 2 + TH][2 * e], sc[2 * j - 2 + TH][2 * e + 1]), ic<2 * j - 2 * e>{});
        ow[E] = a | b;
      });
      sfor<3>([&](auto M) { hf[M][TH] = u32x4{ow[4 * M], ow[4 * M + 1], ow[4 * M + 2], ow[4 * M + 3]}; pin(hf[M][TH]); store16_sc1<1024 * CV(M)>(hf[M][TH], sh, l16, ho + TH * KSTOT * 1024); });
    });
    STAMP(16);
    GAP(); lds_barrier(); GAP();
    sfor<DA - 1>([&](auto N) { aread(ic<NIT + CV(N)>{}, ic<0>{}); aread(ic<NIT + CV(N)>{}, ic<1>{}); });
    __builtin_amdgcn_s_setprio(3);
    GAP();
    STAMP(17);
  }
  sfor<NIT2>([&](auto M) {
    constexpr int m = CV(M), n = NIT + m, st = P::st2(m), i = P::ti2(m);
    if constexpr (P::bar2(m) >= 0) { wait_barrier<P::wait2(m, W2, P::bar2(m))>(); GAP(); }
    {
      constexpr int lo = [] { int k = m; while (k >= 0 && P::bar2(k) < 0) --k; return k < 0 ? -3 : k; }(), hi = [] { int k = m + 1; while (k < NIT2 && P::bar2(k) < 0) ++k; return k; }();
      constexpr int len = (hi - lo + 3) / 4;
      if constexpr ((m - lo) % len == 0) { __builtin_amdgcn_s_setprio(3 - (m - lo) / len); GAP(); }
    }
    if constexpr (P::first2(m) && st > 0) { wait_vm<P::wait2(m, H2, st)>(); GAP(); }
    const bf16x8 a = cat8(al[n % DA], ah[n % DA]);
    sfor<2>([&](auto T) {
      if constexpr (m + DA - 1 < NIT2) { aread(ic<n + DA - 1>{}, T); GAP(); }
      if constexpr (W2X && m + DA - 1 < NIT2 && CV(T) == 0 && n % 2 == 0) { __builtin_amdgcn_s_waitcnt(0xC07F | (3 << 8)); GAP(); }
      if constexpr (st == 0) mfma_new(acc2[i][T], a, as_bf(hf[st][T])); else mfma_acc(acc2[i][T], a, as_bf(hf[st][T]));
      GAP();
      filler(ic<n>{}, T);
      constexpr int c = m - P::M2 - 7;
      if constexpr (c >= 0 && c / 6 < NT - 1 && c % 6 < 4 && CV(T) == 0) {
        constexpr int tl = P::gt(c / 6), e = c % 6;
        gp[e] = v_cvtpk(acc2[tl][e >> 1][2 * (e & 1)], acc2[tl][e >> 1][2 * (e & 1) + 1]);
        if constexpr (e == 3) store16_sc1<0>(gp, sS, l16, sl + tl * 1024);
        GAP();
      }
    });
    if constexpr (!QKV && m == 118) pin(tch[0]);
    constexpr Op o = P::at2(m);
    if constexpr (o.kind != NONE) { vmem(ic<o.kind>{}, ic<o.a>{}, ic<o.b>{}, ic<o.kind == CHECK ? P::wait2(m, SAMPLE, 9) : 0>{}); GAP(); }
    if constexpr (m % (3 * NT) == 3 * NT - 1) STAMP(18 + m / (3 * NT));
  });
  unsigned fo = FL_S + (tile * 8 + wave) * 8 * FS + ((16 * (b2 + 7) - l16) & 0x70) * (FS / 16), f;
  if (!QKV && wave == 0 && bid == 0) {
    unsigned g[4];
    sfor<4>([&](auto I) { load4_sc1(g[I], FL_X + 64 * FS * I + 8 * l16, sF); });
    sfor<4>([&](auto I) { flags_wait<0, 2>(g[I], FL_X + 64 * FS * I + 8 * l16, tag, ~0ull, sF); });
    flag_only(tag, FL_EPOCH, sF);
  }
  load4_sc1(f, fo, sF);
  publish(tag, (unsigned)(FL_S + ((tile * 8 + wave) * 8 + b2) * FS), sF);
  STAMP(26);
  mfma_drain();
  pin(acc2[7][0]); pin(acc2[7][1]);
  sfor<QKV ? 4 : 2>([&](auto I) { pin(xres[I]); });
  u32x4 pv[7], pw[8];
  const rsrc_t sQ = mk_rsrc(ws + WS_SLAB + 8192 + xt * 1024, QKV && !guest ? SLAB_B - 12288 + 1024 : 0u);
  if constexpr (QKV) load16_sc1<0>(pw[7], l16, sl, sQ);
  lane = lane_now();
  const unsigned one = (lane >> 4) == ((lane >> 2) & 3) ? (lane & 1 ? 0x3f800000u : 0x3f80u) : 0u, e0 = lane & 2 ? 0u : one, e1 = lane & 2 ? one : 0u;
  const bf16x8 elo = as_bf(u32x4{e0, e1, 0u, 0u}), ehi = as_bf(u32x4{0u, 0u, e0, e1});
  const bf16x8 xb = as_bf(u32x4{xres[0][0], xres[0][1], xres[1][0], xres[1][1]});
  f32x4 o0 = mfma(elo, xb, acc2[7][0]), o1 = mfma(ehi, xb, acc2[7][1]);
  const unsigned xr = smem + LDS_X + wave * 2048;
  unsigned tw = xr + (lane & 15) * 48 + (lane >> 4) * 8, trd = xr + (lane >> 1) * 48 + (lane & 1) * 16, tst = (lane >> 1) * 1152 + (lane & 1) * 16;
  pin(tw); pin(trd); pin(tst); pin(o0); pin(o1);
  gather7<QKV>(pv, pw, f, fo, l16, l16 + 4096, tag, b2 + 7, spin((tile * 64 + wave) * SLAB_W), sS, sQ, sF);
  STAMP(28);
  const rsrc_t sO = mk_rsrc(A.out, S * D * 2);
  auto finish = [&](auto N, const u32x4* p, unsigned so) {
    sfor<CV(N)>([&](auto J) { o0 = mfma(elo, as_bf(p[J]), o0); o1 = mfma(ehi, as_bf(p[J]), o1); });
    *(LDS u32x2*)tw = u32x2{cvtpk(o0[0], o0[1]), cvtpk(o0[2], o0[3])}; *(LDS u32x2*)(tw + 768) = u32x2{cvtpk(o1[0], o1[1]), cvtpk(o1[2], o1[3])};
    asm volatile("" ::: "memory");
    store16(*(LDS u32x4*)trd, sO, tst, so);
  };
  if constexpr (QKV) pin(pw[7]);
  finish(ic<7>{}, pv, oso + 32 * b2);
  if (QKV && !guest) {
    const bf16x8 x8 = as_bf(u32x4{xres[2][0], xres[2][1], xres[3][0], xres[3][1]});
    o0 = mfma(elo, x8, f32x4{}); o1 = mfma(ehi, x8, f32x4{});
    finish(ic<8>{}, pw, oso + 256 + 32 * xt);
  }
  STAMP(29);
  if constexpr (QKV) asm volatile(".Lpsk_end_a:"); else asm volatile(".Lpsk_end_b:");
}

__global__ __launch_bounds__(512, 2) void fused(Args A) {
  __shared__ __attribute__((aligned(256))) char smem_[LDS_B];
  const unsigned smem = (unsigned)(size_t)(LDS char*)smem_;
  const int wave = rfl(threadIdx.x >> 6), lane = threadIdx.x & 63, bid = blockIdx.x, xcd = bid & 7, r = bid >> 3;
  char* ws = A.ws;
  STAMP(0);
  u32x4 v; unsigned v9, ep, tc;
  ln_load(v, v9, A.x, bid * 8 + wave, lane);
  load4_sc1(ep, FL_EPOCH, mk_rsrc(ws + WS_FL, FL_B));
  asm volatile("s_barrier" ::: "memory");
  const bool qkv = r >= 24;
  const int rt = qkv ? r - 24 : r / 3, col0 = qkv ? 72 * xcd : (xcd * 3 + r % 3) * 192;
  {
    u64 pc; unsigned na, nb;
    asm volatile("s_getpc_b64 %0\n.Lpsk_begin:\n\ts_mov_b32 %1, .Lpsk_end_a-.Lpsk_begin\n\ts_mov_b32 %2, .Lpsk_end_b-.Lpsk_begin" : "=s"(pc), "=s"(na), "=s"(nb));
    const int w3 = wave & 3;
    const unsigned id = (w3 - 1) * 64 + lane;
    unsigned vo;
    if (qkv) { const unsigned k = (wave - 1) * 64 + lane; vo = pick<unsigned>(k < 432, (rt * 216 + (k >> 1)) * 1152 + (k & 1) * 128, 0x80000000u); }
    else vo = pick<unsigned>(id < 72, (rt * 72 + id) * (F * 2), pick<unsigned>(id < 108, (rt * 36 + id - 72) * (F * 2) + 128, 0x80000000u));
    vo += col0 * 2;
    const bool cw = wave == 4 && r < 16;
    if (cw) vo = (r * 64 + lane) * 128;
    const unsigned nby = cw ? (na > nb ? na : nb) : qkv ? (wave ? D * 3 * D * 2 : 0) : w3 ? D * F * 2 : 0;
    load4(tc, vo, mk_rsrc(cw ? (const char*)pc : qkv ? A.wqkv : wave < 4 ? A.wg : A.wu, nby));
  }
  wait_vm<1>();
  pin(v); pin(v9); pin(ep);
  STAMP(1);
  const unsigned tag = rfl(ep) * 1664525u + 1013904223u;
  ln_row(v, v9, smem, wave, lane);
  lds_barrier();
  STAMP(2);
  if (wave == 0) { ln_publish(ws, smem, tag, bid, lane); STAMP(3); }
  asm volatile("s_barrier" ::: "memory");
  if (!qkv) body<false>(A, smem, tag, rt, col0, wave, lane, tc);
  else body<true>(A, smem, tag, rt, col0, wave, lane, tc);
}

}
extern "C" void kernel_launch(void* const* d_in, const int* in_sizes, int n_in, void* d_out, int out_size, void* d_ws, size_t ws_size,
                              hipStream_t stream) {
  psk::Args A;
  A.x = (const char*)d_in[0]; A.wqkv = (const char*)d_in[1]; A.wo = (const char*)d_in[2]; A.wg = (const char*)d_in[4];
  A.wu = (const char*)d_in[5]; A.wd = (const char*)d_in[6]; A.ws = (char*)d_ws; A.out = (char*)d_out;
  psk::fused<<<256, 512, 0, stream>>>(A);
}
```

```cpp
#include <hip/hip_runtime.h>
#include <utility>
#pragma clang diagnostic ignored "-Wint-to-pointer-cast"

typedef float    f32x4  __attribute__((ext_vector_type(4)));
typedef float    f32x2  __attribute__((ext_vector_type(2)));
typedef __bf16   bf16x8 __attribute__((ext_vector_type(8)));
typedef __bf16   bf16x4 __attribute__((ext_vector_type(4)));
typedef __bf16   bf16x2 __attribute__((ext_vector_type(2)));
typedef unsigned u32x4  __attribute__((ext_vector_type(4)));
typedef unsigned u32x2  __attribute__((ext_vector_type(2)));
typedef unsigned long long u64;
typedef __amdgpu_buffer_rsrc_t rsrc_t;
#define LDS __attribute__((address_space(3)))
#define DI __device__ __forceinline__
#define GAP() __builtin_amdgcn_sched_barrier(0)
#define CV(X) (decltype(X)::value)

#define LF 8
#define LQ 7
#define PB 49
#define SM 61
#define QS 125
#define HT 1
#define NE 3
#define SE 16
namespace psk {

constexpr int S = 2048, D = 576, F = 4608;
constexpr int KSTOT = 168;
constexpr unsigned HBUF_B = 128u * KSTOT * 1024u;
constexpr unsigned SLAB_W = 12288, SLAB_B = 32u * 8u * 8u * SLAB_W;
constexpr unsigned XN_B = S * D * 2u, HALO_B = 64u * 5120u;
constexpr unsigned FS = 128;
constexpr unsigned FL_EPOCH = 0, FL_X = FS, FL_H = FL_X + 256 * FS, FL_S = FL_H + 72 * 96 * FS, FL_B = FL_S + 256 * 16 * FS;
constexpr size_t WS_H = 0, WS_SLAB = WS_H + HBUF_B, WS_XN = WS_SLAB + SLAB_B, WS_HALO = WS_XN + XN_B, WS_FL = WS_HALO + HALO_B, WS_DBG = WS_FL + FL_B;
constexpr unsigned SPIN_CAP = 1u << 18;

template <int N> using ic = std::integral_constant<int, N>;
template <class Fn, int... Is> DI void sfor_impl(Fn&& f, std::integer_sequence<int, Is...>) { (f(ic<Is>{}), ...); }
template <int N, class Fn> DI void sfor(Fn&& f) { sfor_impl(f, std::make_integer_sequence<int, N>{}); }

DI unsigned rfl(unsigned x) { return __builtin_amdgcn_readfirstlane(x); }
DI rsrc_t mk_rsrc(const void* p, unsigned bytes) { return __builtin_amdgcn_make_buffer_rsrc((void*)p, (short)0, (int)bytes, 0x00020000); }
DI unsigned cvtpk(float lo, float hi) { f32x2 v = {lo, hi}; return __builtin_bit_cast(unsigned, __builtin_convertvector(v, bf16x2)); }
DI float rbf(float x) { return __builtin_bit_cast(float, cvtpk(x, x) << 16); }
DI float bflo(unsigned v) { return __builtin_bit_cast(float, v << 16); }
DI float bfhi(unsigned v) { return __builtin_bit_cast(float, v & 0xffff0000u); }
DI bf16x4 trread(unsigned addr, int off) { return __builtin_amdgcn_ds_read_tr16_b64_v4bf16((LDS bf16x4*)(addr + off)); }
DI bf16x8 cat8(bf16x4 a, bf16x4 b) { return __builtin_shufflevector(a, b, 0, 1, 2, 3, 4, 5, 6, 7); }
DI bf16x8 as_bf(u32x4 v) { return __builtin_bit_cast(bf16x8, v); }
DI f32x4 mfma(bf16x8 a, bf16x8 b, f32x4 c) { return __builtin_amdgcn_mfma_f32_16x16x32_bf16(a, b, c, 0, 0, 0); }
DI void mfma_acc(f32x4& c, bf16x8 a, bf16x8 b) { asm volatile("v_mfma_f32_16x16x32_bf16 %0, %1, %2, %0" : "+v"(c) : "v"(a), "v"(b)); }
DI void mfma_new(f32x4& c, bf16x8 a, bf16x8 b) { asm volatile("v_mfma_f32_16x16x32_bf16 %0, %1, %2, 0" : "=v"(c) : "v"(a), "v"(b)); }
DI void mfma_drain() { asm volatile("s_nop 7\n\ts_nop 1"); GAP(); }
template <class T> DI void pin(T& x) { asm volatile("" : "+v"(x)); }
template <class T> DI void keep(T x) { asm volatile("" ::"v"(x)); }
DI unsigned lane_now() { unsigned l; asm volatile("v_mbcnt_lo_u32_b32 %0, -1, 0\n\tv_mbcnt_hi_u32_b32 %0, -1, %0" : "=v"(l)); return l; }
DI unsigned spin(unsigned x) { asm volatile("" : "+s"(x)); return x; }
template <int N> DI void wait_vm() { if constexpr (N < 63) asm volatile("s_waitcnt vmcnt(%0)" ::"i"(N) : "memory"); }
template <int N> DI void wait_barrier() { asm volatile("s_waitcnt vmcnt(%0) lgkmcnt(0)\n\ts_barrier" ::"i"(N) : "memory"); }
DI void lds_barrier() { asm volatile("s_waitcnt lgkmcnt(0)\n\ts_barrier" ::: "memory"); }
DI float v_mul(float a, float b) { float r; asm volatile("v_mul_f32 %0, %1, %2" : "=v"(r) : "v"(a), "v"(b)); return r; }
DI float v_expn(float a) { float r; asm volatile("v_exp_f32_e64 %0, -%1" : "=v"(r) : "v"(a)); return r; }
DI float v_fmak(float a, float k) { float r; asm volatile("v_fma_f32 %0, %1, %2, %2" : "=v"(r) : "v"(a), "s"(k)); return r; }
DI unsigned v_xors(unsigned a, unsigned k) { unsigned r; asm volatile("v_xor_b32 %0, %2, %1" : "=v"(r) : "v"(a), "s"(k)); return r; }
template <unsigned K> DI unsigned v_addk(unsigned a) { unsigned r; asm volatile("v_add_u32 %0, %2, %1" : "=v"(r) : "v"(a), "i"(K)); return r; }
DI float v_rcp(float a) { float r; asm volatile("v_rcp_f32 %0, %1" : "=v"(r) : "v"(a)); return r; }
DI unsigned v_cvtpk(float a, float b) { unsigned r; asm volatile("v_cvt_pk_bf16_f32 %0, %1, %2" : "=v"(r) : "v"(a), "v"(b)); return r; }

#define STAMP(i) do {} while (0)

template <int LOFF, unsigned GOFF> DI void dma16(unsigned m0base, unsigned voff, rsrc_t srd) {
  unsigned t;
  asm volatile("s_add_u32 m0, %1, %2\n\ts_mov_b32 %0, %3\n\tbuffer_load_dwordx4 %4, %5, %0 offen lds"
               : "=&s"(t) : "s"(m0base), "i"(LOFF), "i"(GOFF), "v"(voff), "s"(srd) : "scc", "memory");
}
template <int GOFF> DI void load16_sc1(u32x4& d, unsigned voff, unsigned sbase, rsrc_t srd) {
  unsigned t;
  asm volatile("s_add_u32 %1, %3, %4\n\tbuffer_load_dwordx4 %0, %2, %5, %1 offen sc1" : "=v"(d), "=&s"(t) : "v"(voff), "s"(sbase), "i"(GOFF), "s"(srd) : "scc");
}
template <int OFF> DI void store16_sc1(u32x4 d, rsrc_t srd, unsigned voff, unsigned soff) {
  asm volatile("buffer_store_dwordx4 %0, %1, %2, %3 offen offset:%4 sc1\n\ts_nop 1" ::"v"(d), "v"(voff), "s"(srd), "s"(soff), "i"(OFF) : "memory");
}
DI void store16(u32x4 d, rsrc_t srd, unsigned voff, unsigned soff) {
  asm volatile("buffer_store_dwordx4 %0, %1, %2, %3 offen sc1\n\ts_nop 1" ::"v"(d), "v"(voff), "s"(srd), "s"(soff) : "memory");
}
DI void load16(u32x4& d, unsigned voff, rsrc_t srd) { asm volatile("buffer_load_dwordx4 %0, %1, %2, 0 offen" : "=v"(d) : "v"(voff), "s"(srd)); }
DI void load4(unsigned& d, unsigned voff, rsrc_t srd) { asm volatile("buffer_load_dword %0, %1, %2, 0 offen" : "=v"(d) : "v"(voff), "s"(srd)); }
template <unsigned SOFF> DI void load4s(unsigned& d, unsigned voff, rsrc_t srd) {
  unsigned t;
  asm volatile("s_mov_b32 %1, %4\n\tbuffer_load_dword %0, %2, %3, %1 offen" : "=v"(d), "=&s"(t) : "v"(voff), "s"(srd), "i"(SOFF));
}
DI void load4_sc1(unsigned& d, unsigned voff, rsrc_t srd) { asm volatile("buffer_load_dword %0, %1, %2, 0 offen sc1" : "=v"(d) : "v"(voff), "s"(srd)); }
template <int N, int SLEEP = 2> DI void flags_wait(unsigned& f, unsigned voff, unsigned tag, u64 mask, rsrc_t srd) {
  unsigned cnt;
  asm volatile("s_waitcnt vmcnt(%6)\n\ts_mov_b32 %1, %7\n"
               "1:\n\tv_cmp_ne_u32 vcc, %3, %0\n\ts_nop 1\n\ts_and_b64 vcc, vcc, %4\n\ts_cbranch_scc0 2f\n\t"
               "s_sleep %8\n\tbuffer_load_dword %0, %2, %5, 0 offen sc1\n\ts_waitcnt vmcnt(0)\n\ts_sub_u32 %1, %1, 1\n\ts_cbranch_scc0 1b\n"
               "2:"
               : "+v"(f), "=&s"(cnt) : "v"(voff), "s"(tag), "s"(mask), "s"(srd), "i"(N), "i"(SPIN_CAP), "i"(SLEEP) : "vcc", "scc", "memory");
}
DI void publish(unsigned tag, unsigned soff, rsrc_t srd) {
  unsigned t;
  asm volatile("v_mov_b32 %0, %1\n\ts_waitcnt vmcnt(0)\n\ts_mov_b64 exec, 1\n\tbuffer_store_dword %0, off, %3, %2 sc1\n\ts_mov_b64 exec, -1"
               : "=&v"(t) : "s"(tag), "s"(soff), "s"(srd) : "memory");
}

template <bool TWO> DI void gather7(u32x4 (&p)[7], u32x4 (&q)[8], unsigned& f, unsigned fo, unsigned v0, unsigned v1, unsigned tag, unsigned src0, unsigned s0, rsrc_t sp, rsrc_t sq, rsrc_t sf) {
  unsigned pend, rdy, t, cnt;
  if constexpr (TWO) {
#define X(R) "buffer_load_dwordx4 " R ", %20, %26, %17 offen sc1\n"
  asm volatile("s_mov_b32 %15, 0x7f\n\ts_mov_b32 %18, %28\n"
               "1:\n\tv_cmp_eq_u32 vcc, %22, %14\n\ts_nop 1\n\ts_and_b32 %16, vcc_lo, %15\n\ts_andn2_b32 %15, %15, %16\n\t"
               "s_bitcmp1_b32 %16, 0\n\ts_cbranch_scc0 3f\n\ts_sub_u32 %17, %23, 0\n\ts_and_b32 %17, %17, 7\n\ts_mul_i32 %17, %17, %29\n\ts_add_u32 %17, %17, %24\n\t"
               "buffer_load_dwordx4 %0, %20, %25, %17 offen offset:0 sc1\n\t" X("%7") "3:\n\t"
               "s_bitcmp1_b32 %16, 1\n\ts_cbranch_scc0 3f\n\ts_sub_u32 %17, %23, 1\n\ts_and_b32 %17, %17, 7\n\ts_mul_i32 %17, %17, %29\n\ts_add_u32 %17, %17, %24\n\t"
               "buffer_load_dwordx4 %1, %20, %25, %17 offen offset:1024 sc1\n\t" X("%8") "3:\n\t"
               "s_bitcmp1_b32 %16, 2\n\ts_cbranch_scc0 3f\n\ts_sub_u32 %17, %23, 2\n\ts_and_b32 %17, %17, 7\n\ts_mul_i32 %17, %17, %29\n\ts_add_u32 %17, %17, %24\n\t"
               "buffer_load_dwordx4 %2, %20, %25, %17 offen offset:2048 sc1\n\t" X("%9") "3:\n\t"
               "s_bitcmp1_b32 %16, 3\n\ts_cbranch_scc0 3f\n\ts_sub_u32 %17, %23, 3\n\ts_and_b32 %17, %17, 7\n\ts_mul_i32 %17, %17, %29\n\ts_add_u32 %17, %17, %24\n\t"
               "buffer_load_dwordx4 %3, %20, %25, %17 offen offset:3072 sc1\n\t" X("%10") "3:\n\t"
               "s_bitcmp1_b32 %16, 4\n\ts_cbranch_scc0 3f\n\ts_sub_u32 %17, %23, 4\n\ts_and_b32 %17, %17, 7\n\ts_mul_i32 %17, %17, %29\n\ts_add_u32 %17, %17, %24\n\t"
               "buffer_load_dwordx4 %4, %21, %25, %17 offen offset:0 sc1\n\t" X("%11") "3:\n\t"
               "s_bitcmp1_b32 %16, 5\n\ts_cbranch_scc0 3f\n\ts_sub_u32 %17, %23, 5\n\ts_and_b32 %17, %17, 7\n\ts_mul_i32 %17, %17, %29\n\ts_add_u32 %17, %17, %24\n\t"
               "buffer_load_dwordx4 %5, %21, %25, %17 offen offset:1024 sc1\n\t" X("%12") "3:\n\t"
               "s_bitcmp1_b32 %16, 6\n\ts_cbranch_scc0 3f\n\ts_sub_u32 %17, %23, 6\n\ts_and_b32 %17, %17, 7\n\ts_mul_i32 %17, %17, %29\n\ts_add_u32 %17, %17, %24\n\t"
               "buffer_load_dwordx4 %6, %21, %25, %17 offen offset:2048 sc1\n\t" X("%13") "3:\n\t"
               "s_cmp_eq_u32 %15, 0\n\ts_cbranch_scc1 2f\n\ts_sleep 1\n\t"
               "s_mov_b32 exec_lo, %15\n\ts_mov_b32 exec_hi, 0\n\tbuffer_load_dword %14, %19, %27, 0 offen sc1\n\ts_mov_b64 exec, -1\n\t"
               "s_waitcnt vmcnt(0)\n\ts_sub_u32 %18, %18, 1\n\ts_cbranch_scc0 1b\n2:\n\ts_waitcnt vmcnt(0)"
               : "=&v"(p[0]), "=&v"(p[1]), "=&v"(p[2]), "=&v"(p[3]), "=&v"(p[4]), "=&v"(p[5]), "=&v"(p[6]), "=&v"(q[0]), "=&v"(q[1]), "=&v"(q[2]), "=&v"(q[3]), "=&v"(q[4]), "=&v"(q[5]), "=&v"(q[6]), "+v"(f), "=&s"(pend), "=&s"(rdy), "=&s"(t), "=&s"(cnt)
               : "v"(fo), "v"(v0), "v"(v1), "s"(tag), "s"(src0), "s"(s0), "s"(sp), "s"(sq), "s"(sf), "i"(SPIN_CAP), "i"(8 * SLAB_W) : "vcc", "scc", "memory");
#undef X
  } else {
#define X(R) "\n"
  asm volatile("s_mov_b32 %15, 0x7f\n\ts_mov_b32 %18, %28\n"
               "1:\n\tv_cmp_eq_u32 vcc, %22, %14\n\ts_nop 1\n\ts_and_b32 %16, vcc_lo, %15\n\ts_andn2_b32 %15, %15, %16\n\t"
               "s_bitcmp1_b32 %16, 0\n\ts_cbranch_scc0 3f\n\ts_sub_u32 %17, %23, 0\n\ts_and_b32 %17, %17, 7\n\ts_mul_i32 %17, %17, %29\n\ts_add_u32 %17, %17, %24\n\t"
               "buffer_load_dwordx4 %0, %20, %25, %17 offen offset:0 sc1\n\t" X("%7") "3:\n\t"
               "s_bitcmp1_b32 %16, 1\n\ts_cbranch_scc0 3f\n\ts_sub_u32 %17, %23, 1\n\ts_and_b32 %17, %17, 7\n\ts_mul_i32 %17, %17, %29\n\ts_add_u32 %17, %17, %24\n\t"
               "buffer_load_dwordx4 %1, %20, %25, %17 offen offset:1024 sc1\n\t" X("%8") "3:\n\t"
               "s_bitcmp1_b32 %16, 2\n\ts_cbranch_scc0 3f\n\ts_sub_u32 %17, %23, 2\n\ts_and_b32 %17, %17, 7\n\ts_mul_i32 %17, %17, %29\n\ts_add_u32 %17, %17, %24\n\t"
               "buffer_load_dwordx4 %2, %20, %25, %17 offen offset:2048 sc1\n\t" X("%9") "3:\n\t"
               "s_bitcmp1_b32 %16, 3\n\ts_cbranch_scc0 3f\n\ts_sub_u32 %17, %23, 3\n\ts_and_b32 %17, %17, 7\n\ts_mul_i32 %17, %17, %29\n\ts_add_u32 %17, %17, %24\n\t"
               "buffer_load_dwordx4 %3, %20, %25, %17 offen offset:3072 sc1\n\t" X("%10") "3:\n\t"
               "s_bitcmp1_b32 %16, 4\n\ts_cbranch_scc0 3f\n\ts_sub_u32 %17, %23, 4\n\ts_and_b32 %17, %17, 7\n\ts_mul_i32 %17, %17, %29\n\ts_add_u32 %17, %17, %24\n\t"
               "buffer_load_dwordx4 %4, %21, %25, %17 offen offset:0 sc1\n\t" X("%11") "3:\n\t"
               "s_bitcmp1_b32 %16, 5\n\ts_cbranch_scc0 3f\n\ts_sub_u32 %17, %23, 5\n\ts_and_b32 %17, %17, 7\n\ts_mul_i32 %17, %17, %29\n\ts_add_u32 %17, %17, %24\n\t"
               "buffer_load_dwordx4 %5, %21, %25, %17 offen offset:1024 sc1\n\t" X("%12") "3:\n\t"
               "s_bitcmp1_b32 %16, 6\n\ts_cbranch_scc0 3f\n\ts_sub_u32 %17, %23, 6\n\ts_and_b32 %17, %17, 7\n\ts_mul_i32 %17, %17, %29\n\ts_add_u32 %17, %17, %24\n\t"
               "buffer_load_dwordx4 %6, %21, %25, %17 offen offset:2048 sc1\n\t" X("%13") "3:\n\t"
               "s_cmp_eq_u32 %15, 0\n\ts_cbranch_scc1 2f\n\ts_sleep 1\n\t"
               "s_mov_b32 exec_lo, %15\n\ts_mov_b32 exec_hi, 0\n\tbuffer_load_dword %14, %19, %27, 0 offen sc1\n\ts_mov_b64 exec, -1\n\t"
               "s_waitcnt vmcnt(0)\n\ts_sub_u32 %18, %18, 1\n\ts_cbranch_scc0 1b\n2:\n\ts_waitcnt vmcnt(0)"
               : "=&v"(p[0]), "=&v"(p[1]), "=&v"(p[2]), "=&v"(p[3]), "=&v"(p[4]), "=&v"(p[5]), "=&v"(p[6]), "=&v"(q[0]), "=&v"(q[1]), "=&v"(q[2]), "=&v"(q[3]), "=&v"(q[4]), "=&v"(q[5]), "=&v"(q[6]), "+v"(f), "=&s"(pend), "=&s"(rdy), "=&s"(t), "=&s"(cnt)
               : "v"(fo), "v"(v0), "v"(v1), "s"(tag), "s"(src0), "s"(s0), "s"(sp), "s"(sq), "s"(sf), "i"(SPIN_CAP), "i"(8 * SLAB_W) : "vcc", "scc", "memory");
#undef X
  }
}
template <int OFF, int W> DI void store8_sc1(u32x2 d, rsrc_t srd, unsigned voff, unsigned wave) {
  asm volatile("s_cmp_lg_u32 %4, %5\n\ts_cbranch_scc1 1f\n\tbuffer_store_dwordx2 %0, %1, %2, 0 offen offset:%3 sc1\n1:" ::"v"(d), "v"(voff), "s"(srd), "i"(OFF), "s"(wave), "i"(W) : "scc", "memory");
}
DI void load8x5_sc1(u32x2* d, rsrc_t srd, unsigned voff, unsigned wave) {
  asm volatile("s_cmp_lg_u32 %7, 0\n\ts_cbranch_scc1 1f\n\tbuffer_load_dwordx2 %0, %5, %6, 0 offen sc1\n\tbuffer_load_dwordx2 %1, %5, %6, 0 offen offset:512 sc1\n\t"
               "buffer_load_dwordx2 %2, %5, %6, 0 offen offset:1024 sc1\n\tbuffer_load_dwordx2 %3, %5, %6, 0 offen offset:1536 sc1\n\tbuffer_load_dwordx2 %4, %5, %6, 0 offen offset:2048 sc1\n1:"
               : "=&v"(d[0]), "=&v"(d[1]), "=&v"(d[2]), "=&v"(d[3]), "=&v"(d[4]) : "v"(voff), "s"(srd), "s"(wave) : "scc");
}
DI void publish_if(unsigned tag, unsigned soff, rsrc_t srd, unsigned off) {
  unsigned t;
  asm volatile("s_cmp_lg_u32 %4, 0\n\ts_cbranch_scc1 1f\n\tv_mov_b32 %0, %1\n\ts_waitcnt vmcnt(0)\n\ts_mov_b64 exec, 1\n\tbuffer_store_dword %0, off, %3, %2 sc1\n\ts_mov_b64 exec, -1\n1:"
               : "=&v"(t) : "s"(tag), "s"(soff), "s"(srd), "s"(off) : "scc", "memory");
}
DI void flag_only(unsigned tag, unsigned soff, rsrc_t srd) {
  unsigned t;
  asm volatile("v_mov_b32 %0, %1\n\ts_mov_b64 exec, 1\n\tbuffer_store_dword %0, off, %3, %2 sc1\n\ts_mov_b64 exec, -1" : "=&v"(t) : "s"(tag), "s"(soff), "s"(srd) : "memory");
}
DI void load8_if(u32x2& d, unsigned voff, unsigned soff, rsrc_t srd, unsigned off) {
  asm volatile("s_cmp_lg_u32 %4, 0\n\ts_cbranch_scc1 1f\n\tbuffer_load_dwordx2 %0, %1, %2, %3 offen\n1:" : "=v"(d) : "v"(voff), "s"(srd), "s"(soff), "s"(off) : "scc");
}

template <class T> DI T pick(bool c, T a, T b) { return c ? a : b; }
DI unsigned vand(unsigned a, unsigned b) { return a & b; }
DI float fma4(float a, float b, float c, float d, const float (&k)[4]) {
  float r = a * k[0]; pin(r); r = __builtin_fmaf(b, k[1], r); r = __builtin_fmaf(c, k[2], r); return __builtin_fmaf(d, k[3], r);
}
DI float vmax(float a, float b) { float r; asm("v_max_f32 %0, %1, %2" : "=v"(r) : "v"(a), "v"(b)); return r; }
template <bool MAX> DI float x16(float v) {
  const unsigned u = __builtin_bit_cast(unsigned, v);
  auto a = __builtin_amdgcn_permlane16_swap(u, u, false, false);
  const float p = __builtin_bit_cast(float, (unsigned)a[0]), q = __builtin_bit_cast(float, (unsigned)a[1]);
  return MAX ? vmax(p, q) : p + q;
}
template <bool MAX> DI float x32(float v) {
  const unsigned u = __builtin_bit_cast(unsigned, v);
  auto a = __builtin_amdgcn_permlane32_swap(u, u, false, false);
  const float p = __builtin_bit_cast(float, (unsigned)a[0]), q = __builtin_bit_cast(float, (unsigned)a[1]);
  return MAX ? vmax(p, q) : p + q;
}
template <int W> DI void swp(float& a, float& b) {
  const unsigned x = __builtin_bit_cast(unsigned, a), y = __builtin_bit_cast(unsigned, b);
  if constexpr (W == 16) { auto r = __builtin_amdgcn_permlane16_swap(x, y, false, false); a = __builtin_bit_cast(float, (unsigned)r[0]); b = __builtin_bit_cast(float, (unsigned)r[1]); }
  else { auto r = __builtin_amdgcn_permlane32_swap(x, y, false, false); a = __builtin_bit_cast(float, (unsigned)r[0]); b = __builtin_bit_cast(float, (unsigned)r[1]); }
}
template <bool MAX> DI void xq8(float (&m)[8], float k) {
  auto op = [](float p, float q) { return MAX ? vmax(p, q) : p + q; };
  float a[4], d[4];
  sfor<4>([&](auto I) { swp<16>(m[2 * I], m[2 * I + 1]); }); GAP();
  sfor<4>([&](auto I) { a[I] = op(m[2 * I], m[2 * I + 1]); pin(a[I]); }); GAP();
  sfor<2>([&](auto I) { swp<32>(a[2 * I], a[2 * I + 1]); }); GAP();
  sfor<2>([&](auto I) { float c = op(a[2 * I], a[2 * I + 1]); if constexpr (!MAX) c = __builtin_amdgcn_rcpf(c) * k; d[2 * I] = c; d[2 * I + 1] = c; pin(d[2 * I + 1]); }); GAP();
  sfor<2>([&](auto I) { swp<32>(d[2 * I], d[2 * I + 1]); }); GAP();
  sfor<4>([&](auto I) { m[2 * I] = d[I]; m[2 * I + 1] = d[I]; pin(m[2 * I + 1]); }); GAP();
  sfor<4>([&](auto I) { swp<16>(m[2 * I], m[2 * I + 1]); }); GAP();
}
template <int CTRL> DI float dpp_add(float v) { return v + __builtin_bit_cast(float, __builtin_amdgcn_update_dpp(0, __builtin_bit_cast(int, v), CTRL, 0xf, 0xf, true)); }
DI float wave_sum(float v) { return x32<false>(x16<false>(dpp_add<0x140>(dpp_add<0x141>(dpp_add<0x4e>(dpp_add<0xb1>(v)))))); }

constexpr int LDS_V = 46080 + 2560, LDS_K = 92160 + 2560;
constexpr int LDS_X = 6 * 24576, LDS_B = LDS_X + 16384;
constexpr int DA = 4;
constexpr float LOG2E = 1.4426950408889634f;
constexpr int cmin(int a, int b) { return a < b ? a : b; }

enum { NONE, W1, XF, TOUCH, W2, H2, SAMPLE,   CHECK, PUB, PUBH, XRES, HALO };
struct Op { int kind, a, b; };
template <bool QKV> struct Plan {
  static constexpr int NCG = QKV ? 5 : 8, KS = QKV ? 6 : 9, NST = 54 / KS, SPU = 18 / KS, NSLOT = QKV ? 3 : 2;
  static constexpr int SB = QKV ? 6 * 5120 : 9 * 8192, IPS = KS * NCG, NIT = NST * IPS, BAR = IPS - (DA - 1), NPRO = 6;
  static constexpr bool kmajor(int n) { return QKV ? n % 90 < 75 : n < 96; }
  static constexpr int jof(int n) { return kmajor(n) ? n % NCG : QKV ? (n % 15) / 3 : (n < 144 ? (n - 96) / 12 : (n % IPS) / 18) * 2 + n % 2; }
  static constexpr int sof(int n) { return kmajor(n) ? (n % (18 * NCG)) / NCG : QKV ? 15 + n % 3 : n < 144 ? 12 + (n % 12) / 2 : KS * ((n / IPS) % SPU) + (n % 18) / 2; }
  static constexpr int slot(int st) { return QKV ? (st + 1) % 3 : st % 2; }
  static constexpr int NT = QKV ? 12 : 8, NIT2 = 21 * NT, A2 = QKV ? 9 : 6, M1 = A2 * NT, LT = QKV ? LQ : LF, M2 = M1 + (21 - LT - A2) * (NT - 1), M3 = M2 + LT * (NT - 1);
  static constexpr int SB2 = NT * 1024, NSL2 = 147456 / SB2, WIN = QKV ? 4 : 8, O2 = QKV ? 0 : 12;
  static constexpr int gt(int k) { return QKV ? (k + 8) % 12 : k; }
  static constexpr int st2(int m) { return m < M1 ? m / NT : m < M2 ? A2 + (m - M1) / (NT - 1) : m < M3 ? 21 - LT + (m - M2) % LT : A2 + m - M3; }
  static constexpr int ti2(int m) { return m < M1 ? m % NT : m < M3 ? gt(m < M2 ? (m - M1) % (NT - 1) : (m - M2) / LT) : 7; }
  static constexpr bool first2(int m) { return m < M1 ? m % NT == 0 : m < M2 ? (m - M1) % (NT - 1) == 0 : m < M2 + LT; }
  static constexpr int win2(int m) { return (st2(m) % NSL2) / WIN; }
  static constexpr int pwin2(int m) { for (int i = m - 1; i >= 0; --i) if (ti2(i) == ti2(m)) return win2(i); return -1; }
  static constexpr int bar2(int m) {
    if (QKV) return m == 57 ? 11 : m == M1 - 3 ? 14 : m == M2 + LT - 9 ? 20 : -1;
    return m == 66 ? 17 : m == M2 + LT - 6 ? 20 : -1;
  }
  static constexpr int hstep(int a) { return a < O2 ? a : a - 3; }
  static constexpr bool rebase(int m) {
    if (m >= NIT) return m - NIT < NIT2 && win2(m - NIT) != pwin2(m - NIT) && !(QKV && m - NIT < NT);
    if (QKV) return (m / IPS) % 3 != 1 && m >= IPS && m % IPS < 5;
    return m < 144 ? (m >= 72 && m < 80) : m % 18 < 2;
  }
  static constexpr int estart(int su) { return su == 0 ? 108 : 144 * su + 92; }
  static constexpr int eidx(int su, int n, int t) {
    if (n < estart(su) || n > estart(su) + 100 || (t == 1 && rebase(n + 4))) return -1;
    int c = 0;
    for (int i = estart(su); i < n; ++i) c += rebase(i + 4) ? 1 : 2;
    return c + t < 176 ? c + t : -1;
  }
  static constexpr int eat(int su, int e) { int n = estart(su); for (int c = 0; (c += rebase(n + 4) ? 1 : 2) <= e;) ++n; return n; }
  static constexpr Op w2op(int i) {
    if (!QKV) return Op{W2, i, 0};
    if (i < 5) return i == 2 ? Op{W2, 0, 1} : i == 4 ? Op{W2, 2, 1} : Op{W2, i < 2 ? i : 2, 0};
    return (i - 5) % 3 == 2 ? Op{W2, 3 + (i - 5) / 3 * 2, 1} : Op{W2, 3 + (i - 5) / 3 * 2 + (i - 5) % 3, 0};
  }
  static constexpr int NPRE2 = QKV ? 0 : 21;
  static constexpr Op pre2(int i) { return i < 9 ? w2op(i) : Op{H2, (i - 9) / 2, (i - 9) & 1}; }
  static constexpr Op at1(int n) {
    const int st = n / IPS, it = n % IPS;
    if (QKV) {
      if (n % 2 == 0 && NPRO + n / 2 < 36) return Op{XF, NPRO + n / 2, 0};
      if (n >= 45 && n < 60 && n % 4 == 1) return Op{W1, 3, (n - 45) / 4};
      if (st == 2 && it % 3 == 1 && it < 12) return Op{W1, 4, it / 3};
      if (st > 2 && st + 2 < NST && it % 3 == 1 && it < 12) return Op{W1, st + 2, it / 3};
      if (n == 200) return Op{PUBH, 0, 0};
      if (n == 234) return Op{SAMPLE, 0, 0};
      if (n == 254) return Op{CHECK, 3, 0};
      if (n == 255 || n == 257) return Op{HALO, (n - 255) / 2, 0};
      return Op{NONE, 0, 0};
    }
    if (n % 3 == 0 && NPRO + n / 3 < 36) return Op{XF, NPRO + n / 3, 0};
    if (st >= 1 && st <= 4) { const int i0 = st == 3 ? 10 : 1; if (it >= i0 && (it - i0) % 3 == 0 && (it - i0) / 3 < 9) return Op{W1, st + 1, (it - i0) / 3}; }
    if (n == 224) return Op{PUB, 0, 0};
    if (st >= 1 && st <= 3 && it == (st == 3 ? 37 : 31)) return Op{TOUCH, st + 2, 0};
    if (st >= 1 && st <= 3 && it == (st == 3 ? 39 : 33)) return Op{TOUCH, st - 1, 1};
    if (n == 330) return Op{SAMPLE, 0, 0};
    if (n == 352) return Op{PUB, 1, 0};
    if (n == 353) return Op{CHECK, 0, 0};
    if (n == 407) return Op{SAMPLE, 1, 0};
    if (n == 431) return Op{CHECK, 1, 0};
    if (n >= 358 && n % 3 == 1 && (n - 358) / 3 < 21) return pre2((n - 358) / 3);
    return Op{NONE, 0, 0};
  }
  static constexpr Op at2(int m) {
    if (QKV) {
      if (m == 12) return Op{PUB, 3, 0};
      if (m >= 14 && m < 44 && m % 3 == 2) return w2op(8 + (m - 14) / 3);
      if (m == 43) return Op{W2, 11, 2};
      if (m == 47) return Op{SAMPLE, 1, 0};
      if (m >= 58 && m < 68 && m % 2 == 0) return m == 60 ? Op{W2, 12, 2} : w2op(18 + (m - 58) / 2);
      if (m == 69) return Op{CHECK, 1, 0};
      if (m >= 70 && m < 94 && m % 2 == 0) return Op{H2, 9 + (m - 70) / 4, ((m - 70) / 2) & 1};
      if (m >= 106 && m < 124 && m % 2 == 0) return w2op(23 + (m - 106) / 2);
      if (m == QS) return Op{SAMPLE, 2, 0};
      if (m == QS + 18) return Op{CHECK, 2, 0};
      if (m > QS + 18 && m <= QS + 30) return Op{H2, 15 + (m - QS - 19) / 2, (m - QS - 19) & 1};
      if (m >= M2 + LT && m < M2 + LT + 4) return Op{XRES, m - M2 - LT, 0};
      return Op{NONE, 0, 0};
    }
    if (m < 42 && m % 2 == 0) { const int e = m / 2; return e < 18 ? (e % 3 == 2 ? w2op(9 + e / 3) : Op{H2, 6 + e / 3, e % 3}) : w2op(e - 3); }
    if (m == 27) return Op{SAMPLE, 1, 0};
    if (m == PB) return Op{PUB, 2, 0};
    if (HT && m == PB + 1) return Op{TOUCH, 0, 2};
    if (m == PB + 2) return Op{CHECK, 4, 0};
    if (m > PB + 2 && m <= PB + 8) return Op{H2, 12 + (m - PB - 3) / 2, (m - PB - 3) & 1};
    if (m == SM) return Op{SAMPLE, 2, 0};
    if (m >= 68 && m < 74 && m % 2 == 0) return w2op(18 + (m - 68) / 2);
    if (m == SM + 18) return Op{CHECK, 2, 0};
    if (m > SM + 18 && m <= SM + 30) return Op{H2, 15 + (m - SM - 19) / 2, (m - SM - 19) & 1};
    if (m == M2 + LT || m == M2 + LT + 2) return Op{XRES, (m - M2 - LT) / 2, 0};
    return Op{NONE, 0, 0};
  }
  static constexpr int wait1(int n0, int kind, int amax) {
    int cnt = 0, need = -1;
    for (int i = -NPRO; i < n0; ++i) {
      const Op o = i < 0 ? Op{XF, i + NPRO, 0} : at1(i);
      if (o.kind == PUB) need = -1;
      if (o.kind == NONE || o.kind > SAMPLE) continue;
      ++cnt;
      if (o.kind == kind && o.a <= amax) need = cnt;
    }
    return need < 0 || cnt - need > 62 ? 63 : cnt - need;
  }
  static constexpr int wait2(int m0, int kind, int amax) {
    int cnt = 0, need = -1;
    for (int i = -NPRE2; i < m0; ++i) {
      const Op o = i < 0 ? pre2(i + NPRE2) : at2(i);
      if (o.kind == PUB) need = -1;
      if (o.kind == NONE || o.kind > SAMPLE) continue;
      ++cnt;
      if (o.kind == kind && o.a <= amax) need = cnt;
    }
    return need < 0 || cnt - need > 62 ? 63 : cnt - need;
  }
};
using PF = Plan<false>;
static_assert(PF::eat(0, 175) < 224 - 16 && PF::eat(1, 175) < 352 - 16 && PF::eat(2, 175) < PF::NIT + PB - 6, "a unit is published after its last store");
static_assert(PF::eat(0, 44) >= 96 + 26 && PF::eat(0, 88) >= 96 + 38 && PF::eat(0, 132) >= 96 + 50, "unit 0: accumulators complete before they are read");
static_assert(PF::eat(0, 39) < 144 && PF::eat(0, 83) < 162 && PF::eat(0, 127) < 180 && PF::eat(0, 171) < 198, "unit 0: accumulators read before they restart");
static_assert(PF::eat(1, 44) >= 216 + 38 && PF::eat(1, 88) >= 216 + 56 && PF::eat(1, 132) >= 216 + 74 && PF::eat(1, 171) < 288 + 54, "unit 1");
static_assert(PF::eat(0, 153) > 144 + 30 && PF::eat(0, 153) < 213 && PF::eat(1, 65) > 216 + 40 && PF::eat(1, 65) < 285, "early weight waits follow the last request");
static_assert(PF::eat(2, 44) >= 360 + 38 && PF::eat(2, 88) >= 360 + 56 && PF::eat(2, 132) >= 360 + 74, "unit 2");

struct Args { const char* x; const char* wqkv; const char* wo; const char* wg; const char* wu; const char* wd; char* ws; char* out; };

DI void ln_load(u32x4& v, unsigned& v9, const char* x, int row, int lane) {
  const rsrc_t sx = mk_rsrc(x, S * D * 2);
  const unsigned xo = (unsigned)(row * 1152);
  load16(v, xo + lane * 16, sx);
  asm volatile("buffer_load_ushort %0, %1, %2, 0 offen offset:1024" : "=v"(v9) : "v"(xo + lane * 2), "s"(sx));
}
DI void ln_row(u32x4 v, unsigned v9, unsigned smem, int wave, int lane) {
  float f[9];
  sfor<4>([&](auto E) { f[2 * E] = bflo(v[(int)E]); f[2 * E + 1] = bfhi(v[(int)E]); });
  f[8] = bflo(v9);
  const float mean = wave_sum(((f[0] + f[1]) + (f[2] + f[3])) + ((f[4] + f[5]) + (f[6] + f[7])) + f[8]) * (1.0f / D);
  float q = 0.f;
  sfor<9>([&](auto E) { const float d = f[E] - mean; q += d * d; });
  const float var = wave_sum(q) * (1.0f / D);
  const float mb = rbf(mean), rs = LOG2E * __builtin_amdgcn_rcpf(rbf(__builtin_amdgcn_sqrtf(rbf(rbf(var) + 1e-5f))));
  sfor<4>([&](auto E) { v[(int)E] = cvtpk((f[2 * E] - mb) * rs, (f[2 * E + 1] - mb) * rs); });
  *(LDS u32x4*)(smem + lane * 128 + wave * 16) = v;
  *(LDS unsigned short*)(smem + 8192 + (lane >> 3) * 128 + wave * 16 + (lane & 7) * 2) = (unsigned short)cvtpk((f[8] - mb) * rs, 0.f);
}
DI void ln_publish(char* ws, unsigned smem, unsigned tag, int gid, int lane) {
  u32x4 t[9];
  const unsigned fo = spin(rfl(FL_X + FS * gid));
  sfor<9>([&](auto I) { t[I] = *(LDS u32x4*)(smem + 1024 * I + lane * 16); });
  const unsigned o = (unsigned)((gid >> 1) * 18432 + (gid & 1) * 128 + (lane >> 3) * 256 + (lane & 7) * 16);
  const rsrc_t sxn = mk_rsrc(ws + WS_XN, XN_B);
  sfor<9>([&](auto I) { store16_sc1<0>(t[I], sxn, o, 2048 * I); });
  publish(tag, fo, mk_rsrc(ws + WS_FL, FL_B));
}

template <bool QKV>
DI void body(const Args& A, unsigned smem, unsigned tag, int rt, int col0, int wave, int lane, unsigned tch0) {
  using P = Plan<QKV>;
  constexpr int NCG = P::NCG, KS = P::KS, IPS = P::IPS, NIT = P::NIT, SPU = P::SPU, SB = P::SB, NT = P::NT, NIT2 = P::NIT2;
  constexpr int PL1 = QKV ? 3 : 4;
  constexpr unsigned RS = QKV ? 3u * D * 2u : F * 2u, WB = QKV ? D * 3 * D * 2 : D * F * 2;
  char* ws = A.ws;
  const int bid = blockIdx.x, isU = wave >> 2, w3 = wave & 3;
  const rsrc_t sF = mk_rsrc(ws + WS_FL, FL_B), sxn = mk_rsrc(ws + WS_XN, XN_B);
  const int cgl = ((lane & 7) >> 1) ^ ((lane >> 4) & 3);
  const int pc = 2 * cgl + (lane & 1);
  const unsigned wv = (unsigned)(((QKV ? 32 * isU : 0) + 16 * (w3 & 1) + 4 * (w3 >> 1) + 8 * (lane >> 5) + ((lane >> 3) & 3)) * RS + col0 * 2 + pc * 16);
  const unsigned wu = (unsigned)((8 * ((lane >> 3) & 3) + 4 * (lane >> 5) + ((lane >> 1) & 3)) * RS + col0 * 2 + 128);
  const char* wA = QKV ? A.wqkv : A.wg;
  const rsrc_t sw = mk_rsrc(isU && !QKV ? A.wu : wA, WB);
  const unsigned m0b = smem + isU * (QKV ? 5120 : SB / 2) + w3 * 1024;
  const int us = min(wave, 5);
  const unsigned m0u = smem + 4096 + us * 5120, wus = wu + us * 32 * RS;
  auto w1 = [&](auto ST, auto B, unsigned m0, unsigned vo, rsrc_t srd) {
    constexpr int a = CV(ST), b = CV(B) % (QKV ? 3 : 9), su = a / SPU;
    constexpr unsigned sucol = QKV ? (su == 0 ? 1152u : su == 1 ? 2304u : 0u) : su * 128u;
    dma16<P::slot(a) * SB + b * (QKV ? 10240 : 4096), 32 * (KS * (a % SPU) + (QKV ? 2 : 1) * b) * RS + sucol>(m0, vo, srd);
  };
  if (isU) {
    const rsrc_t sa = mk_rsrc(wA, WB);
    if constexpr (!QKV) sfor<2>([&](auto ST) { sfor<9>([&](auto B) { w1(ST, B, m0b - SB / 2, wv, sa); w1(ST, B, m0b, wv, sw); }); });
    else sfor<3>([&](auto ST) {
      sfor<3>([&](auto B) { w1(ST, B, m0b - 5120, wv - 32 * RS, sw); w1(ST, B, m0b, wv, sw); });
      w1(ST, ic<3>{}, smem + 4096 + w3 * 5120, wu + w3 * 32 * RS, sw); w1(ST, ic<3>{}, m0u, wus, sw);
    });
  }
  const int t16 = rt * 16 + 2 * wave;
  const unsigned xs = spin(t16 * 18432), l16 = lane * 16;
  u32x4 xf[18][2];
  auto xload = [&](auto AA) { constexpr int a = CV(AA); load16_sc1<(a / 2) * 1024 + (a & 1) * 18432>(xf[a / 2][a & 1], l16, xs, sxn); };
  if (wave == 0) {
    const unsigned fo = FL_X + FS * (rt * 32 + (lane & 31));
    __builtin_amdgcn_s_sleep(20);
    unsigned f; load4_sc1(f, fo, sF); flags_wait<0, 1>(f, fo, tag, ~0ull, sF);
  }
  asm volatile("s_barrier" ::: "memory");
  STAMP(4);
  sfor<P::NPRO>(xload);
  GAP();
  const int b2 = bid & 7, tile = bid >> 3, nc2 = QKV ? 768 : (tile - 3 * rt) * 256;
  const rsrc_t sh = mk_rsrc(ws + WS_H, HBUF_B), sS = mk_rsrc(ws + WS_SLAB, SLAB_B);
  const rsrc_t sWd = mk_rsrc(A.wd + 576 * 1152 * b2, 576 * 1152), sWo = mk_rsrc(A.wo + 72 * 1152 * b2, 64 * 1152), sWp = mk_rsrc(A.wo + (72 * b2 + 64) * 1152, 8 * 1152);
  const unsigned m0c = spin(smem + wave * 1024), m0d = spin(smem + 8192 + w3 * 1024 + isU * P::SB2);
  const unsigned hb = spin((t16 * KSTOT + 18 * b2) * 1024), ho = spin((t16 * KSTOT + 144 + 3 * b2) * 1024), hst = spin((t16 * KSTOT + (col0 >> 5)) * 1024);
  const unsigned oso = spin(t16 * 16 * 1152 + nc2), sl = spin(((tile * 8 + b2) * 8 + wave) * SLAB_W);
  const unsigned xt = spin(((wave - b2) & 7) >> 1), guest = spin((wave - b2) & 1);
  const unsigned fme = spin(FL_H + ((rt * 8 + wave) * 96 + (QKV ? 12 * b2 : 4 * (col0 / 192))) * FS);
  const unsigned tab = smem + LDS_X + wave * 2048 + l16;
  const bool wdo = tile * 8 + wave >= 162;
  unsigned ab[NCG];
  u32x4 t0;
  {
    const unsigned l = lane, b0 = smem + (l >> 2) * 128 + ((l >> 3) & 3) * 32 + 8 * (l & 3);
    t0[0] = (8 * w3 + (l >> 3)) * 1152 + nc2 + isU * 128 + pc * 16;
    t0[1] = FL_H + ((rt * 8 + wave) * 96 + 12 * b2 + (l < 12 ? l : 0)) * FS;
    t0[2] = b0;
    t0[3] = (l & 15) * 1152 + (l >> 4) * 8;
    *(LDS u32x4*)tab = t0;
    if constexpr (!QKV) *(LDS u32x2*)(tab + 1024) = u32x2{
      pick<unsigned>(l < 9, (rt * 36 + w3 * 9 + l) * RS + col0 * 2, 0x80000000u),
      pick<unsigned>(l < 11, (wdo ? tile * 8 + wave - 162 : tile * 8 + wave) * 4096 + l * 128, 0x80000000u)};
    if constexpr (QKV) sfor<5>([&](auto C) { ab[C] = (CV(C) < 4 ? b0 ^ (CV(C) * 32) : smem + 4096 + 8 * l) + SB; pin(ab[C]); });
    else sfor<8>([&](auto J) { ab[J] = (b0 ^ ((CV(J) >> 1) * 32)) + (CV(J) & 1) * (SB / 2); pin(ab[J]); });
  }
  u32x4 hf[21][2], hp[2], gp;
  unsigned fl, cur[2], wv3, tch[2], tk[3][2], cb[NT];
  u32x2 xres[4], pk[5][2], hl[10];
  unsigned l8 = lane * 8;
  if constexpr (QKV) pin(l8);
  const unsigned stg = smem + wave * 5120 + l8;
  auto ldtab = [&](int i) { return *(volatile LDS unsigned*)(tab + 1024 + 4 * i); };
  auto vmem = [&](auto K, auto AA, auto BB, auto NN) {
    constexpr int kind = CV(K), a = CV(AA), b = CV(BB);
    if constexpr (kind == W1) w1(AA, BB, QKV && b == 3 ? m0u : m0b, QKV && b == 3 ? wus : wv, sw);
    else if constexpr (kind == XF) xload(AA);
    else if constexpr (kind == TOUCH) {
      if constexpr (b == 2) load4_sc1(tch[0], (l16 & 0xf0) * 8 + (l16 & 0x100) * (KSTOT * 4) + (l16 >> 9) * 0x80000000u + hst + 4096, sh);
      else if constexpr (b == 0) load4s<288 * (a % 2) * RS + (a / 2) * 128>(tch[0], tch[0], sw);
      else load4s<a * 11 * 128>(tch[1], tch[1], mk_rsrc(wdo ? A.wo + 72 * 1152 * b2 : A.wd + 576 * 1152 * b2, wdo ? 72 * 1152 - 4096 : 576 * 1152 - 4096));
    } else if constexpr (kind == XRES) load8_if(xres[a], t0[3], oso + (a & 1) * 16 * 1152 + (a < 2 ? 32 * b2 : 256 + 32 * xt), mk_rsrc(A.x, S * D * 2), a < 2 ? 0u : guest);
    else if constexpr (kind == W2) {
      constexpr bool isO = a >= P::O2 && a < P::O2 + 3;
      constexpr int e = P::hstep(a), row = isO ? (a - P::O2 == 1 ? 32 : 0) : 192 * ((e % 6) / 2) + 64 * (e / 6) + 32 * (e & 1);
      const rsrc_t r = !isO ? sWd : a - P::O2 < 2 ? sWo : sWp;
      if constexpr (b == 0) dma16<(a % P::NSL2) * P::SB2, row * 1152>(m0c, t0[0], r);
      else if constexpr (b == 1 && !(isO && a == 2)) dma16<(a % P::NSL2) * P::SB2, row * 1152>(m0d, wv3, r);
      else dma16<(a % P::NSL2) * P::SB2 + 8192, row * 1152 + 256>(smem + w3 * 1024, t0[0] - isU * 128, r);
    } else if constexpr (kind == H2) {
      constexpr int e = P::hstep(a);
      if constexpr (a >= P::O2 && a < P::O2 + 3) load16_sc1<(b * KSTOT + a - P::O2) * 1024>(hf[a][b], l16, ho, sh);
      else load16_sc1<(b * KSTOT + 6 * ((e % 6) / 2) + 2 * (e / 6) + (e & 1)) * 1024>(hf[a][b], l16, hb, sh);
    } else if constexpr (kind == HALO) load8x5_sc1(hl + 5 * a, mk_rsrc(ws + WS_HALO + (bid - 200) * 5120 + a * 2560, rt > 0 ? 2560 : 0), l8, wave);
    else if constexpr (kind == SAMPLE) load4_sc1(fl, t0[1], sF);
    else if constexpr (kind == CHECK) flags_wait<CV(NN), QKV ? 4 : 2>(fl, t0[1], tag, a == 0 ? 0x111ull : a == 1 ? 0x222ull : a == 2 ? 0x444ull : a == 4 ? 0x8ull : wave == 0 && rt > 0 ? 0x80ull : 0ull, sF);
    else if constexpr (kind == PUB) publish(tag, fme + a * FS, sF);
    else if constexpr (kind == PUBH) publish_if(tag, fme + (96 + 7) * FS, sF, wave ^ 7);
  };
  f32x4 acc[NCG][2], acc2[NT][2];
  bf16x4 al[DA], ah[DA];
  auto aread = [&](auto N, auto T) {
    constexpr int n = CV(N);
    bf16x4 r;
    if constexpr (n >= NIT) r = trread(cb[P::ti2(n - NIT)], ((P::st2(n - NIT) % P::NSL2) % P::WIN) * P::SB2 + CV(T) * 2048);
    else if constexpr (QKV) r = trread(ab[P::jof(n)], (P::sof(n) % 12) * 5120 + CV(T) * (P::jof(n) < 4 ? 2048 : 512));
    else r = trread(n < 144 ? ab[P::jof(n)] : cur[P::jof(n) & 1], (P::sof(n) % KS) * 4096 + CV(T) * 2048);
    if constexpr (CV(T) == 0) al[n % DA] = r; else ah[n % DA] = r;
  };
  auto rebase = [&](auto M) {
    constexpr int m = CV(M);
    if constexpr (m >= NIT) {
      constexpr int p = m - NIT, i = P::ti2(p);
      if constexpr (P::pwin2(p) >= 0) cb[i] = v_addk<(unsigned)((P::win2(p) - P::pwin2(p)) * P::WIN * P::SB2)>(cb[i]);
      else { const unsigned ph = i < 8 ? (i + b2 + 1) & 7 : i; cb[i] = v_xors(t0[2], (ph & 3) * 32 + (ph >> 2) * 4096); }
    } else if constexpr (QKV) ab[P::jof(m)] = v_addk<(m / IPS) % 3 == 0 ? (unsigned)SB : (unsigned)-SB>(ab[P::jof(m)]);
    else if constexpr (m < 144) ab[P::jof(m)] = v_addk<SB>(ab[P::jof(m)]);
    else cur[P::jof(m) & 1] = v_addk<(unsigned)((((m / IPS) & 1) - 1) * SB + (P::jof(m) & 1) * (SB / 2))>(ab[P::jof(m) & ~1]);
  };
  float ee, eg, eh[2];
  const float k2 = __builtin_bit_cast(float, spin(__builtin_bit_cast(unsigned, LOG2E * LOG2E)));
  auto epi = [&](auto SU, auto E) {
    constexpr int e = CV(E), w = e / 11, ph = e % 11, p = w >> 2, th = (w >> 1) & 1, r = 2 * (w & 1) + (ph >= 4 && ph != 5);
    if constexpr (ph == 0 || ph == 4) { if constexpr (ph == 4) eh[0] = ee; ee = v_expn(acc[2 * p][th][r]); }
    if constexpr (ph == 1 || ph == 6) eg = v_mul(acc[2 * p][th][r], acc[2 * p + 1][th][r]);
    if constexpr (ph == 2 || ph == 7) ee = v_fmak(ee, k2);
    if constexpr (ph == 3 || ph == 8) ee = v_rcp(ee);
    if constexpr (ph == 5) eh[0] = v_mul(eg, eh[0]);
    if constexpr (ph == 9) eh[1] = v_mul(eg, ee);
    if constexpr (ph == 10) {
      hp[th][2 * (p & 1) + (w & 1)] = v_cvtpk(eh[0], eh[1]);
      if constexpr ((p & 1) && (w & 1)) store16_sc1<(p >> 1) * 1024>(hp[th], sh, l16, hst + (th * KSTOT + 2 * CV(SU)) * 1024);
    }
  };
  auto filler = [&](auto N, auto T) {
    constexpr int n = CV(N), t = CV(T);
    if constexpr (t == 1 && P::rebase(n + 4)) rebase(ic<n + 4>{});
    else if constexpr (!QKV) sfor<3>([&](auto SU) { constexpr int e = P::eidx(CV(SU), n, t); if constexpr (e >= 0) epi(SU, ic<e>{}); });
    GAP();
  };
  auto epiq = [&](auto SU, auto E, auto T) {
    constexpr int su = CV(SU), e = CV(E) - 4, t = CV(T), cg = e / 3, th = e % 3;
    if constexpr (e >= 0 && cg < 5 && th < 2) {
      pk[cg][th][t] = v_cvtpk(acc[cg][th][2 * t], acc[cg][th][2 * t + 1]);
      if constexpr (su == 0 && t == 1) *(LDS u32x2*)(stg + LDS_K + th * 2560 + 512 * cg) = pk[cg][th];
      if constexpr (su < 2 && t == 1 && th == 1) store8_sc1<512 * cg, 7>(pk[cg][1], mk_rsrc(ws + WS_HALO + (bid - 192) * 5120 + su * 2560, 2560), l8, wave);
      GAP();
    }
  };
  GAP(); wait_barrier<P::NPRO - 2>(); GAP();
  keep(tch0);
  STAMP(5);
  __builtin_amdgcn_s_setprio(PL1 - 1);
  sfor<DA - 1>([&](auto N) { aread(N, ic<0>{}); aread(N, ic<1>{}); });
  GAP();
  sfor<NIT>([&](auto N) {
    constexpr int n = CV(N), st = n / IPS, it = n % IPS, j = P::jof(n), sg = P::sof(n), su = st / SPU, m = n + DA - 1;
    constexpr bool more = m < NIT || !QKV;
    if constexpr (QKV && n == NIT - (DA - 1)) { lds_barrier(); GAP(); }
    constexpr bool early = QKV ? st == 2 || st == 5 : st == 2 || st == 3;
    if constexpr (early && n == (QKV ? st * IPS + 19 : P::eat(st - 2, st == 2 ? 153 : 65) - 1)) { wait_vm<P::wait1(n, W1, st + 1)>(); GAP(); }
    if constexpr (it == P::BAR && st < P::NST - 1) { wait_barrier<early ? 63 : P::wait1(n, W1, st + 1)>(); GAP(); }
    if constexpr (!QKV && n == NIT - (DA - 1)) { wait_barrier<cmin(P::wait1(n, W2, 8), P::wait1(n, H2, 0))>(); GAP(); }
    { constexpr int pp = (it + DA - 1) % IPS; if constexpr (pp % (IPS / PL1) == 0) { __builtin_amdgcn_s_setprio(PL1 - 1 - pp / (IPS / PL1)); GAP(); } }
    if constexpr (su == 0 && j == 0 && n > 0) { wait_vm<P::wait1(n, XF, 2 * sg + 1)>(); GAP(); }
    const bf16x8 a = cat8(al[n % DA], ah[n % DA]);
    constexpr bool swp = QKV && su == 1;
    sfor<2>([&](auto T) {
      if constexpr (more) { aread(ic<m>{}, T); GAP(); }
      if constexpr (more && CV(T) == 0 && n % 2 == 0) { __builtin_amdgcn_s_waitcnt(0xC07F | (3 << 8)); GAP(); }
      const bf16x8 b = as_bf(xf[sg][T]);
      if constexpr (sg == 0) mfma_new(acc[j][T], swp ? b : a, swp ? a : b); else mfma_acc(acc[j][T], swp ? b : a, swp ? a : b);
      GAP();
      filler(N, T);
      if constexpr (QKV && n % 90 >= 75) epiq(ic<su>{}, ic<n % 90 - 75>{}, T);
      if constexpr (QKV && n % 90 < 5 && su > 0) epiq(ic<su - 1>{}, ic<n % 90 + 15>{}, T);
    });
    if constexpr (QKV) {
      if constexpr (st == 8 && it < 10) *(LDS u32x2*)(stg + LDS_V + (it % 2) * 2560 + 512 * (it / 2)) = pk[it / 2][it % 2];
      if constexpr (n == 220) t0 = *(volatile LDS u32x4*)tab;
    } else {
      if constexpr (st >= 1 && st <= 3 && it == 24) { tch[0] = ldtab(0); tch[1] = ldtab(1); }
      if constexpr (st >= 1 && st <= 3 && it == 41) { tk[st - 1][0] = tch[0]; tk[st - 1][1] = tch[1]; }
      if constexpr (n == 200 || n == 230 || n == 354) { constexpr int q = n == 200 ? 0 : n == 230 ? 1 : 2; pin(tk[q][0]); pin(tk[q][1]); }
      if constexpr (n == 320) t0 = *(volatile LDS u32x4*)tab;
    }
    constexpr Op o = P::at1(n);
    if constexpr (o.kind != NONE) vmem(ic<o.kind>{}, ic<o.a>{}, ic<o.b>{}, ic<o.kind == CHECK ? P::wait1(n, SAMPLE, 9) : 0>{});
    GAP();
    if constexpr (n % (NIT / 9) == NIT / 9 - 1) STAMP(6 + n / (NIT / 9));
  });
  if constexpr (QKV) {
    mfma_drain();
    sfor<4>([&](auto J) { epiq(ic<2>{}, ic<16 + CV(J) / 2>{}, ic<CV(J) % 2>{}); });
    vmem(ic<SAMPLE>{}, ic<0>{}, ic<0>{}, ic<0>{});
    const int i16 = lane & 15, q4 = lane >> 4, odd = lane & 1;
    const bool cur0 = 4 * q4 + odd <= i16, cur1 = 4 * q4 + 2 + odd <= i16;
    const unsigned s0 = odd ? 0x01000c0cu : 0x0c0c0100u, s1 = odd ? 0x03020c0cu : 0x0c0c0302u;
    const unsigned sel[4] = {cur0 ? 0x0c0c0c0cu : s0, cur1 ? 0x0c0c0c0cu : s1, cur0 ? s0 : 0x0c0c0c0cu, cur1 ? s1 : 0x0c0c0c0cu};
    constexpr float SC = 0.23570226039551584f / LOG2E;
    auto ld2 = [&](unsigned a0, unsigned a1) { const u32x2 u = *(volatile LDS u32x2*)a0, v = *(volatile LDS u32x2*)a1; return as_bf(u32x4{u[0], u[1], v[0], v[1]}); };
    const float cp = t16 == 0 ? -1.0e30f : 0.f;
    unsigned ge[2], lt[2];
    sfor<2>([&](auto I) { ge[I] = (unsigned)((CV(I) - q4) >> 31); lt[I] = ~ge[I]; pin(lt[I]); pin(ge[I]); });
    auto mge = [&](unsigned v, auto C) { constexpr int c = CV(C); if constexpr (c <= 0) return v; else return v & ge[(c - 1) / 4]; };
    auto mlt = [&](unsigned v, auto C) { constexpr int c = CV(C); if constexpr (c <= 0) return 0u; else return v & lt[(c - 1) / 4]; };
    float ca[4], cc[4];
    sfor<4>([&](auto I) { const bool par = odd == (CV(I) & 1); ca[I] = par && cur0 == (CV(I) < 2) ? SC : 0.f; cc[I] = par && cur1 == (CV(I) < 2) ? SC : 0.f; pin(ca[I]); pin(cc[I]); });
    wv3 = t0[0] + 256 + isU * (32 * 1152 - 128); pin(wv3);
    wait_vm<1>();
    sfor<10>([&](auto I) { pin(hl[I]); });
    if (wave == 0) sfor<10>([&](auto I) { *(volatile LDS u32x2*)(stg + (CV(I) < 5 ? LDS_K : LDS_V) - 2560 + 512 * (CV(I) % 5)) = hl[I]; });
    f32x4 sp[8], sc[8];
    float l0[8], l1[8], mx[8];
    bf16x8 pb[8], kb[4][3], vb[2][5];
    sfor<12>([&](auto I) { constexpr int h = CV(I) / 3, g = CV(I) % 3; kb[h][g] = ld2(stg + LDS_K + (g - 1) * 2560 + 512 * h, stg + LDS_K + (g - 1) * 2560 + 512 * h + 512); });
    GAP();
    auto pf = [&](auto I) { constexpr Op o = P::w2op(CV(I)); GAP(); vmem(ic<o.kind>{}, ic<o.a>{}, ic<o.b>{}, ic<0>{}); GAP(); };
    auto ph = [&](auto I) { GAP(); vmem(ic<H2>{}, ic<3 + CV(I)>{}, ic<0>{}, ic<0>{}); vmem(ic<H2>{}, ic<3 + CV(I)>{}, ic<1>{}, ic<0>{}); GAP(); };
    pf(ic<0>{});
    sfor<4>([&](auto HH) {
      constexpr int hd = CV(HH);
      sfor<2>([&](auto TH) {
        const bf16x8 qb = as_bf(u32x4{mge(pk[hd][TH][0], ic<2 * hd>{}), mge(pk[hd][TH][1], ic<2 * hd - 2>{}), mlt(pk[hd + 1][TH][0], ic<2 * hd + 2>{}), mlt(pk[hd + 1][TH][1], ic<2 * hd>{})});
        sp[2 * hd + TH] = CV(TH) ? mfma(kb[hd][1], qb, f32x4{}) : mfma(kb[hd][0], qb, f32x4{cp, cp, cp, cp});
        sc[2 * hd + TH] = mfma(kb[hd][1 + TH], qb, f32x4{});
      });
      pf(ic<1 + hd>{});
    });
    sfor<10>([&](auto I) { constexpr int th = CV(I) / 5, j = CV(I) % 5; vb[th][j] = ld2(stg + LDS_V + (th - 1) * 2560 + 512 * j, stg + LDS_V + th * 2560 + 512 * j); });
    GAP();
    lds_barrier();
    vmem(ic<CHECK>{}, ic<0>{}, ic<0>{}, ic<5>{});
    sfor<8>([&](auto E) {
      l0[E] = fma4(sc[E][0], sc[E][1], sp[E][0], sp[E][1], ca);
      l1[E] = fma4(sc[E][2], sc[E][3], sp[E][2], sp[E][3], cc);
      mx[E] = vmax(l0[E], l1[E]); pin(mx[E]); pin(l0[E]); pin(l1[E]);
      if constexpr (CV(E) % 3 == 0) pf(ic<5 + CV(E) / 3>{});
    });
    ph(ic<0>{});
    xq8<true>(mx, 0.f);
    ph(ic<1>{});
    sfor<8>([&](auto E) { l0[E] = __builtin_amdgcn_exp2f(l0[E] - mx[E]); l1[E] = __builtin_amdgcn_exp2f(l1[E] - mx[E]); mx[E] = l0[E] + l1[E]; });
    ph(ic<2>{});
    xq8<false>(mx, 1.f / LOG2E);
    ph(ic<3>{});
    sfor<8>([&](auto E) {
      const unsigned pp = cvtpk(l0[E] * mx[E], l1[E] * mx[E]);
      pb[E] = as_bf(u32x4{__builtin_amdgcn_perm(pp, pp, sel[0]), __builtin_amdgcn_perm(pp, pp, sel[1]),
                          __builtin_amdgcn_perm(pp, pp, sel[2]), __builtin_amdgcn_perm(pp, pp, sel[3])});
    });
    ph(ic<4>{});
    sfor<8>([&](auto E) { sp[E] = mfma(vb[CV(E) & 1][CV(E) >> 1], pb[E], f32x4{}); sc[E] = mfma(vb[CV(E) & 1][(CV(E) >> 1) + 1], pb[E], f32x4{}); });
    ph(ic<5>{});
    sfor<NT>([&](auto I) { rebase(ic<NIT + CV(I)>{}); });
    wait_vm<12>();
    sfor<2>([&](auto TH) {
      unsigned ow[12];
      sfor<12>([&](auto E) {
        constexpr int j = CV(E) >> 1, e = CV(E) & 1;
        unsigned a = 0u, b = 0u;
        if constexpr (j < 4) a = mge(cvtpk(sp[2 * j + TH][2 * e], sp[2 * j + TH][2 * e + 1]), ic<2 * j - 2 * e>{});
        if constexpr (j > 0 && j < 5) b = mlt(cvtpk(sc[2 * j - 2 + TH][2 * e], sc[2 * j - 2 + TH][2 * e + 1]), ic<2 * j - 2 * e>{});
        ow[E] = a | b;
      });
      sfor<3>([&](auto M) { hf[M][TH] = u32x4{ow[4 * M], ow[4 * M + 1], ow[4 * M + 2], ow[4 * M + 3]}; pin(hf[M][TH]); store16_sc1<1024 * CV(M)>(hf[M][TH], sh, l16, ho + TH * KSTOT * 1024); });
    });
    STAMP(16);
    GAP(); lds_barrier(); GAP();
    sfor<DA - 1>([&](auto N) { aread(ic<NIT + CV(N)>{}, ic<0>{}); aread(ic<NIT + CV(N)>{}, ic<1>{}); });
    __builtin_amdgcn_s_setprio(3);
    GAP();
    STAMP(17);
  }
  const unsigned fs0 = spin(FL_S + (tile * 8 + wave) * 16 * FS);
  unsigned fo, f;
  sfor<NIT2>([&](auto M) {
    constexpr int m = CV(M), n = NIT + m, st = P::st2(m), i = P::ti2(m);
    if constexpr (P::bar2(m) >= 0) { wait_barrier<P::wait2(m, W2, P::bar2(m))>(); GAP(); }
    {
      constexpr int lo = [] { int k = m; while (k >= 0 && P::bar2(k) < 0) --k; return k < 0 ? -3 : k; }(), hi = [] { int k = m + 1; while (k < NIT2 && P::bar2(k) < 0) ++k; return k; }();
      constexpr int len = (hi - lo + 3) / 4;
      if constexpr ((m - lo) % len == 0) { __builtin_amdgcn_s_setprio(3 - (m - lo) / len); GAP(); }
    }
    if constexpr (P::first2(m) && st > 0) { wait_vm<P::wait2(m, H2, st)>(); GAP(); }
    const bf16x8 a = cat8(al[n % DA], ah[n % DA]);
    sfor<2>([&](auto T) {
      if constexpr (m + DA - 1 < NIT2) { aread(ic<n + DA - 1>{}, T); GAP(); }
      if constexpr (m + DA - 1 < NIT2 && CV(T) == 0 && n % 2 == 0) { __builtin_amdgcn_s_waitcnt(0xC07F | (3 << 8)); GAP(); }
      if constexpr (st == 0) mfma_new(acc2[i][T], a, as_bf(hf[st][T])); else mfma_acc(acc2[i][T], a, as_bf(hf[st][T]));
      GAP();
      filler(ic<n>{}, T);
      constexpr int c = m - P::M2 - P::LT - 1;
      if constexpr (c >= 0 && c / P::LT < NT - 1 && c % P::LT < 4 && CV(T) == 0) {
        constexpr int tl = P::gt(c / P::LT), e = c % P::LT;
        gp[e] = v_cvtpk(acc2[tl][e >> 1][2 * (e & 1)], acc2[tl][e >> 1][2 * (e & 1) + 1]);
        if constexpr (e == 3 && tl == NE) publish(tag, fs0 + b2 * FS, sF);
        if constexpr (e == 3) store16_sc1<0>(gp, sS, l16, sl + tl * 1024);
        GAP();
      }
    });
    if constexpr (HT && !QKV && m == 118) pin(tch[0]);
    if constexpr (m == NIT2 - 30) { fo = fs0 + ((16 * (b2 + 7) - l16) & 0x70) * (FS / 16) + ((l16 & 0x70) < 16 * NE ? 0 : 8 * FS); pin(fo); }
    if constexpr (m == NIT2 - 1 - SE) { load4_sc1(f, fo, sF); GAP(); }
    constexpr Op o = P::at2(m);
    if constexpr (o.kind != NONE) { vmem(ic<o.kind>{}, ic<o.a>{}, ic<o.b>{}, ic<o.kind == CHECK ? P::wait2(m, SAMPLE, 9) : 0>{}); GAP(); }
    if constexpr (m % (3 * NT) == 3 * NT - 1) STAMP(18 + m / (3 * NT));
  });
  if (!QKV && wave == 0 && bid == 0) {
    unsigned g[4];
    sfor<4>([&](auto I) { load4_sc1(g[I], FL_X + 64 * FS * I + 8 * l16, sF); });
    sfor<4>([&](auto I) { flags_wait<0, 2>(g[I], FL_X + 64 * FS * I + 8 * l16, tag, ~0ull, sF); });
    flag_only(tag, FL_EPOCH, sF);
  }
  publish(tag, fs0 + (8 + b2) * FS, sF);
  STAMP(26);
  mfma_drain();
  pin(acc2[7][0]); pin(acc2[7][1]);
  sfor<QKV ? 4 : 2>([&](auto I) { pin(xres[I]); });
  u32x4 pv[7], pw[8];
  const rsrc_t sQ = mk_rsrc(ws + WS_SLAB + 8192 + xt * 1024, QKV && !guest ? SLAB_B - 12288 + 1024 : 0u);
  if constexpr (QKV) load16_sc1<0>(pw[7], l16, sl, sQ);
  lane = lane_now();
  const unsigned one = (lane >> 4) == ((lane >> 2) & 3) ? (lane & 1 ? 0x3f800000u : 0x3f80u) : 0u, e0 = lane & 2 ? 0u : one, e1 = lane & 2 ? one : 0u;
  const bf16x8 elo = as_bf(u32x4{e0, e1, 0u, 0u}), ehi = as_bf(u32x4{0u, 0u, e0, e1});
  const bf16x8 xb = as_bf(u32x4{xres[0][0], xres[0][1], xres[1][0], xres[1][1]});
  f32x4 o0 = mfma(elo, xb, acc2[7][0]), o1 = mfma(ehi, xb, acc2[7][1]);
  const unsigned xr = smem + LDS_X + wave * 2048;
  unsigned tw = xr + (lane & 15) * 48 + (lane >> 4) * 8, trd = xr + (lane >> 1) * 48 + (lane & 1) * 16, tst = (lane >> 1) * 1152 + (lane & 1) * 16;
  pin(tw); pin(trd); pin(tst); pin(o0); pin(o1);
  gather7<QKV>(pv, pw, f, fo, l16, l16 + 4096, tag, b2 + 7, spin((tile * 64 + wave) * SLAB_W), sS, sQ, sF);
  STAMP(28);
  const rsrc_t sO = mk_rsrc(A.out, S * D * 2);
  auto finish = [&](auto N, const u32x4* p, unsigned so) {
    sfor<CV(N)>([&](auto J) { o0 = mfma(elo, as_bf(p[J]), o0); o1 = mfma(ehi, as_bf(p[J]), o1); });
    *(LDS u32x2*)tw = u32x2{cvtpk(o0[0], o0[1]), cvtpk(o0[2], o0[3])}; *(LDS u32x2*)(tw + 768) = u32x2{cvtpk(o1[0], o1[1]), cvtpk(o1[2], o1[3])};
    asm volatile("" ::: "memory");
    store16(*(LDS u32x4*)trd, sO, tst, so);
  };
  if constexpr (QKV) pin(pw[7]);
  finish(ic<7>{}, pv, oso + 32 * b2);
  if (QKV && !guest) {
    const bf16x8 x8 = as_bf(u32x4{xres[2][0], xres[2][1], xres[3][0], xres[3][1]});
    o0 = mfma(elo, x8, f32x4{}); o1 = mfma(ehi, x8, f32x4{});
    finish(ic<8>{}, pw, oso + 256 + 32 * xt);
  }
  STAMP(29);
  if constexpr (QKV) asm volatile(".Lpsk_end_a:"); else asm volatile(".Lpsk_end_b:");
}

__global__ __launch_bounds__(512, 2) void fused(Args A) {
  __shared__ __attribute__((aligned(256))) char smem_[LDS_B];
  const unsigned smem = (unsigned)(size_t)(LDS char*)smem_;
  const int wave = rfl(threadIdx.x >> 6), lane = threadIdx.x & 63, bid = blockIdx.x, xcd = bid & 7, r = bid >> 3;
  char* ws = A.ws;
  STAMP(0);
  u32x4 v; unsigned v9, ep, tc;
  ln_load(v, v9, A.x, bid * 8 + wave, lane);
  load4_sc1(ep, FL_EPOCH, mk_rsrc(ws + WS_FL, FL_B));
  asm volatile("s_barrier" ::: "memory");
  const bool qkv = r >= 24;
  const int rt = qkv ? r - 24 : r / 3, col0 = qkv ? 72 * xcd : (xcd * 3 + r % 3) * 192;
  {
    u64 pc; unsigned na, nb;
    asm volatile("s_getpc_b64 %0\n.Lpsk_begin:\n\ts_mov_b32 %1, .Lpsk_end_a-.Lpsk_begin\n\ts_mov_b32 %2, .Lpsk_end_b-.Lpsk_begin" : "=s"(pc), "=s"(na), "=s"(nb));
    const int w3 = wave & 3;
    const unsigned id = (w3 - 1) * 64 + lane;
    unsigned vo;
    if (qkv) { const unsigned k = (wave - 1) * 64 + lane; vo = pick<unsigned>(k < 432, (rt * 216 + (k >> 1)) * 1152 + (k & 1) * 128, 0x80000000u); }
    else vo = pick<unsigned>(id < 72, (rt * 72 + id) * (F * 2), pick<unsigned>(id < 108, (rt * 36 + id - 72) * (F * 2) + 128, 0x80000000u));
    vo += col0 * 2;
    const bool cw = wave == 4 && r < 16;
    if (cw) vo = (r * 64 + lane) * 128;
    const unsigned nby = cw ? (na > nb ? na : nb) : qkv ? (wave ? D * 3 * D * 2 : 0) : w3 ? D * F * 2 : 0;
    load4(tc, vo, mk_rsrc(cw ? (const char*)pc : qkv ? A.wqkv : wave < 4 ? A.wg : A.wu, nby));
  }
  wait_vm<1>();
  pin(v); pin(v9); pin(ep);
  STAMP(1);
  const unsigned tag = rfl(ep) * 1664525u + 1013904223u;
  ln_row(v, v9, smem, wave, lane);
  lds_barrier();
  STAMP(2);
  if (wave == 0) { ln_publish(ws, smem, tag, bid, lane); STAMP(3); }
  asm volatile("s_barrier" ::: "memory");
  if (!qkv) body<false>(A, smem, tag, rt, col0, wave, lane, tc);
  else body<true>(A, smem, tag, rt, col0, wave, lane, tc);
}

}
extern "C" void kernel_launch(void* const* d_in, const int* in_sizes, int n_in, void* d_out, int out_size, void* d_ws, size_t ws_size,
                              hipStream_t stream) {
  psk::Args A;
  A.x = (const char*)d_in[0]; A.wqkv = (const char*)d_in[1]; A.wo = (const char*)d_in[2]; A.wg = (const char*)d_in[4];
  A.wu = (const char*)d_in[5]; A.wd = (const char*)d_in[6]; A.ws = (char*)d_ws; A.out = (char*)d_out;
  psk::fused<<<256, 512, 0, stream>>>(A);
}
```

```cpp
#include <hip/hip_runtime.h>
#include <utility>
#pragma clang diagnostic ignored "-Wint-to-pointer-cast"

typedef float    f32x4  __attribute__((ext_vector_type(4)));
typedef float    f32x2  __attribute__((ext_vector_type(2)));
typedef __bf16   bf16x8 __attribute__((ext_vector_type(8)));
typedef __bf16   bf16x4 __attribute__((ext_vector_type(4)));
typedef __bf16   bf16x2 __attribute__((ext_vector_type(2)));
typedef unsigned u32x4  __attribute__((ext_vector_type(4)));
typedef unsigned u32x2  __attribute__((ext_vector_type(2)));
typedef unsigned long long u64;
typedef __amdgpu_buffer_rsrc_t rsrc_t;
#define LDS __attribute__((address_space(3)))
#define DI __device__ __forceinline__
#define GAP() __builtin_amdgcn_sched_barrier(0)
#define CV(X) (decltype(X)::value)

namespace psk {

constexpr int S = 2048, D = 576, F = 4608;
constexpr int KSTOT = 168;
constexpr unsigned HBUF_B = 128u * KSTOT * 1024u;
constexpr unsigned SLAB_W = 12288, SLAB_B = 32u * 8u * 8u * SLAB_W;
constexpr unsigned XN_B = S * D * 2u, HALO_B = 64u * 5120u;
constexpr unsigned FS = 128;
constexpr unsigned FL_EPOCH = 0, FL_X = FS, FL_H = FL_X + 256 * FS, FL_S = FL_H + 72 * 96 * FS, FL_B = FL_S + 256 * 16 * FS;
constexpr size_t WS_H = 0, WS_SLAB = WS_H + HBUF_B, WS_XN = WS_SLAB + SLAB_B, WS_HALO = WS_XN + XN_B, WS_FL = WS_HALO + HALO_B;
constexpr unsigned SPIN_CAP = 1u << 18;

template <int N> using ic = std::integral_constant<int, N>;
template <class Fn, int... Is> DI void sfor_impl(Fn&& f, std::integer_sequence<int, Is...>) { (f(ic<Is>{}), ...); }
template <int N, class Fn> DI void sfor(Fn&& f) { sfor_impl(f, std::make_integer_sequence<int, N>{}); }

DI unsigned rfl(unsigned x) { return __builtin_amdgcn_readfirstlane(x); }
DI rsrc_t mk_rsrc(const void* p, unsigned bytes) { return __builtin_amdgcn_make_buffer_rsrc((void*)p, (short)0, (int)bytes, 0x00020000); }
DI unsigned cvtpk(float lo, float hi) { f32x2 v = {lo, hi}; return __builtin_bit_cast(unsigned, __builtin_convertvector(v, bf16x2)); }
DI float rbf(float x) { return __builtin_bit_cast(float, cvtpk(x, x) << 16); }
DI float bflo(unsigned v) { return __builtin_bit_cast(float, v << 16); }
DI float bfhi(unsigned v) { return __builtin_bit_cast(float, v & 0xffff0000u); }
DI bf16x4 trread(unsigned addr, int off) { return __builtin_amdgcn_ds_read_tr16_b64_v4bf16((LDS bf16x4*)(addr + off)); }
DI bf16x8 cat8(bf16x4 a, bf16x4 b) { return __builtin_shufflevector(a, b, 0, 1, 2, 3, 4, 5, 6, 7); }
DI bf16x8 as_bf(u32x4 v) { return __builtin_bit_cast(bf16x8, v); }
DI f32x4 mfma(bf16x8 a, bf16x8 b, f32x4 c) { return __builtin_amdgcn_mfma_f32_16x16x32_bf16(a, b, c, 0, 0, 0); }
DI void mfma_acc(f32x4& c, bf16x8 a, bf16x8 b) { asm volatile("v_mfma_f32_16x16x32_bf16 %0, %1, %2, %0" : "+v"(c) : "v"(a), "v"(b)); }
DI void mfma_new(f32x4& c, bf16x8 a, bf16x8 b) { asm volatile("v_mfma_f32_16x16x32_bf16 %0, %1, %2, 0" : "=v"(c) : "v"(a), "v"(b)); }
DI void mfma_drain() { asm volatile("s_nop 7\n\ts_nop 1"); GAP(); }
template <class T> DI void pin(T& x) { asm volatile("" : "+v"(x)); }
template <class T> DI void keep(T x) { asm volatile("" ::"v"(x)); }
DI unsigned lane_now() { unsigned l; asm volatile("v_mbcnt_lo_u32_b32 %0, -1, 0\n\tv_mbcnt_hi_u32_b32 %0, -1, %0" : "=v"(l)); return l; }
DI unsigned spin(unsigned x) { asm volatile("" : "+s"(x)); return x; }
template <int N> DI void wait_vm() { if constexpr (N < 63) asm volatile("s_waitcnt vmcnt(%0)" ::"i"(N) : "memory"); }
template <int N> DI void wait_barrier() { asm volatile("s_waitcnt vmcnt(%0) lgkmcnt(0)\n\ts_barrier" ::"i"(N) : "memory"); }
DI void lds_barrier() { asm volatile("s_waitcnt lgkmcnt(0)\n\ts_barrier" ::: "memory"); }
DI float v_mul(float a, float b) { float r; asm volatile("v_mul_f32 %0, %1, %2" : "=v"(r) : "v"(a), "v"(b)); return r; }
DI float v_expn(float a) { float r; asm volatile("v_exp_f32_e64 %0, -%1" : "=v"(r) : "v"(a)); return r; }
DI float v_fmak(float a, float k) { float r; asm volatile("v_fma_f32 %0, %1, %2, %2" : "=v"(r) : "v"(a), "s"(k)); return r; }
DI unsigned v_xors(unsigned a, unsigned k) { unsigned r; asm volatile("v_xor_b32 %0, %2, %1" : "=v"(r) : "v"(a), "s"(k)); return r; }
template <unsigned K> DI unsigned v_addk(unsigned a) { unsigned r; asm volatile("v_add_u32 %0, %2, %1" : "=v"(r) : "v"(a), "i"(K)); return r; }
DI float v_rcp(float a) { float r; asm volatile("v_rcp_f32 %0, %1" : "=v"(r) : "v"(a)); return r; }
DI unsigned v_cvtpk(float a, float b) { unsigned r; asm volatile("v_cvt_pk_bf16_f32 %0, %1, %2" : "=v"(r) : "v"(a), "v"(b)); return r; }

template <int LOFF, unsigned GOFF> DI void dma16(unsigned m0base, unsigned voff, rsrc_t srd) {
  unsigned t;
  asm volatile("s_add_u32 m0, %1, %2\n\ts_mov_b32 %0, %3\n\tbuffer_load_dwordx4 %4, %5, %0 offen lds"
               : "=&s"(t) : "s"(m0base), "i"(LOFF), "i"(GOFF), "v"(voff), "s"(srd) : "scc", "memory");
}
template <int GOFF> DI void load16_sc1(u32x4& d, unsigned voff, unsigned sbase, rsrc_t srd) {
  unsigned t;
  asm volatile("s_add_u32 %1, %3, %4\n\tbuffer_load_dwordx4 %0, %2, %5, %1 offen sc1" : "=v"(d), "=&s"(t) : "v"(voff), "s"(sbase), "i"(GOFF), "s"(srd) : "scc");
}
template <int OFF> DI void store16_sc1(u32x4 d, rsrc_t srd, unsigned voff, unsigned soff) {
  asm volatile("buffer_store_dwordx4 %0, %1, %2, %3 offen offset:%4 sc1\n\ts_nop 1" ::"v"(d), "v"(voff), "s"(srd), "s"(soff), "i"(OFF) : "memory");
}
DI void load16(u32x4& d, unsigned voff, rsrc_t srd) { asm volatile("buffer_load_dwordx4 %0, %1, %2, 0 offen" : "=v"(d) : "v"(voff), "s"(srd)); }
DI void load4(unsigned& d, unsigned voff, rsrc_t srd) { asm volatile("buffer_load_dword %0, %1, %2, 0 offen" : "=v"(d) : "v"(voff), "s"(srd)); }
template <unsigned SOFF> DI void load4s(unsigned& d, unsigned voff, rsrc_t srd) {
  unsigned t;
  asm volatile("s_mov_b32 %1, %4\n\tbuffer_load_dword %0, %2, %3, %1 offen" : "=v"(d), "=&s"(t) : "v"(voff), "s"(srd), "i"(SOFF));
}
DI void load4_sc1(unsigned& d, unsigned voff, rsrc_t srd) { asm volatile("buffer_load_dword %0, %1, %2, 0 offen sc1" : "=v"(d) : "v"(voff), "s"(srd)); }
template <int N, int SLEEP = 2> DI void flags_wait(unsigned& f, unsigned voff, unsigned tag, u64 mask, rsrc_t srd) {
  unsigned cnt;
  asm volatile("s_waitcnt vmcnt(%6)\n\ts_mov_b32 %1, %7\n"
               "1:\n\tv_cmp_ne_u32 vcc, %3, %0\n\ts_nop 1\n\ts_and_b64 vcc, vcc, %4\n\ts_cbranch_scc0 2f\n\t"
               "s_sleep %8\n\tbuffer_load_dword %0, %2, %5, 0 offen sc1\n\ts_waitcnt vmcnt(0)\n\ts_sub_u32 %1, %1, 1\n\ts_cbranch_scc0 1b\n"
               "2:"
               : "+v"(f), "=&s"(cnt) : "v"(voff), "s"(tag), "s"(mask), "s"(srd), "i"(N), "i"(SPIN_CAP), "i"(SLEEP) : "vcc", "scc", "memory");
}
DI void publish(unsigned tag, unsigned soff, rsrc_t srd) {
  unsigned t;
  asm volatile("v_mov_b32 %0, %1\n\ts_waitcnt vmcnt(0)\n\ts_mov_b64 exec, 1\n\tbuffer_store_dword %0, off, %3, %2 sc1\n\ts_mov_b64 exec, -1"
               : "=&v"(t) : "s"(tag), "s"(soff), "s"(srd) : "memory");
}

template <bool TWO> DI void gather7(u32x4 (&p)[7], u32x4 (&q)[8], unsigned& f, unsigned fo, unsigned v0, unsigned v1, unsigned tag, unsigned src0, unsigned s0, rsrc_t sp, rsrc_t sq, rsrc_t sf) {
  unsigned pend, rdy, t, cnt;
  if constexpr (TWO) {
#define X(R) "buffer_load_dwordx4 " R ", %20, %26, %17 offen sc1\n"
  asm volatile("s_mov_b32 %15, 0x7f\n\ts_mov_b32 %18, %28\n"
               "1:\n\tv_cmp_eq_u32 vcc, %22, %14\n\ts_nop 1\n\ts_and_b32 %16, vcc_lo, %15\n\ts_andn2_b32 %15, %15, %16\n\t"
               "s_bitcmp1_b32 %16, 0\n\ts_cbranch_scc0 3f\n\ts_sub_u32 %17, %23, 0\n\ts_and_b32 %17, %17, 7\n\ts_mul_i32 %17, %17, %29\n\ts_add_u32 %17, %17, %24\n\t"
               "buffer_load_dwordx4 %0, %20, %25, %17 offen offset:0 sc1\n\t" X("%7") "3:\n\t"
               "s_bitcmp1_b32 %16, 1\n\ts_cbranch_scc0 3f\n\ts_sub_u32 %17, %23, 1\n\ts_and_b32 %17, %17, 7\n\ts_mul_i32 %17, %17, %29\n\ts_add_u32 %17, %17, %24\n\t"
               "buffer_load_dwordx4 %1, %20, %25, %17 offen offset:1024 sc1\n\t" X("%8") "3:\n\t"
               "s_bitcmp1_b32 %16, 2\n\ts_cbranch_scc0 3f\n\ts_sub_u32 %17, %23, 2\n\ts_and_b32 %17, %17, 7\n\ts_mul_i32 %17, %17, %29\n\ts_add_u32 %17, %17, %24\n\t"
               "buffer_load_dwordx4 %2, %20, %25, %17 offen offset:2048 sc1\n\t" X("%9") "3:\n\t"
               "s_bitcmp1_b32 %16, 3\n\ts_cbranch_scc0 3f\n\ts_sub_u32 %17, %23, 3\n\ts_and_b32 %17, %17, 7\n\ts_mul_i32 %17, %17, %29\n\ts_add_u32 %17, %17, %24\n\t"
               "buffer_load_dwordx4 %3, %20, %25, %17 offen offset:3072 sc1\n\t" X("%10") "3:\n\t"
               "s_bitcmp1_b32 %16, 4\n\ts_cbranch_scc0 3f\n\ts_sub_u32 %17, %23, 4\n\ts_and_b32 %17, %17, 7\n\ts_mul_i32 %17, %17, %29\n\ts_add_u32 %17, %17, %24\n\t"
               "buffer_load_dwordx4 %4, %21, %25, %17 offen offset:0 sc1\n\t" X("%11") "3:\n\t"
               "s_bitcmp1_b32 %16, 5\n\ts_cbranch_scc0 3f\n\ts_sub_u32 %17, %23, 5\n\ts_and_b32 %17, %17, 7\n\ts_mul_i32 %17, %17, %29\n\ts_add_u32 %17, %17, %24\n\t"
               "buffer_load_dwordx4 %5, %21, %25, %17 offen offset:1024 sc1\n\t" X("%12") "3:\n\t"
               "s_bitcmp1_b32 %16, 6\n\ts_cbranch_scc0 3f\n\ts_sub_u32 %17, %23, 6\n\ts_and_b32 %17, %17, 7\n\ts_mul_i32 %17, %17, %29\n\ts_add_u32 %17, %17, %24\n\t"
               "buffer_load_dwordx4 %6, %21, %25, %17 offen offset:2048 sc1\n\t" X("%13") "3:\n\t"
               "s_cmp_eq_u32 %15, 0\n\ts_cbranch_scc1 2f\n\ts_sleep 1\n\t"
               "s_mov_b32 exec_lo, %15\n\ts_mov_b32 exec_hi, 0\n\tbuffer_load_dword %14, %19, %27, 0 offen sc1\n\ts_mov_b64 exec, -1\n\t"
               "s_waitcnt vmcnt(0)\n\ts_sub_u32 %18, %18, 1\n\ts_cbranch_scc0 1b\n2:\n\ts_waitcnt vmcnt(0)"
               : "=&v"(p[0]), "=&v"(p[1]), "=&v"(p[2]), "=&v"(p[3]), "=&v"(p[4]), "=&v"(p[5]), "=&v"(p[6]), "=&v"(q[0]), "=&v"(q[1]), "=&v"(q[2]), "=&v"(q[3]), "=&v"(q[4]), "=&v"(q[5]), "=&v"(q[6]), "+v"(f), "=&s"(pend), "=&s"(rdy), "=&s"(t), "=&s"(cnt)
               : "v"(fo), "v"(v0), "v"(v1), "s"(tag), "s"(src0), "s"(s0), "s"(sp), "s"(sq), "s"(sf), "i"(SPIN_CAP), "i"(8 * SLAB_W) : "vcc", "scc", "memory");
#undef X
  } else {
#define X(R) "\n"
  asm volatile("s_mov_b32 %15, 0x7f\n\ts_mov_b32 %18, %28\n"
               "1:\n\tv_cmp_eq_u32 vcc, %22, %14\n\ts_nop 1\n\ts_and_b32 %16, vcc_lo, %15\n\ts_andn2_b32 %15, %15, %16\n\t"
               "s_bitcmp1_b32 %16, 0\n\ts_cbranch_scc0 3f\n\ts_sub_u32 %17, %23, 0\n\ts_and_b32 %17, %17, 7\n\ts_mul_i32 %17, %17, %29\n\ts_add_u32 %17, %17, %24\n\t"
               "buffer_load_dwordx4 %0, %20, %25, %17 offen offset:0 sc1\n\t" X("%7") "3:\n\t"
               "s_bitcmp1_b32 %16, 1\n\ts_cbranch_scc0 3f\n\ts_sub_u32 %17, %23, 1\n\ts_and_b32 %17, %17, 7\n\ts_mul_i32 %17, %17, %29\n\ts_add_u32 %17, %17, %24\n\t"
               "buffer_load_dwordx4 %1, %20, %25, %17 offen offset:1024 sc1\n\t" X("%8") "3:\n\t"
               "s_bitcmp1_b32 %16, 2\n\ts_cbranch_scc0 3f\n\ts_sub_u32 %17, %23, 2\n\ts_and_b32 %17, %17, 7\n\ts_mul_i32 %17, %17, %29\n\ts_add_u32 %17, %17, %24\n\t"
               "buffer_load_dwordx4 %2, %20, %25, %17 offen offset:2048 sc1\n\t" X("%9") "3:\n\t"
               "s_bitcmp1_b32 %16, 3\n\ts_cbranch_scc0 3f\n\ts_sub_u32 %17, %23, 3\n\ts_and_b32 %17, %17, 7\n\ts_mul_i32 %17, %17, %29\n\ts_add_u32 %17, %17, %24\n\t"
               "buffer_load_dwordx4 %3, %20, %25, %17 offen offset:3072 sc1\n\t" X("%10") "3:\n\t"
               "s_bitcmp1_b32 %16, 4\n\ts_cbranch_scc0 3f\n\ts_sub_u32 %17, %23, 4\n\ts_and_b32 %17, %17, 7\n\ts_mul_i32 %17, %17, %29\n\ts_add_u32 %17, %17, %24\n\t"
               "buffer_load_dwordx4 %4, %21, %25, %17 offen offset:0 sc1\n\t" X("%11") "3:\n\t"
               "s_bitcmp1_b32 %16, 5\n\ts_cbranch_scc0 3f\n\ts_sub_u32 %17, %23, 5\n\ts_and_b32 %17, %17, 7\n\ts_mul_i32 %17, %17, %29\n\ts_add_u32 %17, %17, %24\n\t"
               "buffer_load_dwordx4 %5, %21, %25, %17 offen offset:1024 sc1\n\t" X("%12") "3:\n\t"
               "s_bitcmp1_b32 %16, 6\n\ts_cbranch_scc0 3f\n\ts_sub_u32 %17, %23, 6\n\ts_and_b32 %17, %17, 7\n\ts_mul_i32 %17, %17, %29\n\ts_add_u32 %17, %17, %24\n\t"
               "buffer_load_dwordx4 %6, %21, %25, %17 offen offset:2048 sc1\n\t" X("%13") "3:\n\t"
               "s_cmp_eq_u32 %15, 0\n\ts_cbranch_scc1 2f\n\ts_sleep 1\n\t"
               "s_mov_b32 exec_lo, %15\n\ts_mov_b32 exec_hi, 0\n\tbuffer_load_dword %14, %19, %27, 0 offen sc1\n\ts_mov_b64 exec, -1\n\t"
               "s_waitcnt vmcnt(0)\n\ts_sub_u32 %18, %18, 1\n\ts_cbranch_scc0 1b\n2:\n\ts_waitcnt vmcnt(0)"
               : "=&v"(p[0]), "=&v"(p[1]), "=&v"(p[2]), "=&v"(p[3]), "=&v"(p[4]), "=&v"(p[5]), "=&v"(p[6]), "=&v"(q[0]), "=&v"(q[1]), "=&v"(q[2]), "=&v"(q[3]), "=&v"(q[4]), "=&v"(q[5]), "=&v"(q[6]), "+v"(f), "=&s"(pend), "=&s"(rdy), "=&s"(t), "=&s"(cnt)
               : "v"(fo), "v"(v0), "v"(v1), "s"(tag), "s"(src0), "s"(s0), "s"(sp), "s"(sq), "s"(sf), "i"(SPIN_CAP), "i"(8 * SLAB_W) : "vcc", "scc", "memory");
#undef X
  }
}
template <int OFF, int W> DI void store8_sc1(u32x2 d, rsrc_t srd, unsigned voff, unsigned wave) {
  asm volatile("s_cmp_lg_u32 %4, %5\n\ts_cbranch_scc1 1f\n\tbuffer_store_dwordx2 %0, %1, %2, 0 offen offset:%3 sc1\n1:" ::"v"(d), "v"(voff), "s"(srd), "i"(OFF), "s"(wave), "i"(W) : "scc", "memory");
}
DI void load8x5_sc1(u32x2* d, rsrc_t srd, unsigned voff, unsigned wave) {
  asm volatile("s_cmp_lg_u32 %7, 0\n\ts_cbranch_scc1 1f\n\tbuffer_load_dwordx2 %0, %5, %6, 0 offen sc1\n\tbuffer_load_dwordx2 %1, %5, %6, 0 offen offset:512 sc1\n\t"
               "buffer_load_dwordx2 %2, %5, %6, 0 offen offset:1024 sc1\n\tbuffer_load_dwordx2 %3, %5, %6, 0 offen offset:1536 sc1\n\tbuffer_load_dwordx2 %4, %5, %6, 0 offen offset:2048 sc1\n1:"
               : "=&v"(d[0]), "=&v"(d[1]), "=&v"(d[2]), "=&v"(d[3]), "=&v"(d[4]) : "v"(voff), "s"(srd), "s"(wave) : "scc");
}
DI void halo_park(const u32x2* d, unsigned ak, unsigned av, unsigned wave) {
  asm volatile("s_cmp_lg_u32 %12, 0\n\ts_cbranch_scc1 1f\n\ts_waitcnt vmcnt(0)\n\t"
               "ds_write_b64 %10, %0\n\tds_write_b64 %10, %1 offset:512\n\tds_write_b64 %10, %2 offset:1024\n\tds_write_b64 %10, %3 offset:1536\n\tds_write_b64 %10, %4 offset:2048\n\t"
               "ds_write_b64 %11, %5\n\tds_write_b64 %11, %6 offset:512\n\tds_write_b64 %11, %7 offset:1024\n\tds_write_b64 %11, %8 offset:1536\n\tds_write_b64 %11, %9 offset:2048\n1:"
               ::"v"(d[0]), "v"(d[1]), "v"(d[2]), "v"(d[3]), "v"(d[4]), "v"(d[5]), "v"(d[6]), "v"(d[7]), "v"(d[8]), "v"(d[9]), "v"(ak), "v"(av), "s"(wave) : "scc", "memory");
}
DI void publish_if(unsigned tag, unsigned soff, rsrc_t srd, unsigned off) {
  unsigned t;
  asm volatile("s_cmp_lg_u32 %4, 0\n\ts_cbranch_scc1 1f\n\tv_mov_b32 %0, %1\n\ts_waitcnt vmcnt(0)\n\ts_mov_b64 exec, 1\n\tbuffer_store_dword %0, off, %3, %2 sc1\n\ts_mov_b64 exec, -1\n1:"
               : "=&v"(t) : "s"(tag), "s"(soff), "s"(srd), "s"(off) : "scc", "memory");
}
DI void flag_only(unsigned tag, unsigned soff, rsrc_t srd) {
  unsigned t;
  asm volatile("v_mov_b32 %0, %1\n\ts_mov_b64 exec, 1\n\tbuffer_store_dword %0, off, %3, %2 sc1\n\ts_mov_b64 exec, -1" : "=&v"(t) : "s"(tag), "s"(soff), "s"(srd) : "memory");
}
DI void load8_if(u32x2& d, unsigned voff, unsigned soff, rsrc_t srd, unsigned off) {
  asm volatile("s_cmp_lg_u32 %4, 0\n\ts_cbranch_scc1 1f\n\tbuffer_load_dwordx2 %0, %1, %2, %3 offen\n1:" : "=v"(d) : "v"(voff), "s"(srd), "s"(soff), "s"(off) : "scc");
}

template <class T> DI T pick(bool c, T a, T b) { return c ? a : b; }
DI float fma4(float a, float b, float c, float d, f32x4 k) {
  float r = v_mul(a, k[0]); r = __builtin_fmaf(b, k[1], r); r = __builtin_fmaf(c, k[2], r); return __builtin_fmaf(d, k[3], r);
}
DI float vmax(float a, float b) { float r; asm("v_max_f32 %0, %1, %2" : "=v"(r) : "v"(a), "v"(b)); return r; }
template <bool MAX> DI float x16(float v) {
  const unsigned u = __builtin_bit_cast(unsigned, v);
  auto a = __builtin_amdgcn_permlane16_swap(u, u, false, false);
  const float p = __builtin_bit_cast(float, (unsigned)a[0]), q = __builtin_bit_cast(float, (unsigned)a[1]);
  return MAX ? vmax(p, q) : p + q;
}
template <bool MAX> DI float x32(float v) {
  const unsigned u = __builtin_bit_cast(unsigned, v);
  auto a = __builtin_amdgcn_permlane32_swap(u, u, false, false);
  const float p = __builtin_bit_cast(float, (unsigned)a[0]), q = __builtin_bit_cast(float, (unsigned)a[1]);
  return MAX ? vmax(p, q) : p + q;
}
template <int W> DI void swp(float& a, float& b) {
  const unsigned x = __builtin_bit_cast(unsigned, a), y = __builtin_bit_cast(unsigned, b);
  if constexpr (W == 16) { auto r = __builtin_amdgcn_permlane16_swap(x, y, false, false); a = __builtin_bit_cast(float, (unsigned)r[0]); b = __builtin_bit_cast(float, (unsigned)r[1]); }
  else { auto r = __builtin_amdgcn_permlane32_swap(x, y, false, false); a = __builtin_bit_cast(float, (unsigned)r[0]); b = __builtin_bit_cast(float, (unsigned)r[1]); }
}
template <bool MAX> DI void xq8(float (&m)[8], float k) {
  auto op = [](float p, float q) { return MAX ? vmax(p, q) : p + q; };
  float a[4], d[4];
  sfor<4>([&](auto I) { swp<16>(m[2 * I], m[2 * I + 1]); }); GAP();
  sfor<4>([&](auto I) { a[I] = op(m[2 * I], m[2 * I + 1]); pin(a[I]); }); GAP();
  sfor<2>([&](auto I) { swp<32>(a[2 * I], a[2 * I + 1]); }); GAP();
  sfor<2>([&](auto I) { float c = op(a[2 * I], a[2 * I + 1]); if constexpr (!MAX) c = __builtin_amdgcn_rcpf(c) * k; d[2 * I] = c; d[2 * I + 1] = c; pin(d[2 * I + 1]); }); GAP();
  sfor<2>([&](auto I) { swp<32>(d[2 * I], d[2 * I + 1]); }); GAP();
  sfor<4>([&](auto I) { m[2 * I] = d[I]; m[2 * I + 1] = d[I]; pin(m[2 * I + 1]); }); GAP();
  sfor<4>([&](auto I) { swp<16>(m[2 * I], m[2 * I + 1]); }); GAP();
}
template <int CTRL> DI float dpp_add(float v) { return v + __builtin_bit_cast(float, __builtin_amdgcn_update_dpp(0, __builtin_bit_cast(int, v), CTRL, 0xf, 0xf, true)); }
DI float wave_sum(float v) { return x32<false>(x16<false>(dpp_add<0x140>(dpp_add<0x141>(dpp_add<0x4e>(dpp_add<0xb1>(v)))))); }

constexpr int LDS_V = 46080 + 2560, LDS_K = 92160 + 2560;
constexpr int LDS_X = 6 * 24576, LDS_B = LDS_X + 16384;
constexpr int DA = 4;
constexpr int LF = 8, LQ = 7, PB = 49, SM = 61, QS = 125, P0 = 216, I1 = 19, NE = 3, SE = 16;
constexpr float LOG2E = 1.4426950408889634f;
constexpr int cmax(int a, int b) { return a > b ? a : b; }
constexpr int cmin(int a, int b) { return a < b ? a : b; }

enum { NONE, W1, XF, TOUCH, W2, H2, SAMPLE,   CHECK, PUB, PUBH, XRES, HALO };
struct Op { int kind, a, b; };
template <bool QKV> struct Plan {
  static constexpr int NCG = QKV ? 5 : 8, KS = QKV ? 6 : 9, NST = 54 / KS, SPU = 18 / KS, NSLOT = QKV ? 3 : 2;
  static constexpr int SB = QKV ? 6 * 5120 : 9 * 8192, IPS = KS * NCG, NIT = NST * IPS, BAR = IPS - (DA - 1), NPRO = 6;
  static constexpr bool kmajor(int n) { return QKV ? n % 90 < 75 : n < 96; }
  static constexpr int jof(int n) { return kmajor(n) ? n % NCG : QKV ? (n % 15) / 3 : (n < 144 ? (n - 96) / 12 : (n % IPS) / 18) * 2 + n % 2; }
  static constexpr int sof(int n) { return kmajor(n) ? (n % (18 * NCG)) / NCG : QKV ? 15 + n % 3 : n < 144 ? 12 + (n % 12) / 2 : KS * ((n / IPS) % SPU) + (n % 18) / 2; }
  static constexpr int slot(int st) { return QKV ? (st + 1) % 3 : st % 2; }
  static constexpr int NT = QKV ? 12 : 8, NIT2 = 21 * NT, A2 = QKV ? 9 : 6, M1 = A2 * NT, LT = QKV ? LQ : LF, M2 = M1 + (21 - LT - A2) * (NT - 1), M3 = M2 + LT * (NT - 1);
  static constexpr int SB2 = NT * 1024, NSL2 = 147456 / SB2, WIN = QKV ? 4 : 8, O2 = QKV ? 0 : 12;
  static constexpr int gt(int k) { return QKV ? (k + 8) % 12 : k; }
  static constexpr int st2(int m) { return m < M1 ? m / NT : m < M2 ? A2 + (m - M1) / (NT - 1) : m < M3 ? 21 - LT + (m - M2) % LT : A2 + m - M3; }
  static constexpr int ti2(int m) { return m < M1 ? m % NT : m < M3 ? gt(m < M2 ? (m - M1) % (NT - 1) : (m - M2) / LT) : 7; }
  static constexpr bool first2(int m) { return m < M1 ? m % NT == 0 : m < M2 ? (m - M1) % (NT - 1) == 0 : m < M2 + LT; }
  static constexpr int win2(int m) { return (st2(m) % NSL2) / WIN; }
  static constexpr int pwin2(int m) { for (int i = m - 1; i >= 0; --i) if (ti2(i) == ti2(m)) return win2(i); return -1; }
  static constexpr int bar2(int m) {
    if (QKV) return m == 57 ? 11 : m == M1 - 3 ? 14 : m == M2 + LT - 9 ? 20 : -1;
    return m == 66 ? 17 : m == M2 + LT - 6 ? 20 : -1;
  }
  static constexpr int hstep(int a) { return a < O2 ? a : a - 3; }
  static constexpr bool rebase(int m) {
    if (m >= NIT) return m - NIT < NIT2 && win2(m - NIT) != pwin2(m - NIT) && !(QKV && m - NIT < NT);
    if (QKV) return (m / IPS) % 3 != 1 && m >= IPS && m % IPS < 5;
    return m < 144 ? (m >= 72 && m < 80) : m % 18 < 2;
  }
  static constexpr int estart(int su) { return su == 0 ? 108 : 144 * su + 92; }
  static constexpr int eidx(int su, int n, int t) {
    if (n < estart(su) || n > estart(su) + 100 || (t == 1 && rebase(n + 4))) return -1;
    int c = 0;
    for (int i = estart(su); i < n; ++i) c += rebase(i + 4) ? 1 : 2;
    return c + t < 176 ? c + t : -1;
  }
  static constexpr int eat(int su, int e) { int n = estart(su); for (int c = 0; (c += rebase(n + 4) ? 1 : 2) <= e;) ++n; return n; }
  static constexpr Op w2op(int i) {
    if (!QKV) return Op{W2, i, 0};
    if (i < 5) return i == 2 ? Op{W2, 0, 1} : i == 4 ? Op{W2, 2, 1} : Op{W2, i < 2 ? i : 2, 0};
    return (i - 5) % 3 == 2 ? Op{W2, 3 + (i - 5) / 3 * 2, 1} : Op{W2, 3 + (i - 5) / 3 * 2 + (i - 5) % 3, 0};
  }
  static constexpr int NPRE2 = QKV ? 0 : 21;
  static constexpr Op pre2(int i) { return i < 9 ? w2op(i) : Op{H2, (i - 9) / 2, (i - 9) & 1}; }
  static constexpr Op at1(int n) {
    const int st = n / IPS, it = n % IPS;
    if (QKV) {
      if (n % 2 == 0 && NPRO + n / 2 < 36) return Op{XF, NPRO + n / 2, 0};
      if (n >= 45 && n < 60 && n % 4 == 1) return Op{W1, 3, (n - 45) / 4};
      if (st == 2 && it % 3 == 1 && it < 12) return Op{W1, 4, it / 3};
      if (st > 2 && st + 2 < NST && it % 3 == 1 && it < 12) return Op{W1, st + 2, it / 3};
      if (n == 200) return Op{PUBH, 0, 0};
      if (n == 202 || n == 225) return Op{SAMPLE, 0, 0};
      if (n == 221) return Op{CHECK, 0, 0};
      if (n >= 222 && n < 246 && n % 2 == 0) return Op{H2, 3 + (n - 222) / 4, ((n - 222) / 2) & 1};
      if (n == 245) return Op{CHECK, 3, 0};
      if (n == 246 || n == 247) return Op{HALO, n - 246, 0};
      return Op{NONE, 0, 0};
    }
    if (n % 3 == 0 && NPRO + n / 3 < 36) return Op{XF, NPRO + n / 3, 0};
    if (st >= 1 && st <= 4) { const int i0 = st == 3 ? P0 - 214 : st == 1 ? I1 : 1; if (it >= i0 && (it - i0) % 3 == 0 && (it - i0) / 3 < 9) return Op{W1, st + 1, (it - i0) / 3}; }
    if (n == P0) return Op{PUB, 0, 0};
    if (st >= 1 && st <= 3 && it == (st == 3 ? P0 - 187 : st == 1 ? cmax(I1 + 27, 31) : 31)) return Op{TOUCH, st + 2, 0};
    if (st >= 1 && st <= 3 && it == (st == 3 ? P0 - 185 : st == 1 ? cmax(I1 + 29, 33) : 33)) return Op{TOUCH, st - 1, 1};
    if (n == 330) return Op{SAMPLE, 0, 0};
    if (n == 352) return Op{PUB, 1, 0};
    if (n == 353) return Op{CHECK, 0, 0};
    if (n == 407) return Op{SAMPLE, 1, 0};
    if (n == 431) return Op{CHECK, 1, 0};
    if (n >= 358 && n % 3 == 1 && (n - 358) / 3 < 21) return pre2((n - 358) / 3);
    return Op{NONE, 0, 0};
  }
  static constexpr Op at2(int m) {
    if (QKV) {
      if (m == 12) return Op{PUB, 3, 0};
      if (m >= 14 && m < 44 && m % 3 == 2) return w2op(8 + (m - 14) / 3);
      if (m == 43) return Op{W2, 11, 2};
      if (m == 47) return Op{SAMPLE, 1, 0};
      if (m >= 58 && m < 68 && m % 2 == 0) return m == 60 ? Op{W2, 12, 2} : w2op(18 + (m - 58) / 2);
      if (m == 69) return Op{CHECK, 1, 0};
      if (m >= 70 && m < 94 && m % 2 == 0) return Op{H2, 9 + (m - 70) / 4, ((m - 70) / 2) & 1};
      if (m >= 106 && m < 124 && m % 2 == 0) return w2op(23 + (m - 106) / 2);
      if (m == QS) return Op{SAMPLE, 2, 0};
      if (m == QS + 18) return Op{CHECK, 2, 0};
      if (m > QS + 18 && m <= QS + 30) return Op{H2, 15 + (m - QS - 19) / 2, (m - QS - 19) & 1};
      if (m >= M2 + LT && m < M2 + LT + 4) return Op{XRES, m - M2 - LT, 0};
      return Op{NONE, 0, 0};
    }
    if (m < 42 && m % 2 == 0) { const int e = m / 2; return e < 18 ? (e % 3 == 2 ? w2op(9 + e / 3) : Op{H2, 6 + e / 3, e % 3}) : w2op(e - 3); }
    if (m == 27) return Op{SAMPLE, 1, 0};
    if (m == PB) return Op{PUB, 2, 0};
    if (m == PB + 1) return Op{TOUCH, 0, 2};
    if (m == PB + 2) return Op{CHECK, 4, 0};
    if (m > PB + 2 && m <= PB + 8) return Op{H2, 12 + (m - PB - 3) / 2, (m - PB - 3) & 1};
    if (m == SM) return Op{SAMPLE, 2, 0};
    if (m >= 68 && m < 74 && m % 2 == 0) return w2op(18 + (m - 68) / 2);
    if (m == SM + 18) return Op{CHECK, 2, 0};
    if (m > SM + 18 && m <= SM + 30) return Op{H2, 15 + (m - SM - 19) / 2, (m - SM - 19) & 1};
    if (m == M2 + LT || m == M2 + LT + 2) return Op{XRES, (m - M2 - LT) / 2, 0};
    return Op{NONE, 0, 0};
  }
  static constexpr int wait1(int n0, int kind, int amax) {
    int cnt = 0, need = -1;
    for (int i = -NPRO; i < n0; ++i) {
      const Op o = i < 0 ? Op{XF, i + NPRO, 0} : at1(i);
      if (o.kind == PUB) need = -1;
      if (o.kind == NONE || o.kind > SAMPLE) continue;
      ++cnt;
      if (o.kind == kind && o.a <= amax) need = cnt;
    }
    return need < 0 || cnt - need > 62 ? 63 : cnt - need;
  }
  static constexpr int wait2(int m0, int kind, int amax) {
    int cnt = 0, need = -1;
    for (int i = -NPRE2; i < m0; ++i) {
      const Op o = i < 0 ? pre2(i + NPRE2) : at2(i);
      if (o.kind == PUB) need = -1;
      if (o.kind == NONE || o.kind > SAMPLE) continue;
      ++cnt;
      if (o.kind == kind && o.a <= amax) need = cnt;
    }
    return need < 0 || cnt - need > 62 ? 63 : cnt - need;
  }
};
using PF = Plan<false>;
static_assert(PF::eat(0, 175) < P0 - 14 && PF::eat(1, 175) < 352 - 16 && PF::eat(2, 175) < PF::NIT + PB - 6, "a unit is published after its last store");
static_assert(PF::eat(0, 44) >= 96 + 26 && PF::eat(0, 88) >= 96 + 38 && PF::eat(0, 132) >= 96 + 50, "unit 0: accumulators complete before they are read");
static_assert(PF::eat(0, 39) < 144 && PF::eat(0, 83) < 162 && PF::eat(0, 127) < 180 && PF::eat(0, 171) < 198, "unit 0: accumulators read before they restart");
static_assert(PF::eat(1, 44) >= 216 + 38 && PF::eat(1, 88) >= 216 + 56 && PF::eat(1, 132) >= 216 + 74 && PF::eat(1, 171) < 288 + 54, "unit 1");
static_assert(PF::eat(0, 153) > 144 + 30 && PF::eat(0, 153) < 213 && PF::eat(1, 65) > 216 + 40 && PF::eat(1, 65) < 285, "early weight waits follow the last request");
static_assert(PF::eat(2, 44) >= 360 + 38 && PF::eat(2, 88) >= 360 + 56 && PF::eat(2, 132) >= 360 + 74, "unit 2");

struct Args { const char* x; const char* wqkv; const char* wo; const char* wg; const char* wu; const char* wd; char* ws; char* out; };

DI void ln_load(u32x4& v, unsigned& v9, const char* x, int row, int lane) {
  const rsrc_t sx = mk_rsrc(x, S * D * 2);
  const unsigned xo = (unsigned)(row * 1152);
  load16(v, xo + lane * 16, sx);
  asm volatile("buffer_load_ushort %0, %1, %2, 0 offen offset:1024" : "=v"(v9) : "v"(xo + lane * 2), "s"(sx));
}
DI void ln_row(u32x4 v, unsigned v9, unsigned smem, int wave, int lane) {
  float f[9];
  sfor<4>([&](auto E) { f[2 * E] = bflo(v[(int)E]); f[2 * E + 1] = bfhi(v[(int)E]); });
  f[8] = bflo(v9);
  const float mean = wave_sum(((f[0] + f[1]) + (f[2] + f[3])) + ((f[4] + f[5]) + (f[6] + f[7])) + f[8]) * (1.0f / D);
  float q = 0.f;
  sfor<9>([&](auto E) { const float d = f[E] - mean; q += d * d; });
  const float var = wave_sum(q) * (1.0f / D);
  const float mb = rbf(mean), rs = LOG2E * __builtin_amdgcn_rcpf(rbf(__builtin_amdgcn_sqrtf(rbf(rbf(var) + 1e-5f))));
  sfor<4>([&](auto E) { v[(int)E] = cvtpk((f[2 * E] - mb) * rs, (f[2 * E + 1] - mb) * rs); });
  *(LDS u32x4*)(smem + lane * 128 + wave * 16) = v;
  *(LDS unsigned short*)(smem + 8192 + (lane >> 3) * 128 + wave * 16 + (lane & 7) * 2) = (unsigned short)cvtpk((f[8] - mb) * rs, 0.f);
}
DI void ln_publish(char* ws, unsigned smem, unsigned tag, int gid, int lane) {
  u32x4 t[9];
  const unsigned fo = spin(rfl(FL_X + FS * gid));
  sfor<9>([&](auto I) { t[I] = *(LDS u32x4*)(smem + 1024 * I + lane * 16); });
  const unsigned o = (unsigned)((gid >> 1) * 18432 + (gid & 1) * 128 + (lane >> 3) * 256 + (lane & 7) * 16);
  const rsrc_t sxn = mk_rsrc(ws + WS_XN, XN_B);
  sfor<9>([&](auto I) { store16_sc1<0>(t[I], sxn, o, 2048 * I); });
  publish(tag, fo, mk_rsrc(ws + WS_FL, FL_B));
}

template <bool QKV>
DI void body(const Args& A, unsigned smem, unsigned tag, int rt, int col0, int wave, int lane, unsigned tch0) {
  using P = Plan<QKV>;
  constexpr int NCG = P::NCG, KS = P::KS, IPS = P::IPS, NIT = P::NIT, SPU = P::SPU, SB = P::SB, NT = P::NT, NIT2 = P::NIT2;
  constexpr int PL1 = QKV ? 3 : 4;
  constexpr unsigned RS = QKV ? 3u * D * 2u : F * 2u, WB = QKV ? D * 3 * D * 2 : D * F * 2;
  char* ws = A.ws;
  const int bid = blockIdx.x, isU = wave >> 2, w3 = wave & 3;
  const rsrc_t sF = mk_rsrc(ws + WS_FL, FL_B), sxn = mk_rsrc(ws + WS_XN, XN_B);
  const int cgl = ((lane & 7) >> 1) ^ ((lane >> 4) & 3);
  const int pc = 2 * cgl + (lane & 1);
  const unsigned wv = (unsigned)(((QKV ? 32 * isU : 0) + 16 * (w3 & 1) + 4 * (w3 >> 1) + 8 * (lane >> 5) + ((lane >> 3) & 3)) * RS + col0 * 2 + pc * 16);
  const unsigned wu = (unsigned)((8 * ((lane >> 3) & 3) + 4 * (lane >> 5) + ((lane >> 1) & 3)) * RS + col0 * 2 + 128);
  const char* wA = QKV ? A.wqkv : A.wg;
  const rsrc_t sw = mk_rsrc(isU && !QKV ? A.wu : wA, WB);
  const unsigned m0b = smem + isU * (QKV ? 5120 : SB / 2) + w3 * 1024;
  const int us = min(wave, 5);
  const unsigned m0u = smem + 4096 + us * 5120, wus = wu + us * 32 * RS;
  auto w1 = [&](auto ST, auto B, unsigned m0, unsigned vo, rsrc_t srd) {
    constexpr int a = CV(ST), b = CV(B) % (QKV ? 3 : 9), su = a / SPU;
    constexpr unsigned sucol = QKV ? (su == 0 ? 1152u : su == 1 ? 2304u : 0u) : su * 128u;
    dma16<P::slot(a) * SB + b * (QKV ? 10240 : 4096), 32 * (KS * (a % SPU) + (QKV ? 2 : 1) * b) * RS + sucol>(m0, vo, srd);
  };
  if (isU) {
    const rsrc_t sa = mk_rsrc(wA, WB);
    if constexpr (!QKV) sfor<2>([&](auto ST) { sfor<9>([&](auto B) { w1(ST, B, m0b - SB / 2, wv, sa); w1(ST, B, m0b, wv, sw); }); });
    else sfor<3>([&](auto ST) {
      sfor<3>([&](auto B) { w1(ST, B, m0b - 5120, wv - 32 * RS, sw); w1(ST, B, m0b, wv, sw); });
      w1(ST, ic<3>{}, smem + 4096 + w3 * 5120, wu + w3 * 32 * RS, sw); w1(ST, ic<3>{}, m0u, wus, sw);
    });
  }
  const int t16 = rt * 16 + 2 * wave;
  const unsigned xs = spin(t16 * 18432), l16 = lane * 16;
  u32x4 xf[18][2];
  auto xload = [&](auto AA) { constexpr int a = CV(AA); load16_sc1<(a / 2) * 1024 + (a & 1) * 18432>(xf[a / 2][a & 1], l16, xs, sxn); };
  if (wave == 0) {
    const unsigned fo = FL_X + FS * (rt * 32 + (lane & 31));
    __builtin_amdgcn_s_sleep(20);
    unsigned f; load4_sc1(f, fo, sF); flags_wait<0, 1>(f, fo, tag, ~0ull, sF);
  }
  asm volatile("s_barrier" ::: "memory");
  sfor<P::NPRO>(xload);
  GAP();
  const int b2 = bid & 7, tile = bid >> 3, nc2 = QKV ? 768 : (tile - 3 * rt) * 256;
  const rsrc_t sh = mk_rsrc(ws + WS_H, HBUF_B), sS = mk_rsrc(ws + WS_SLAB, SLAB_B);
  const rsrc_t sWd = mk_rsrc(A.wd + 576 * 1152 * b2, 576 * 1152), sWo = mk_rsrc(A.wo + 72 * 1152 * b2, 64 * 1152), sWp = mk_rsrc(A.wo + (72 * b2 + 64) * 1152, 8 * 1152);
  const unsigned m0c = spin(smem + wave * 1024), m0d = spin(smem + 8192 + w3 * 1024 + isU * P::SB2);
  const unsigned hb = spin((t16 * KSTOT + 18 * b2) * 1024), ho = spin((t16 * KSTOT + 144 + 3 * b2) * 1024), hst = spin((t16 * KSTOT + (col0 >> 5)) * 1024);
  const unsigned oso = spin(t16 * 16 * 1152 + nc2), sl = spin(((tile * 8 + b2) * 8 + wave) * SLAB_W);
  const unsigned xt = spin(((wave - b2) & 7) >> 1), guest = spin((wave - b2) & 1);
  const unsigned fme = spin(FL_H + ((rt * 8 + wave) * 96 + (QKV ? 12 * b2 : 4 * (col0 / 192))) * FS);
  const unsigned tab = smem + LDS_X + wave * 2048 + l16;
  const unsigned atab = smem + 141312 + l16;
  const bool wdo = tile * 8 + wave >= 162;
  unsigned ab[NCG];
  u32x4 t0;
  {
    const unsigned l = lane, b0 = smem + (l >> 2) * 128 + ((l >> 3) & 3) * 32 + 8 * (l & 3);
    t0[0] = (8 * w3 + (l >> 3)) * 1152 + nc2 + isU * 128 + pc * 16;
    t0[1] = FL_H + ((rt * 8 + wave) * 96 + 12 * b2 + (l < 12 ? l : 0)) * FS;
    t0[2] = b0;
    t0[3] = (l & 15) * 1152 + (l >> 4) * 8;
    *(LDS u32x4*)tab = t0;
    if constexpr (!QKV) *(LDS u32x2*)(tab + 1024) = u32x2{
      pick<unsigned>(l < 9, (rt * 36 + w3 * 9 + l) * RS + col0 * 2, 0x80000000u),
      pick<unsigned>(l < 11, (wdo ? tile * 8 + wave - 162 : tile * 8 + wave) * 4096 + l * 128, 0x80000000u)};
    if constexpr (QKV) if (wave == 1) {
      const int i16 = lane & 15, q4 = lane >> 4, odd = lane & 1;
      const bool cur0 = 4 * q4 + odd <= i16, cur1 = 4 * q4 + 2 + odd <= i16;
      const unsigned s0 = odd ? 0x01000c0cu : 0x0c0c0100u, s1 = odd ? 0x03020c0cu : 0x0c0c0302u, g0 = (unsigned)(-q4 >> 31), g1 = (unsigned)((1 - q4) >> 31);
      constexpr float SC = 0.23570226039551584f / LOG2E;
      f32x4 ca, cc;
      sfor<4>([&](auto I) { const bool par = odd == (CV(I) & 1); ca[(int)I] = par && cur0 == (CV(I) < 2) ? SC : 0.f; cc[(int)I] = par && cur1 == (CV(I) < 2) ? SC : 0.f; });
      *(LDS u32x4*)atab = u32x4{cur0 ? 0x0c0c0c0cu : s0, cur1 ? 0x0c0c0c0cu : s1, cur0 ? s0 : 0x0c0c0c0cu, cur1 ? s1 : 0x0c0c0c0cu};
      *(LDS u32x4*)(atab + 1024) = u32x4{g0, g1, ~g0, ~g1};
      *(LDS f32x4*)(atab + 2048) = ca; *(LDS f32x4*)(atab + 3072) = cc;
    }
    if constexpr (QKV) *(LDS unsigned*)(tab + 1024) = t0[0] + 256 + isU * (32 * 1152 - 128);
    if constexpr (QKV) sfor<5>([&](auto C) { ab[C] = (CV(C) < 4 ? b0 ^ (CV(C) * 32) : smem + 4096 + 8 * l) + SB; pin(ab[C]); });
    else sfor<8>([&](auto J) { ab[J] = (b0 ^ ((CV(J) >> 1) * 32)) + (CV(J) & 1) * (SB / 2); pin(ab[J]); });
  }
  u32x4 hf[21][2], hp[2], gp;
  unsigned fl, cur[2], wv3, tch[2], tk[3][2], cb[NT];
  u32x2 xres[4], pk[5][2], hl[10];
  unsigned l8 = lane * 8;
  if constexpr (QKV) pin(l8);
  const unsigned stg = smem + wave * 5120 + l8;
  auto ldtab = [&](int i) { return *(volatile LDS unsigned*)(tab + 1024 + 4 * i); };
  auto vmem = [&](auto K, auto AA, auto BB, auto NN) {
    constexpr int kind = CV(K), a = CV(AA), b = CV(BB);
    if constexpr (kind == W1) w1(AA, BB, QKV && b == 3 ? m0u : m0b, QKV && b == 3 ? wus : wv, sw);
    else if constexpr (kind == XF) xload(AA);
    else if constexpr (kind == TOUCH) {
      if constexpr (b == 2) load4_sc1(tch[0], (l16 & 0xf0) * 8 + (l16 & 0x100) * (KSTOT * 4) + (l16 >> 9) * 0x80000000u + hst + 4096, sh);
      else if constexpr (b == 0) load4s<288 * (a % 2) * RS + (a / 2) * 128>(tch[0], tch[0], sw);
      else load4s<a * 11 * 128>(tch[1], tch[1], mk_rsrc(wdo ? A.wo + 72 * 1152 * b2 : A.wd + 576 * 1152 * b2, wdo ? 72 * 1152 - 4096 : 576 * 1152 - 4096));
    } else if constexpr (kind == XRES) load8_if(xres[a], t0[3], oso + (a & 1) * 16 * 1152 + (a < 2 ? 32 * b2 : 256 + 32 * xt), mk_rsrc(A.x, S * D * 2), a < 2 ? 0u : guest);
    else if constexpr (kind == W2) {
      constexpr bool isO = a >= P::O2 && a < P::O2 + 3;
      constexpr int e = P::hstep(a), row = isO ? (a - P::O2 == 1 ? 32 : 0) : 192 * ((e % 6) / 2) + 64 * (e / 6) + 32 * (e & 1);
      const rsrc_t r = !isO ? sWd : a - P::O2 < 2 ? sWo : sWp;
      if constexpr (b == 0) dma16<(a % P::NSL2) * P::SB2, row * 1152>(m0c, t0[0], r);
      else if constexpr (b == 1 && !(isO && a == 2)) dma16<(a % P::NSL2) * P::SB2, row * 1152>(m0d, wv3, r);
      else dma16<(a % P::NSL2) * P::SB2 + 8192, row * 1152 + 256>(smem + w3 * 1024, t0[0] - isU * 128, r);
    } else if constexpr (kind == H2) {
      constexpr int e = P::hstep(a);
      if constexpr (a >= P::O2 && a < P::O2 + 3) load16_sc1<(b * KSTOT + a - P::O2) * 1024>(hf[a][b], l16, ho, sh);
      else load16_sc1<(b * KSTOT + 6 * ((e % 6) / 2) + 2 * (e / 6) + (e & 1)) * 1024>(hf[a][b], l16, hb, sh);
    } else if constexpr (kind == HALO) load8x5_sc1(hl + 5 * a, mk_rsrc(ws + WS_HALO + (bid - 200) * 5120 + a * 2560, rt > 0 ? 2560 : 0), l8, wave);
    else if constexpr (kind == SAMPLE) load4_sc1(fl, t0[1], sF);
    else if constexpr (kind == CHECK) flags_wait<CV(NN), QKV ? 4 : 2>(fl, t0[1], tag, a == 0 ? 0x111ull : a == 1 ? 0x222ull : a == 2 ? 0x444ull : a == 4 ? 0x8ull : wave == 0 && rt > 0 ? 0x80ull : 0ull, sF);
    else if constexpr (kind == PUB) publish(tag, fme + a * FS, sF);
    else if constexpr (kind == PUBH) publish_if(tag, fme + (96 + 7) * FS, sF, wave ^ 7);
  };
  f32x4 acc[NCG][2], acc2[NT][2];
  bf16x4 al[DA], ah[DA];
  auto aread = [&](auto N, auto T) {
    constexpr int n = CV(N);
    bf16x4 r;
    if constexpr (n >= NIT) r = trread(cb[P::ti2(n - NIT)], ((P::st2(n - NIT) % P::NSL2) % P::WIN) * P::SB2 + CV(T) * 2048);
    else if constexpr (QKV) r = trread(ab[P::jof(n)], (P::sof(n) % 12) * 5120 + CV(T) * (P::jof(n) < 4 ? 2048 : 512));
    else r = trread(n < 144 ? ab[P::jof(n)] : cur[P::jof(n) & 1], (P::sof(n) % KS) * 4096 + CV(T) * 2048);
    if constexpr (CV(T) == 0) al[n % DA] = r; else ah[n % DA] = r;
  };
  auto rebase = [&](auto M) {
    constexpr int m = CV(M);
    if constexpr (m >= NIT) {
      constexpr int p = m - NIT, i = P::ti2(p);
      if constexpr (P::pwin2(p) >= 0) cb[i] = v_addk<(unsigned)((P::win2(p) - P::pwin2(p)) * P::WIN * P::SB2)>(cb[i]);
      else { const unsigned ph = i < 8 ? (i + b2 + 1) & 7 : i; cb[i] = v_xors(t0[2], (ph & 3) * 32 + (ph >> 2) * 4096); }
    } else if constexpr (QKV) ab[P::jof(m)] = v_addk<(m / IPS) % 3 == 0 ? (unsigned)SB : (unsigned)-SB>(ab[P::jof(m)]);
    else if constexpr (m < 144) ab[P::jof(m)] = v_addk<SB>(ab[P::jof(m)]);
    else cur[P::jof(m) & 1] = v_addk<(unsigned)((((m / IPS) & 1) - 1) * SB + (P::jof(m) & 1) * (SB / 2))>(ab[P::jof(m) & ~1]);
  };
  float ee, eg, eh[2];
  const float k2 = __builtin_bit_cast(float, spin(__builtin_bit_cast(unsigned, LOG2E * LOG2E)));
  auto epi = [&](auto SU, auto E) {
    constexpr int e = CV(E), w = e / 11, ph = e % 11, p = w >> 2, th = (w >> 1) & 1, r = 2 * (w & 1) + (ph >= 4 && ph != 5);
    if constexpr (ph == 0 || ph == 4) { if constexpr (ph == 4) eh[0] = ee; ee = v_expn(acc[2 * p][th][r]); }
    if constexpr (ph == 1 || ph == 6) eg = v_mul(acc[2 * p][th][r], acc[2 * p + 1][th][r]);
    if constexpr (ph == 2 || ph == 7) ee = v_fmak(ee, k2);
    if constexpr (ph == 3 || ph == 8) ee = v_rcp(ee);
    if constexpr (ph == 5) eh[0] = v_mul(eg, eh[0]);
    if constexpr (ph == 9) eh[1] = v_mul(eg, ee);
    if constexpr (ph == 10) {
      hp[th][2 * (p & 1) + (w & 1)] = v_cvtpk(eh[0], eh[1]);
      if constexpr ((p & 1) && (w & 1)) store16_sc1<(p >> 1) * 1024>(hp[th], sh, l16, hst + (th * KSTOT + 2 * CV(SU)) * 1024);
    }
  };
  auto filler = [&](auto N, auto T) {
    constexpr int n = CV(N), t = CV(T);
    if constexpr (t == 1 && P::rebase(n + 4)) rebase(ic<n + 4>{});
    else if constexpr (!QKV) sfor<3>([&](auto SU) { constexpr int e = P::eidx(CV(SU), n, t); if constexpr (e >= 0) epi(SU, ic<e>{}); });
    GAP();
  };
  auto epiq = [&](auto SU, auto E, auto T) {
    constexpr int su = CV(SU), e = CV(E) - 4, t = CV(T), cg = e / 3, th = e % 3;
    if constexpr (e >= 0 && cg < 5 && th < 2) {
      pk[cg][th][t] = v_cvtpk(acc[cg][th][2 * t], acc[cg][th][2 * t + 1]);
      if constexpr (su == 0 && t == 1) *(LDS u32x2*)(stg + LDS_K + th * 2560 + 512 * cg) = pk[cg][th];
      if constexpr (su < 2 && t == 1 && th == 1) store8_sc1<512 * cg, 7>(pk[cg][1], mk_rsrc(ws + WS_HALO + (bid - 192) * 5120 + su * 2560, 2560), l8, wave);
      GAP();
    }
  };
  GAP(); wait_barrier<P::NPRO - 2>(); GAP();
  keep(tch0);
  __builtin_amdgcn_s_setprio(PL1 - 1);
  sfor<DA - 1>([&](auto N) { aread(N, ic<0>{}); aread(N, ic<1>{}); });
  GAP();
  sfor<NIT>([&](auto N) {
    constexpr int n = CV(N), st = n / IPS, it = n % IPS, j = P::jof(n), sg = P::sof(n), su = st / SPU, m = n + DA - 1;
    constexpr bool more = m < NIT || !QKV;
    if constexpr (QKV && n == NIT - (DA - 1)) { lds_barrier(); GAP(); }
    constexpr bool early = QKV ? st == 2 || st == 5 : st == 2 || st == 3;
    if constexpr (early && n == (QKV ? st * IPS + 19 : P::eat(st - 2, st == 2 ? 153 : 65) - 1)) { wait_vm<P::wait1(n, W1, st + 1)>(); GAP(); }
    if constexpr (it == P::BAR && st < P::NST - 1) { wait_barrier<early ? 63 : P::wait1(n, W1, st + 1)>(); GAP(); }
    if constexpr (!QKV && n == NIT - (DA - 1)) { wait_barrier<cmin(P::wait1(n, W2, 8), P::wait1(n, H2, 0))>(); GAP(); }
    { constexpr int pp = (it + DA - 1) % IPS; if constexpr (pp % (IPS / PL1) == 0) { __builtin_amdgcn_s_setprio(PL1 - 1 - pp / (IPS / PL1)); GAP(); } }
    if constexpr (su == 0 && j == 0 && n > 0) { wait_vm<P::wait1(n, XF, 2 * sg + 1)>(); GAP(); }
    const bf16x8 a = cat8(al[n % DA], ah[n % DA]);
    constexpr bool swp = QKV && su == 1;
    sfor<2>([&](auto T) {
      if constexpr (more) { aread(ic<m>{}, T); GAP(); }
      if constexpr (more && CV(T) == 0 && n % 2 == 0) { __builtin_amdgcn_s_waitcnt(0xC07F | (3 << 8)); GAP(); }
      const bf16x8 b = as_bf(xf[sg][T]);
      if constexpr (sg == 0) mfma_new(acc[j][T], swp ? b : a, swp ? a : b); else mfma_acc(acc[j][T], swp ? b : a, swp ? a : b);
      GAP();
      filler(N, T);
      if constexpr (QKV && n % 90 >= 75) epiq(ic<su>{}, ic<n % 90 - 75>{}, T);
      if constexpr (QKV && n % 90 < 5 && su > 0) epiq(ic<su - 1>{}, ic<n % 90 + 15>{}, T);
    });
    if constexpr (QKV) {
      if constexpr (st == 8 && it < 10) *(LDS u32x2*)(stg + LDS_V + (it % 2) * 2560 + 512 * (it / 2)) = pk[it / 2][it % 2];
      if constexpr (n == 194) t0 = *(volatile LDS u32x4*)tab;
      if constexpr (n == 250) wv3 = ldtab(0);
      if constexpr (n == 264) halo_park(hl, stg + LDS_K - 2560, stg + LDS_V - 2560, wave);
    } else {
      if constexpr (st >= 1 && st <= 3 && it == 24) { tch[0] = ldtab(0); tch[1] = ldtab(1); }
      if constexpr (st >= 1 && st <= 3 && it == 56) { tk[st - 1][0] = tch[0]; tk[st - 1][1] = tch[1]; }
      if constexpr (n == 200 || n == 230 || n == 354) { constexpr int q = n == 200 ? 0 : n == 230 ? 1 : 2; pin(tk[q][0]); pin(tk[q][1]); }
      if constexpr (n == 320) t0 = *(volatile LDS u32x4*)tab;
    }
    constexpr Op o = P::at1(n);
    if constexpr (o.kind != NONE) vmem(ic<o.kind>{}, ic<o.a>{}, ic<o.b>{}, ic<o.kind == CHECK ? P::wait1(n, SAMPLE, 9) : 0>{});
    GAP();
  });
  if constexpr (QKV) {
    mfma_drain();
    sfor<4>([&](auto J) { epiq(ic<2>{}, ic<16 + CV(J) / 2>{}, ic<CV(J) % 2>{}); });
    const u32x4 sel = *(volatile LDS u32x4*)(atab), mk = *(volatile LDS u32x4*)(atab + 1024);
    const f32x4 ca = *(volatile LDS f32x4*)(atab + 2048), cc = *(volatile LDS f32x4*)(atab + 3072);
    auto ld2 = [&](unsigned a0, unsigned a1) { const u32x2 u = *(volatile LDS u32x2*)a0, v = *(volatile LDS u32x2*)a1; return as_bf(u32x4{u[0], u[1], v[0], v[1]}); };
    const float cp = t16 == 0 ? -1.0e30f : 0.f;
    auto mge = [&](unsigned v, auto C) { constexpr int c = CV(C); if constexpr (c <= 0) return v; else return v & mk[(c - 1) / 4]; };
    auto mlt = [&](unsigned v, auto C) { constexpr int c = CV(C); if constexpr (c <= 0) return 0u; else return v & mk[2 + (c - 1) / 4]; };
    unsigned sk = stg + LDS_K - 2560, sv = stg + LDS_V - 2560;
    pin(sk); pin(sv);
    f32x4 sp[8], sc[8];
    float l0[8], l1[8], mx[8];
    bf16x8 pb[8], kb[4][3], vb[2][5];
    sfor<12>([&](auto I) { constexpr int h = CV(I) / 3, g = CV(I) % 3; kb[h][g] = ld2(sk + g * 2560 + 512 * h, sk + g * 2560 + 512 * h + 512); });
    GAP();
    auto pf = [&](auto I) { constexpr Op o = P::w2op(CV(I)); GAP(); vmem(ic<o.kind>{}, ic<o.a>{}, ic<o.b>{}, ic<0>{}); GAP(); };
    pf(ic<0>{});
    sfor<4>([&](auto HH) {
      constexpr int hd = CV(HH);
      sfor<2>([&](auto TH) {
        const bf16x8 qb = as_bf(u32x4{mge(pk[hd][TH][0], ic<2 * hd>{}), mge(pk[hd][TH][1], ic<2 * hd - 2>{}), mlt(pk[hd + 1][TH][0], ic<2 * hd + 2>{}), mlt(pk[hd + 1][TH][1], ic<2 * hd>{})});
        sp[2 * hd + TH] = CV(TH) ? mfma(kb[hd][1], qb, f32x4{}) : mfma(kb[hd][0], qb, f32x4{cp, cp, cp, cp});
        sc[2 * hd + TH] = mfma(kb[hd][1 + TH], qb, f32x4{});
      });
      pf(ic<1 + hd>{});
    });
    sfor<10>([&](auto I) { constexpr int th = CV(I) / 5, j = CV(I) % 5; vb[th][j] = ld2(sv + th * 2560 + 512 * j, sv + (th + 1) * 2560 + 512 * j); });
    GAP();
    lds_barrier();
    sfor<8>([&](auto E) {
      l0[E] = fma4(sc[E][0], sc[E][1], sp[E][0], sp[E][1], ca);
      l1[E] = fma4(sc[E][2], sc[E][3], sp[E][2], sp[E][3], cc);
      mx[E] = vmax(l0[E], l1[E]);
      if constexpr (CV(E) % 3 == 0) pf(ic<5 + CV(E) / 3>{});
    });
    GAP();
    xq8<true>(mx, 0.f);
    GAP();
    sfor<8>([&](auto E) { l0[E] = __builtin_amdgcn_exp2f(l0[E] - mx[E]); l1[E] = __builtin_amdgcn_exp2f(l1[E] - mx[E]); mx[E] = l0[E] + l1[E]; });
    GAP();
    xq8<false>(mx, 1.f / LOG2E);
    GAP();
    sfor<8>([&](auto E) {
      const unsigned pp = cvtpk(l0[E] * mx[E], l1[E] * mx[E]);
      pb[E] = as_bf(u32x4{__builtin_amdgcn_perm(pp, pp, sel[0]), __builtin_amdgcn_perm(pp, pp, sel[1]),
                          __builtin_amdgcn_perm(pp, pp, sel[2]), __builtin_amdgcn_perm(pp, pp, sel[3])});
    });
    GAP();
    sfor<8>([&](auto E) { sp[E] = mfma(vb[CV(E) & 1][CV(E) >> 1], pb[E], f32x4{}); sc[E] = mfma(vb[CV(E) & 1][(CV(E) >> 1) + 1], pb[E], f32x4{}); });
    GAP();
    sfor<NT>([&](auto I) { rebase(ic<NIT + CV(I)>{}); });
    wait_vm<0>();
    sfor<2>([&](auto TH) {
      unsigned ow[12];
      sfor<12>([&](auto E) {
        constexpr int j = CV(E) >> 1, e = CV(E) & 1;
        unsigned a = 0u, b = 0u;
        if constexpr (j < 4) a = mge(cvtpk(sp[2 * j + TH][2 * e], sp[2 * j + TH][2 * e + 1]), ic<2 * j - 2 * e>{});
        if constexpr (j > 0 && j < 5) b = mlt(cvtpk(sc[2 * j - 2 + TH][2 * e], sc[2 * j - 2 + TH][2 * e + 1]), ic<2 * j - 2 * e>{});
        ow[E] = a | b;
      });
      sfor<3>([&](auto M) { hf[M][TH] = u32x4{ow[4 * M], ow[4 * M + 1], ow[4 * M + 2], ow[4 * M + 3]}; pin(hf[M][TH]); store16_sc1<1024 * CV(M)>(hf[M][TH], sh, l16, ho + TH * KSTOT * 1024); });
    });
    GAP(); lds_barrier(); GAP();
    sfor<DA - 1>([&](auto N) { aread(ic<NIT + CV(N)>{}, ic<0>{}); aread(ic<NIT + CV(N)>{}, ic<1>{}); });
    __builtin_amdgcn_s_setprio(3);
    GAP();
  }
  const unsigned fs0 = spin(FL_S + (tile * 8 + wave) * 16 * FS);
  unsigned fo, f;
  sfor<NIT2>([&](auto M) {
    constexpr int m = CV(M), n = NIT + m, st = P::st2(m), i = P::ti2(m);
    if constexpr (P::bar2(m) >= 0) { wait_barrier<P::wait2(m, W2, P::bar2(m))>(); GAP(); }
    {
      constexpr int lo = [] { int k = m; while (k >= 0 && P::bar2(k) < 0) --k; return k < 0 ? -3 : k; }(), hi = [] { int k = m + 1; while (k < NIT2 && P::bar2(k) < 0) ++k; return k; }();
      constexpr int len = (hi - lo + 3) / 4;
      if constexpr ((m - lo) % len == 0) { __builtin_amdgcn_s_setprio(3 - (m - lo) / len); GAP(); }
    }
    if constexpr (P::first2(m) && st > 0) { wait_vm<P::wait2(m, H2, st)>(); GAP(); }
    const bf16x8 a = cat8(al[n % DA], ah[n % DA]);
    sfor<2>([&](auto T) {
      if constexpr (m + DA - 1 < NIT2) { aread(ic<n + DA - 1>{}, T); GAP(); }
      if constexpr (m + DA - 1 < NIT2 && CV(T) == 0 && n % 2 == 0) { __builtin_amdgcn_s_waitcnt(0xC07F | (3 << 8)); GAP(); }
      if constexpr (st == 0) mfma_new(acc2[i][T], a, as_bf(hf[st][T])); else mfma_acc(acc2[i][T], a, as_bf(hf[st][T]));
      GAP();
      filler(ic<n>{}, T);
      constexpr int c = m - P::M2 - P::LT - 1;
      if constexpr (c >= 0 && c / P::LT < NT - 1 && c % P::LT < 4 && CV(T) == 0) {
        constexpr int tl = P::gt(c / P::LT), e = c % P::LT;
        gp[e] = v_cvtpk(acc2[tl][e >> 1][2 * (e & 1)], acc2[tl][e >> 1][2 * (e & 1) + 1]);
        if constexpr (e == 3 && tl == NE) publish(tag, fs0 + b2 * FS, sF);
        if constexpr (e == 3) store16_sc1<0>(gp, sS, l16, sl + tl * 1024);
        GAP();
      }
    });
    if constexpr (!QKV && m == 118) pin(tch[0]);
    if constexpr (m == NIT2 - 30) { fo = fs0 + ((16 * (b2 + 7) - l16) & 0x70) * (FS / 16) + ((l16 & 0x70) < 16 * NE ? 0 : 8 * FS); pin(fo); }
    if constexpr (m == NIT2 - 1 - SE) { load4_sc1(f, fo, sF); GAP(); }
    constexpr Op o = P::at2(m);
    if constexpr (o.kind != NONE) { vmem(ic<o.kind>{}, ic<o.a>{}, ic<o.b>{}, ic<o.kind == CHECK ? P::wait2(m, SAMPLE, 9) : 0>{}); GAP(); }
  });
  if (!QKV && wave == 0 && bid == 0) {
    unsigned g[4];
    sfor<4>([&](auto I) { load4_sc1(g[I], FL_X + 64 * FS * I + 8 * l16, sF); });
    sfor<4>([&](auto I) { flags_wait<0, 2>(g[I], FL_X + 64 * FS * I + 8 * l16, tag, ~0ull, sF); });
    flag_only(tag, FL_EPOCH, sF);
  }
  publish(tag, fs0 + (8 + b2) * FS, sF);
  mfma_drain();
  pin(acc2[7][0]); pin(acc2[7][1]);
  sfor<QKV ? 4 : 2>([&](auto I) { pin(xres[I]); });
  u32x4 pv[7], pw[8];
  const rsrc_t sQ = mk_rsrc(ws + WS_SLAB + 8192 + xt * 1024, QKV && !guest ? SLAB_B - 12288 + 1024 : 0u);
  if constexpr (QKV) load16_sc1<0>(pw[7], l16, sl, sQ);
  lane = lane_now();
  const unsigned one = (lane >> 4) == ((lane >> 2) & 3) ? (lane & 1 ? 0x3f800000u : 0x3f80u) : 0u, e0 = lane & 2 ? 0u : one, e1 = lane & 2 ? one : 0u;
  const bf16x8 elo = as_bf(u32x4{e0, e1, 0u, 0u}), ehi = as_bf(u32x4{0u, 0u, e0, e1});
  const bf16x8 xb = as_bf(u32x4{xres[0][0], xres[0][1], xres[1][0], xres[1][1]});
  f32x4 o0 = mfma(elo, xb, acc2[7][0]), o1 = mfma(ehi, xb, acc2[7][1]);
  const unsigned xr = smem + LDS_X + wave * 2048;
  unsigned tw = xr + (lane & 15) * 48 + (lane >> 4) * 8, trd = xr + (lane >> 1) * 48 + (lane & 1) * 16, tst = (lane >> 1) * 1152 + (lane & 1) * 16;
  pin(tw); pin(trd); pin(tst); pin(o0); pin(o1);
  gather7<QKV>(pv, pw, f, fo, l16, l16 + 4096, tag, b2 + 7, spin((tile * 64 + wave) * SLAB_W), sS, sQ, sF);
  const rsrc_t sO = mk_rsrc(A.out, S * D * 2);
  auto finish = [&](auto N, const u32x4* p, unsigned so) {
    sfor<CV(N)>([&](auto J) { o0 = mfma(elo, as_bf(p[J]), o0); o1 = mfma(ehi, as_bf(p[J]), o1); });
    *(LDS u32x2*)tw = u32x2{cvtpk(o0[0], o0[1]), cvtpk(o0[2], o0[3])}; *(LDS u32x2*)(tw + 768) = u32x2{cvtpk(o1[0], o1[1]), cvtpk(o1[2], o1[3])};
    asm volatile("" ::: "memory");
    store16_sc1<0>(*(LDS u32x4*)trd, sO, tst, so);
  };
  if constexpr (QKV) pin(pw[7]);
  finish(ic<7>{}, pv, oso + 32 * b2);
  if (QKV && !guest) {
    const bf16x8 x8 = as_bf(u32x4{xres[2][0], xres[2][1], xres[3][0], xres[3][1]});
    o0 = mfma(elo, x8, f32x4{}); o1 = mfma(ehi, x8, f32x4{});
    finish(ic<8>{}, pw, oso + 256 + 32 * xt);
  }
  if constexpr (QKV) asm volatile(".Lpsk_end_a:"); else asm volatile(".Lpsk_end_b:");
}

__global__ __launch_bounds__(512, 2) void fused(Args A) {
  __shared__ __attribute__((aligned(256))) char smem_[LDS_B];
  const unsigned smem = (unsigned)(size_t)(LDS char*)smem_;
  const int wave = rfl(threadIdx.x >> 6), lane = threadIdx.x & 63, bid = blockIdx.x, xcd = bid & 7, r = bid >> 3;
  char* ws = A.ws;
  u32x4 v; unsigned v9, ep, tc;
  ln_load(v, v9, A.x, bid * 8 + wave, lane);
  load4_sc1(ep, FL_EPOCH, mk_rsrc(ws + WS_FL, FL_B));
  asm volatile("s_barrier" ::: "memory");
  const bool qkv = r >= 24;
  const int rt = qkv ? r - 24 : r / 3, col0 = qkv ? 72 * xcd : (xcd * 3 + r % 3) * 192;
  {
    u64 pc; unsigned na, nb;
    asm volatile("s_getpc_b64 %0\n.Lpsk_begin:\n\ts_mov_b32 %1, .Lpsk_end_a-.Lpsk_begin\n\ts_mov_b32 %2, .Lpsk_end_b-.Lpsk_begin" : "=s"(pc), "=s"(na), "=s"(nb));
    const int w3 = wave & 3;
    const unsigned id = (w3 - 1) * 64 + lane;
    unsigned vo;
    if (qkv) { const unsigned k = (wave - 1) * 64 + lane; vo = pick<unsigned>(k < 432, (rt * 216 + (k >> 1)) * 1152 + (k & 1) * 128, 0x80000000u); }
    else vo = pick<unsigned>(id < 72, (rt * 72 + id) * (F * 2), pick<unsigned>(id < 108, (rt * 36 + id - 72) * (F * 2) + 128, 0x80000000u));
    vo += col0 * 2;
    const bool cw = wave == 4 && r < 16;
    if (cw) vo = (r * 64 + lane) * 128;
    const unsigned nby = cw ? (na > nb ? na : nb) : qkv ? (wave ? D * 3 * D * 2 : 0) : w3 ? D * F * 2 : 0;
    load4(tc, vo, mk_rsrc(cw ? (const char*)pc : qkv ? A.wqkv : wave < 4 ? A.wg : A.wu, nby));
  }
  wait_vm<1>();
  pin(v); pin(v9); pin(ep);
  const unsigned tag = rfl(ep) * 1664525u + 1013904223u;
  ln_row(v, v9, smem, wave, lane);
  lds_barrier();
  if (wave == 0) { ln_publish(ws, smem, tag, bid, lane); }
  asm volatile("s_barrier" ::: "memory");
  if (!qkv) body<false>(A, smem, tag, rt, col0, wave, lane, tc);
  else body<true>(A, smem, tag, rt, col0, wave, lane, tc);
}

}
extern "C" void kernel_launch(void* const* d_in, const int* in_sizes, int n_in, void* d_out, int out_size, void* d_ws, size_t ws_size,
                              hipStream_t stream) {
  psk::Args A;
  A.x = (const char*)d_in[0]; A.wqkv = (const char*)d_in[1]; A.wo = (const char*)d_in[2]; A.wg = (const char*)d_in[4];
  A.wu = (const char*)d_in[5]; A.wd = (const char*)d_in[6]; A.ws = (char*)d_ws; A.out = (char*)d_out;
  psk::fused<<<256, 512, 0, stream>>>(A);
}
```
